```python
import torch

def _softmax(x, axis=-1):
    return torch.softmax(x, dim=axis)
def _gelu(x):
    return torch.nn.functional.gelu(x, approximate="tanh")
def _silu(x):
    return torch.nn.functional.silu(x)
def _sigmoid(x):
    return torch.sigmoid(x)
def _layernorm(x, axis=-1, eps=1e-5):
    m = x.mean(dim=axis, keepdim=True)
    v = x.var(dim=axis, keepdim=True, unbiased=False)
    return (x - m) / torch.sqrt(v + eps)
def _scatter_add(tgt, idx, src, axis=0):
    return tgt.clone().index_add_(axis, idx, src)
def _segment_sum(src, seg, n):
    out = torch.zeros((n,) + src.shape[1:], dtype=src.dtype)
    return out.index_add_(0, seg, src)
def _pad(x, pw):
    flat = tuple(v for pair in reversed(pw) for v in pair)
    return torch.nn.functional.pad(x, flat)

kernel_name = 'recurrent_scan__ssm_conv_s4d__dense_activation_gelu__prompt'

def setup_inputs():

    _w = (lambda a: (lambda t: t.to(torch.bfloat16) if t.is_floating_point() else t)(torch.as_tensor(a)))
    def _A_setup():
        Bsz, L, D, N = 1, 2048, 512, 128
        return {
            "u": _w((lambda *s: torch.randn(*s, dtype=torch.bfloat16) * 0.05)(Bsz, L, D)),
            "log_A_real": _w((lambda *s: torch.randn(*s, dtype=torch.bfloat16) * 6.4)(D, N) * 0.5),
            "A_imag": _w((lambda *s: torch.randn(*s, dtype=torch.bfloat16) * 0.2)(D, N) * 3.14),
            "C_real": _w((lambda *s: torch.randn(*s, dtype=torch.bfloat16) * 0.4)(D, N) / N ** 0.5),
            "C_imag": _w((lambda *s: torch.randn(*s, dtype=torch.bfloat16) * 0.4)(D, N) / N ** 0.5),
            "log_dt": _w((lambda *s: torch.randn(*s, dtype=torch.bfloat16) * 0.2)(D) * 0.5 - 4.0),
            "D_skip": _w((lambda *s: torch.randn(*s, dtype=torch.bfloat16) * 0.2)(D)),
        }
    _A_in = _A_setup()

    def _B_setup():
        B, S, D, F = 1, 2048, 512, 1792
        return {
            "x": _w((lambda *s: torch.randn(*s, dtype=torch.bfloat16) * 0.05)(B, S, D)),
            "w1": _w((lambda *s: torch.randn(*s, dtype=torch.bfloat16) * 0.8)(D, F) * (D ** -0.5)),
            "b1": _w((lambda *s: torch.randn(*s, dtype=torch.bfloat16) * 0.1)(F) * 0.01),
            "w2": _w((lambda *s: torch.randn(*s, dtype=torch.bfloat16) * 0.1)(F, D) * (F ** -0.5)),
            "b2": _w((lambda *s: torch.randn(*s, dtype=torch.bfloat16) * 0.1)(D) * 0.01),
        }
    _B_in = _B_setup()

    return {
        "A_u": _A_in["u"],
        "A_log_A_real": _A_in["log_A_real"],
        "A_A_imag": _A_in["A_imag"],
        "A_C_real": _A_in["C_real"],
        "A_C_imag": _A_in["C_imag"],
        "A_log_dt": _A_in["log_dt"],
        "A_D_skip": _A_in["D_skip"],
        "B_x": _B_in["x"],
        "B_w1": _B_in["w1"],
        "B_b1": _B_in["b1"],
        "B_w2": _B_in["w2"],
        "B_b2": _B_in["b2"],
    }

def reference(A_u, A_log_A_real, A_A_imag, A_C_real, A_C_imag, A_log_dt, A_D_skip, B_x, B_w1, B_b1, B_w2, B_b2):
    def _A_ref(u, log_A_real, A_imag, C_real, C_imag, log_dt, D_skip):
        L = u.shape[1]
        dt = torch.exp(log_dt)
        A = -torch.exp(log_A_real) + 1j * A_imag
        C = (C_real + 1j * C_imag) * dt[:, None]
        pos = (lambda x, dt: x.to(dt))(torch.arange(L), log_dt.dtype)
        P = torch.exp((A * dt[:, None])[:, :, None] * pos[None, None, :])
        K = 2.0 * torch.einsum("dn,dnl->dl", C, P).real
        L2 = 2 * L
        u_t = (lambda x, *d: x.permute(*d))(u, 0, 2, 1)
        Kf = (lambda x, *a, **k: torch.fft.rfft(x.float(), *a, **k))(K, L2)
        Uf = (lambda x, *a, **k: torch.fft.rfft(x.float(), *a, **k))(u_t, L2)
        y = (lambda x, *a, **k: torch.fft.irfft(x, *a, **k).to(torch.bfloat16))(Uf * Kf[None, :, :], L2)[:, :, :L]
        y = (lambda x, *d: x.permute(*d))(y, 0, 2, 1)
        return y + u * D_skip[None, None, :]

    def _B_ref(x, w1, b1, w2, b2):
        h = torch.einsum("bsd,df->bsf", x, w1) + b1
        h = _gelu(h)
        return torch.einsum("bsf,fd->bsd", h, w2) + b2

    x = A_u
    xn = _layernorm(x, axis=-1)
    s = _A_ref(xn, A_log_A_real, A_A_imag, A_C_real, A_C_imag, A_log_dt, A_D_skip)
    g = _silu(_B_ref(xn, B_w1, B_b1, B_w2, B_b2))
    return x + s * g
```

```cpp
#include <hip/hip_runtime.h>
#include <hip/hip_bf16.h>
#include <stdint.h>

#define NOPOSROUND 1
#define L_ 2048
#define D_ 512
#define N_ 128
#define F_ 1792

typedef unsigned short u16;
__device__ __forceinline__ float bf2f(u16 v) { return __uint_as_float(((unsigned)v) << 16); }
__device__ __forceinline__ u16 f2bf(float f) { __hip_bfloat16 b = __float2bfloat16(f); return *reinterpret_cast<u16*>(&b); }
__device__ __forceinline__ float rbf(float f) { return bf2f(f2bf(f)); }

__global__ void ln_kernel(const u16* x, u16* xn) {
  int row = blockIdx.x; int lane = threadIdx.x;
  float v[8]; float s = 0.f;
  for (int j = 0; j < 8; ++j) { v[j] = bf2f(x[row * D_ + lane * 8 + j]); s += v[j]; }
  for (int o = 32; o; o >>= 1) s += __shfl_xor(s, o);
  float mean = s / D_;
  float q = 0.f;
  for (int j = 0; j < 8; ++j) { float d = v[j] - mean; q += d * d; }
  for (int o = 32; o; o >>= 1) q += __shfl_xor(q, o);
  float var = rbf(q / D_);
  float m = rbf(mean);
  float sd = rbf(sqrtf(rbf(var + 1e-5f)));
  for (int j = 0; j < 8; ++j) xn[row * D_ + lane * 8 + j] = f2bf(rbf(v[j] - m) / sd);
}

__global__ void kgen_kernel(const u16* lar, const u16* aim, const u16* cre, const u16* cim, const u16* ldt, float* K) {
  int d = blockIdx.x;
  float dt = rbf(expf(bf2f(ldt[d])));
  for (int l = threadIdx.x; l < L_; l += blockDim.x) {
    float pos = (float)l;
    float acc = 0.f;
    for (int n = 0; n < N_; ++n) {
      float ar = -rbf(expf(bf2f(lar[d * N_ + n]))) * dt;
      float ai = bf2f(aim[d * N_ + n]) * dt;
      float cr = bf2f(cre[d * N_ + n]) * dt, ci = bf2f(cim[d * N_ + n]) * dt;
      float e = expf(ar * pos); float sn, cs; sincosf(ai * pos, &sn, &cs);
      acc += cr * (e * cs) - ci * (e * sn);
    }
    K[d * L_ + l] = 2.f * acc;
  }
}

__global__ void conv_kernel(const float* K, const u16* xn, const u16* dskip, u16* s) {
  int d = blockIdx.y * 64 + threadIdx.x; int t = blockIdx.x * 4 + threadIdx.y;
  float acc = 0.f;
  for (int l = 0; l <= t; ++l) acc += K[d * L_ + l] * bf2f(xn[(t - l) * D_ + d]);
  float y = rbf(acc);
  float u = bf2f(xn[t * D_ + d]);
  s[t * D_ + d] = f2bf(y + rbf(u * bf2f(dskip[d])));
}

__global__ void gemm1_kernel(const u16* xn, const u16* w1, const u16* b1, u16* h) {
  int f = blockIdx.x * 64 + threadIdx.x; int t = blockIdx.y * 4 + threadIdx.y;
  float acc = 0.f;
  for (int k = 0; k < D_; ++k) acc += bf2f(xn[t * D_ + k]) * bf2f(w1[k * F_ + f]);
  float v = rbf(rbf(acc) + bf2f(b1[f]));
  float z = 0.7978845608028654f * (v + 0.044715f * v * v * v);
  float g = 0.5f * v * (1.f + tanhf(z));
  h[t * F_ + f] = f2bf(g);
}

__global__ void gemm2_kernel(const u16* h, const u16* w2, const u16* b2, const u16* x, const u16* s, u16* out) {
  int d = blockIdx.x * 64 + threadIdx.x; int t = blockIdx.y * 4 + threadIdx.y;
  float acc = 0.f;
  for (int k = 0; k < F_; ++k) acc += bf2f(h[t * F_ + k]) * bf2f(w2[k * D_ + d]);
  float v = rbf(rbf(acc) + bf2f(b2[d]));
  float g = rbf(v / (1.f + expf(-v)));
  float p = rbf(bf2f(s[t * D_ + d]) * g);
  out[t * D_ + d] = f2bf(bf2f(x[t * D_ + d]) + p);
}

extern "C" void kernel_launch(void* const* d_in, const int* in_sizes, int n_in,
                              void* d_out, int out_size, void* d_ws, size_t ws_size,
                              hipStream_t stream) {
  const u16* x = (const u16*)d_in[0];
  const u16* lar = (const u16*)d_in[1];
  const u16* aim = (const u16*)d_in[2];
  const u16* cre = (const u16*)d_in[3];
  const u16* cim = (const u16*)d_in[4];
  const u16* ldt = (const u16*)d_in[5];
  const u16* dsk = (const u16*)d_in[6];
  const u16* w1 = (const u16*)d_in[8];
  const u16* b1 = (const u16*)d_in[9];
  const u16* w2 = (const u16*)d_in[10];
  const u16* b2 = (const u16*)d_in[11];
  char* ws = (char*)d_ws;
  u16* xn = (u16*)ws;
  float* K = (float*)(ws + (2 << 20));
  u16* s = (u16*)(ws + (6 << 20));
  u16* h = (u16*)(ws + (8 << 20));
  ln_kernel<<<L_, 64, 0, stream>>>(x, xn);
  kgen_kernel<<<D_, 256, 0, stream>>>(lar, aim, cre, cim, ldt, K);
  conv_kernel<<<dim3(L_ / 4, D_ / 64), dim3(64, 4), 0, stream>>>(K, xn, dsk, s);
  gemm1_kernel<<<dim3(F_ / 64, L_ / 4), dim3(64, 4), 0, stream>>>(xn, w1, b1, h);
  gemm2_kernel<<<dim3(D_ / 64, L_ / 4), dim3(64, 4), 0, stream>>>(h, w2, b2, x, s, (u16*)d_out);
}
```

```cpp
#include <hip/hip_runtime.h>
#include <stdint.h>

typedef unsigned short u16;
typedef __attribute__((ext_vector_type(4))) short s16x4;
typedef __attribute__((ext_vector_type(8))) short s16x8;
typedef __attribute__((ext_vector_type(8))) _Float16 f16x8;
typedef __attribute__((ext_vector_type(2))) _Float16 f16x2;
typedef __attribute__((ext_vector_type(2))) __bf16 bf16x2;
typedef __attribute__((ext_vector_type(2))) float f32x2;
typedef __attribute__((ext_vector_type(4))) float f32x4;
typedef __attribute__((ext_vector_type(4))) unsigned u32x4;
typedef __attribute__((ext_vector_type(2))) unsigned u32x2;
#define LDSP __attribute__((address_space(3)))
#define GLBP __attribute__((address_space(1)))
#define DI __device__ __forceinline__

#define L_ 2048
#define D_ 512
#define N_ 128
#define F_ 1792

#define WS_H 0
#define WS_UR (8u << 20)
#define WS_S (10u << 20)
#define WS_FLAG (12u << 20)

DI float bf2f(unsigned v) { return __uint_as_float(v << 16); }
DI float bflo(unsigned v) { return __uint_as_float(v << 16); }
DI float bfhi(unsigned v) { return __uint_as_float(v & 0xffff0000u); }
DI unsigned pkbf(float a, float b) { bf16x2 q = {(__bf16)a, (__bf16)b}; return __builtin_bit_cast(unsigned, q); }
DI float rbf(float a) { __bf16 q = (__bf16)a; return (float)q; }
DI unsigned pkh(float a, float b) { f32x2 v = {a, b}; f16x2 p = __builtin_convertvector(v, f16x2); return __builtin_bit_cast(unsigned, p); }
DI float dot2(unsigned a, unsigned b, float c) { return __builtin_amdgcn_fdot2_f32_bf16(__builtin_bit_cast(bf16x2, a), __builtin_bit_cast(bf16x2, b), c, false); }

template <class T> DI T sel(int c, T a, T b) { return c ? a : b; }
DI void dma16(const void* g, char* l) { __builtin_amdgcn_global_load_lds((const GLBP unsigned*)g, (LDSP unsigned*)l, 16, 0, 0); }
DI s16x8 tr_frag(const char* p0, const char* p1) {
  s16x4 a = __builtin_amdgcn_ds_read_tr16_b64_v4i16((LDSP s16x4*)p0);
  s16x4 b = __builtin_amdgcn_ds_read_tr16_b64_v4i16((LDSP s16x4*)p1);
  return __builtin_shufflevector(a, b, 0, 1, 2, 3, 4, 5, 6, 7);
}
#define WAITV_BAR(n) asm volatile("s_waitcnt vmcnt(" #n ") lgkmcnt(0)\n\ts_barrier" ::: "memory")
#define WAITV(n) asm volatile("s_waitcnt vmcnt(" #n ")" ::: "memory")

DI void group_barrier(unsigned* cnt, unsigned target, int lane) {
  asm volatile("s_waitcnt lgkmcnt(0)" ::: "memory");
  if (lane == 0) __hip_atomic_fetch_add(cnt, 1u, __ATOMIC_RELAXED, __HIP_MEMORY_SCOPE_WORKGROUP);
  while (__hip_atomic_load(cnt, __ATOMIC_RELAXED, __HIP_MEMORY_SCOPE_WORKGROUP) < target) __builtin_amdgcn_s_sleep(1);
  asm volatile("" ::: "memory");
}

#define K1_SLOT 32768
template <int T>
DI void k1_issue(const u16* xs0, const u16* xs1, const u16* ws0, const u16* ws1, char* lds, int wid) {
  char* slot = lds + (T % 5) * K1_SLOT;
  dma16(xs0 + T * 64, slot + wid * 2048);
  dma16(xs1 + T * 64, slot + wid * 2048 + 1024);
  dma16(ws0 + (size_t)T * 64 * F_, slot + 16384 + wid * 2048);
  dma16(ws1 + (size_t)T * 64 * F_, slot + 16384 + wid * 2048 + 1024);
}

DI float gelu_f(float v) {
  const float c0 = -2.302208198f;
  const float c1 = -0.1029432397f;
  float z = v * fmaf(v * v, c1, c0);
  float e = __builtin_amdgcn_exp2f(z);
  return v * __builtin_amdgcn_rcpf(1.f + e);
}

DI void k1_gemm(const u16* __restrict__ x, const u16* __restrict__ w1, const u16* __restrict__ b1, char* __restrict__ ws, char* lds,
                int panel, int fs, int wid, int lane) {
  const int wm = wid >> 2, wf = (wid >> 1) & 1, kh = wid & 1;
  const int n = lane & 15, g = lane >> 4;
  const int row0 = panel * 128, f0 = fs * 128;
  const int fb = f0 + 64 * wf + 32 * kh + 4 * g;
  const u32x2 bia0 = *(const u32x2*)(b1 + fb), bia1 = *(const u32x2*)(b1 + fb + 16);
  const int xr = 16 * wid + (lane >> 3);
  const u16* xs0 = x + (size_t)(row0 + xr) * D_ + (((lane & 7) ^ ((xr >> 1) & 7)) << 3);
  const u16* xs1 = x + (size_t)(row0 + xr + 8) * D_ + (((lane & 7) ^ (((xr + 8) >> 1) & 7)) << 3);
  const int kr = 8 * wid + (lane >> 4);
  const int sw0 = ((kr & 3) << 2) | ((kr >> 2) & 3), sw1 = (((kr + 4) & 3) << 2) | (((kr + 4) >> 2) & 3);
  const u16* ws0 = w1 + (size_t)kr * F_ + f0 + (((lane & 15) ^ sw0) << 3);
  const u16* ws1 = w1 + (size_t)(kr + 4) * F_ + f0 + (((lane & 15) ^ sw1) << 3);
  k1_issue<0>(xs0, xs1, ws0, ws1, lds, wid);
  k1_issue<1>(xs0, xs1, ws0, ws1, lds, wid);
  k1_issue<2>(xs0, xs1, ws0, ws1, lds, wid);
  k1_issue<3>(xs0, xs1, ws0, ws1, lds, wid);
  k1_issue<4>(xs0, xs1, ws0, ws1, lds, wid);
  const int xoff = (64 * wm + n) * 128 + (((4 * kh + g) ^ ((n >> 1) & 7)) << 4);
  const int q = (lane & 15) >> 2, p = lane & 3;
  int woff[4];
  {
    const int r0 = 32 * kh + 8 * g + q;
    const int s0 = (q << 2) | ((2 * g) & 3);
#pragma unroll
    for (int fi = 0; fi < 4; ++fi) {
      const int ch = 2 * (4 * wf + fi) + (p >> 1);
      woff[fi] = 16384 + r0 * 256 + ((ch ^ s0) << 4) + 8 * (p & 1);
    }
  }
  f32x4 acc[4][4];
#pragma unroll
  for (int mi = 0; mi < 4; ++mi)
#pragma unroll
    for (int fi = 0; fi < 4; ++fi) acc[mi][fi] = (f32x4){0.f, 0.f, 0.f, 0.f};
  float s1[2] = {0.f, 0.f}, s2[2] = {0.f, 0.f}, cs[2] = {0.f, 0.f};

#define K1_STAGE(T) { \
    const char* slot = lds + ((T) % 5) * K1_SLOT; \
    s16x8 xf[4], wfr[4]; \
    _Pragma("unroll") for (int mi = 0; mi < 4; ++mi) xf[mi] = *(const s16x8*)(slot + xoff + mi * 2048); \
    _Pragma("unroll") for (int fi = 0; fi < 4; ++fi) wfr[fi] = tr_frag(slot + woff[fi], slot + (woff[fi] ^ 16) + 1024); \
    _Pragma("unroll") for (int mi = 0; mi < 4; ++mi) \
    _Pragma("unroll") for (int fi = 0; fi < 4; ++fi) acc[mi][fi] = __builtin_amdgcn_mfma_f32_16x16x32_bf16(wfr[fi], xf[mi], acc[mi][fi], 0, 0, 0); \
    const unsigned ones = 0x3F803F80u; \
    u32x4 xa = __builtin_bit_cast(u32x4, sel(wf, xf[2], xf[0])), xb = __builtin_bit_cast(u32x4, sel(wf, xf[3], xf[1])); \
    u32x4 wa = __builtin_bit_cast(u32x4, sel(wm, wfr[2], wfr[0])), wb = __builtin_bit_cast(u32x4, sel(wm, wfr[3], wfr[1])); \
    _Pragma("unroll") for (int i = 0; i < 4; ++i) { \
      s1[0] = dot2(xa[i], ones, s1[0]); s2[0] = dot2(xa[i], xa[i], s2[0]); \
      s1[1] = dot2(xb[i], ones, s1[1]); s2[1] = dot2(xb[i], xb[i], s2[1]); \
      cs[0] = dot2(wa[i], ones, cs[0]); cs[1] = dot2(wb[i], ones, cs[1]); \
    } }

  WAITV_BAR(16); K1_STAGE(0);
  WAITV_BAR(12); k1_issue<5>(xs0, xs1, ws0, ws1, lds, wid); K1_STAGE(1);
  WAITV_BAR(12); k1_issue<6>(xs0, xs1, ws0, ws1, lds, wid); K1_STAGE(2);
  WAITV_BAR(12); k1_issue<7>(xs0, xs1, ws0, ws1, lds, wid); K1_STAGE(3);
  WAITV_BAR(12); K1_STAGE(4);
  WAITV_BAR(8); K1_STAGE(5);
  WAITV_BAR(4); K1_STAGE(6);
  WAITV_BAR(0); K1_STAGE(7);

  char* ex = lds + 3 * K1_SLOT;
  float* rowS = (float*)lds;
  float* colS = (float*)(lds + 2048);
  {
    char* dst = ex + wid * 8192 + lane * 16;
#pragma unroll
    for (int mi = 0; mi < 4; ++mi) {
      *(f32x4*)(dst + (2 * mi) * 1024) = sel(kh, acc[mi][0], acc[mi][2]);
      *(f32x4*)(dst + (2 * mi + 1) * 1024) = sel(kh, acc[mi][1], acc[mi][3]);
    }
#pragma unroll
    for (int i = 0; i < 2; ++i) {
      s1[i] += __shfl_xor(s1[i], 16); s1[i] += __shfl_xor(s1[i], 32);
      s2[i] += __shfl_xor(s2[i], 16); s2[i] += __shfl_xor(s2[i], 32);
      cs[i] += __shfl_xor(cs[i], 16); cs[i] += __shfl_xor(cs[i], 32);
    }
    if (g == 0) {
#pragma unroll
      for (int i = 0; i < 2; ++i) {
        const int r = 64 * wm + 16 * (2 * wf + i) + n;
        *(f32x2*)(rowS + (kh * 128 + r) * 2) = (f32x2){s1[i], s2[i]};
        colS[kh * 128 + 64 * wf + 16 * (2 * wm + i) + n] = cs[i];
      }
    }
  }
  asm volatile("s_waitcnt lgkmcnt(0)\n\ts_barrier" ::: "memory");
  {
    const char* src = ex + (wid ^ 1) * 8192 + lane * 16;
    const int fl = 64 * wf + 32 * kh + 4 * g;
    f32x4 c0 = *(const f32x4*)(colS + fl) + *(const f32x4*)(colS + 128 + fl);
    f32x4 c1 = *(const f32x4*)(colS + fl + 16) + *(const f32x4*)(colS + 128 + fl + 16);
    float bb[8] = {bflo(bia0[0]), bfhi(bia0[0]), bflo(bia0[1]), bfhi(bia0[1]), bflo(bia1[0]), bfhi(bia1[0]), bflo(bia1[1]), bfhi(bia1[1])};
    char* hdst = ws + WS_H + ((size_t)(panel * 8 + 4 * wm) * 56 + (4 * fs + 2 * wf + kh)) * 1024 + lane * 16;
#pragma unroll
    for (int mi = 0; mi < 4; ++mi) {
      const int r = 64 * wm + 16 * mi + n;
      f32x2 sa = *(const f32x2*)(rowS + r * 2), sb = *(const f32x2*)(rowS + (128 + r) * 2);
      const float mean = (sa[0] + sb[0]) * (1.f / 512.f);
      float var = fmaf(-mean, mean, (sa[1] + sb[1]) * (1.f / 512.f));
      var = fmaxf(var, 0.f);
      const float mb = rbf(mean);
      const float sd = rbf(sqrtf(rbf(rbf(var) + 1e-5f)));
      const float rinv = 1.f / sd;
      f32x4 a0 = sel(kh, acc[mi][2], acc[mi][0]) + *(const f32x4*)(src + (2 * mi) * 1024);
      f32x4 a1 = sel(kh, acc[mi][3], acc[mi][1]) + *(const f32x4*)(src + (2 * mi + 1) * 1024);
      float o[8];
#pragma unroll
      for (int i = 0; i < 4; ++i) {
        o[i] = gelu_f(rbf(rbf(fmaf(-mb, c0[i], a0[i]) * rinv) + bb[i]));
        o[4 + i] = gelu_f(rbf(rbf(fmaf(-mb, c1[i], a1[i]) * rinv) + bb[4 + i]));
      }
      u32x4 pk = {pkbf(o[0], o[1]), pkbf(o[2], o[3]), pkbf(o[4], o[5]), pkbf(o[6], o[7])};
      *(u32x4*)(hdst + (size_t)mi * 56 * 1024) = pk;
    }
  }
}

#define LN_RS 1040
DI void k1_ln(const u16* __restrict__ x, char* __restrict__ ws, char* lds, int idx, int wid, int lane) {
  const int t0 = idx * 64;
  u32x4 v[8];
#pragma unroll
  for (int i = 0; i < 8; ++i) v[i] = *(const u32x4*)(x + (size_t)(t0 + 8 * wid + i) * D_ + lane * 8);
#pragma unroll
  for (int i = 0; i < 8; ++i) {
    float e[8];
#pragma unroll
    for (int c = 0; c < 4; ++c) { e[2 * c] = bflo(v[i][c]); e[2 * c + 1] = bfhi(v[i][c]); }
    float s = ((e[0] + e[1]) + (e[2] + e[3])) + ((e[4] + e[5]) + (e[6] + e[7]));
#pragma unroll
    for (int o = 1; o < 64; o <<= 1) s += __shfl_xor(s, o);
    const float mean = s * (1.f / 512.f);
    float qq = 0.f;
#pragma unroll
    for (int c = 0; c < 8; ++c) { float d = e[c] - mean; qq = fmaf(d, d, qq); }
#pragma unroll
    for (int o = 1; o < 64; o <<= 1) qq += __shfl_xor(qq, o);
    const float mb = rbf(mean);
    const float sd = rbf(sqrtf(rbf(rbf(qq * (1.f / 512.f)) + 1e-5f)));
    const float rinv = 1.f / sd;
    float y[8];
#pragma unroll
    for (int c = 0; c < 8; ++c) y[c] = rbf(rbf(e[c] - mb) * rinv);
    u32x4 pk = {pkh(y[0], y[1]), pkh(y[2], y[3]), pkh(y[4], y[5]), pkh(y[6], y[7])};
    const int rr = 63 - (8 * wid + i);
    *(u32x4*)(lds + rr * LN_RS + lane * 16) = pk;
  }
  __syncthreads();
  char* t2 = lds + 64 * LN_RS + wid * 8192;
  const int g = lane >> 4, q = (lane & 15) >> 2, p = lane & 3, dl = lane & 15;
#pragma unroll
  for (int dt = 0; dt < 4; ++dt) {
#pragma unroll
    for (int hh = 0; hh < 2; ++hh) {
      const int rc = 4 * hh + g;
      const char* a = lds + (8 * rc + q) * LN_RS + (64 * wid + 16 * dt + 4 * p) * 2;
      s16x8 f = tr_frag(a, a + 4 * LN_RS);
      const int d = 16 * dt + dl;
      *(s16x8*)(t2 + d * 128 + ((rc ^ (d & 7)) << 4)) = f;
    }
  }
  char* ur = ws + WS_UR;
  const int x0 = L_ - 64 - t0;
#pragma unroll
  for (int i = 0; i < 8; ++i) {
    const int d = 8 * i + (lane >> 3), c = lane & 7;
    u32x4 f = *(const u32x4*)(t2 + d * 128 + ((c ^ (d & 7)) << 4));
    *(u32x4*)(ur + (size_t)(64 * wid + d) * 4096 + x0 * 2 + c * 16) = f;
  }
}

__global__ __launch_bounds__(512, 2) void k1_kernel(const u16* __restrict__ x, const u16* __restrict__ w1, const u16* __restrict__ b1,
                                                    char* __restrict__ ws) {
  extern __shared__ __attribute__((aligned(16))) char lds[];
  const int lane = threadIdx.x & 63;
  const int wid = __builtin_amdgcn_readfirstlane(threadIdx.x >> 6);
  const int b = blockIdx.x, xcd = b & 7, j = b >> 3;
  if (j < 28) {
    k1_gemm(x, w1, b1, ws, lds, 2 * xcd + (j >= 14 ? 1 : 0), j >= 14 ? j - 14 : j, wid, lane);
  } else {
    if (b == 255) ((unsigned*)(ws + WS_FLAG))[threadIdx.x] = 0u;
    k1_ln(x, ws, lds, xcd * 4 + (j - 28), wid, lane);
  }
}

#define K2_RING 0
#define K2_W 65536
#define K2_WSZ 33024
#define K2_U (K2_W + 2 * K2_WSZ)
#define K2_USZ 4608
#define K2_KAP (K2_U + 2 * K2_USZ)
#define K2_KAPSZ 2624
#define K2_KP (K2_KAP + 4 * K2_KAPSZ)
#define K2_KPSZ 4160
#define K2_CNT (K2_KP + 2 * K2_KPSZ)
#define K2_LDS (K2_CNT + 64)
#define K2_R 4

DI void gload16(u32x4& v, unsigned voff, const char* sbase) {
  asm volatile("global_load_dwordx4 %0, %1, %2" : "=v"(v) : "v"(voff), "s"(sbase) : "memory");
}

DI int kap_idx(int p) { return p < 256 ? p : (p < 512 ? 256 + ((p - 256) >> 1) : (p < 1024 ? 384 + ((p - 512) >> 2) : 512 + ((p - 1024) >> 3))); }

DI void k2_conv(const u16* __restrict__ lar, const u16* __restrict__ aim, const u16* __restrict__ cre, const u16* __restrict__ cim,
                const u16* __restrict__ ldt, const u16* __restrict__ dsk, char* __restrict__ ws, char* lds, int mb, int cb, int cw, int lane) {
  const int ch = cw >> 1, hf = cw & 1;
  const int d = 64 * cb + 2 * mb + ch;
  const int i16 = lane & 15, g = lane >> 4;
  unsigned* cnt = (unsigned*)(lds + K2_CNT);
  u32x2 pl[4], pa[4], pr[4], pi[4];
#pragma unroll
  for (int s = 0; s < 4; ++s) {
    const int n0 = d * N_ + 64 * hf + 16 * s + 4 * g;
    pl[s] = *(const u32x2*)(lar + n0); pa[s] = *(const u32x2*)(aim + n0);
    pr[s] = *(const u32x2*)(cre + n0); pi[s] = *(const u32x2*)(cim + n0);
  }
  const unsigned call = *(const unsigned*)(cre + d * N_ + 2 * lane), ciall = *(const unsigned*)(cim + d * N_ + 2 * lane);
  const float dt = rbf(expf(bf2f(ldt[d])));
  const float dskip = bf2f(dsk[d]);
  char* ul = lds + K2_U + ch * K2_USZ;
  {
    const char* us = ws + WS_UR + (size_t)d * 4096 + hf * 2048 + lane * 16;
    u32x4 a = *(const u32x4*)us, b = *(const u32x4*)(us + 1024);
    *(u32x4*)(ul + hf * 2048 + lane * 16) = a;
    *(u32x4*)(ul + hf * 2048 + 1024 + lane * 16) = b;
    if (hf == 0 && lane < 32) *(u32x4*)(ul + 4096 + lane * 16) = (u32x4){0u, 0u, 0u, 0u};
  }
  float sabs = fabsf(bflo(call)) + fabsf(bfhi(call)) + fabsf(bflo(ciall)) + fabsf(bfhi(ciall));
#pragma unroll
  for (int o = 1; o < 64; o <<= 1) sabs += __shfl_xor(sabs, o);
  sabs *= 2.f * dt;
  int ex = (int)((__float_as_uint(sabs) >> 23) & 255);
  ex = ex < 20 ? 20 : (ex > 230 ? 230 : ex);
  const float scale = __uint_as_float((unsigned)(127 + 14 + 126 - ex) << 23);
  const float iscale = __uint_as_float((unsigned)(127 - 14 - 126 + ex) << 23);
  const float cmul = 2.f * dt * scale;
  const float pX = (float)(128 * (i16 >> 3) + (i16 & 7));
  const float pY = (float)(i16 < 8 ? 256 + 128 * (i16 >> 2) + 2 * (i16 & 3) : 512 + 128 * ((i16 - 8) >> 1) + 4 * (i16 & 1));
  const float pZ = (float)(i16 < 8 ? 1024 + 128 * i16 : 2048);
  const float pB = (float)(8 * i16);
  f32x4 kX = {0.f, 0.f, 0.f, 0.f}, kY = kX, kZ = kX;
#pragma unroll
  for (int s = 0; s < 4; ++s) {
    u32x4 fX, fY, fZ, fB;
#pragma unroll
    for (int e = 0; e < 4; ++e) {
      const unsigned wl = pl[s][e >> 1], wa = pa[s][e >> 1], wr = pr[s][e >> 1], wi = pi[s][e >> 1];
      const float l = (e & 1) ? bfhi(wl) : bflo(wl), a = (e & 1) ? bfhi(wa) : bflo(wa);
      const float cr = ((e & 1) ? bfhi(wr) : bflo(wr)) * cmul, ci = ((e & 1) ? bfhi(wi) : bflo(wi)) * cmul;
      const float al = -(rbf(expf(l)) * dt) * 1.4426950408889634f;
      const float om = (a * dt) * 0.15915494309189535f;
      float m, ph, c, sn;
      m = __builtin_amdgcn_exp2f(al * pX); ph = __builtin_amdgcn_fractf(om * pX);
      fX[e] = pkh(m * __builtin_amdgcn_cosf(ph), m * __builtin_amdgcn_sinf(ph));
      m = __builtin_amdgcn_exp2f(al * pY); ph = __builtin_amdgcn_fractf(om * pY);
      fY[e] = pkh(m * __builtin_amdgcn_cosf(ph), m * __builtin_amdgcn_sinf(ph));
      m = __builtin_amdgcn_exp2f(al * pZ); ph = __builtin_amdgcn_fractf(om * pZ);
      fZ[e] = pkh(m * __builtin_amdgcn_cosf(ph), m * __builtin_amdgcn_sinf(ph));
      m = __builtin_amdgcn_exp2f(al * pB); ph = __builtin_amdgcn_fractf(om * pB);
      c = m * __builtin_amdgcn_cosf(ph); sn = m * __builtin_amdgcn_sinf(ph);
      fB[e] = pkh(cr * c - ci * sn, -(cr * sn + ci * c));
    }
    const f16x8 hb = __builtin_bit_cast(f16x8, fB);
    kX = __builtin_amdgcn_mfma_f32_16x16x32_f16(__builtin_bit_cast(f16x8, fX), hb, kX, 0, 0, 0);
    kY = __builtin_amdgcn_mfma_f32_16x16x32_f16(__builtin_bit_cast(f16x8, fY), hb, kY, 0, 0, 0);
    kZ = __builtin_amdgcn_mfma_f32_16x16x32_f16(__builtin_bit_cast(f16x8, fZ), hb, kZ, 0, 0, 0);
  }
  {
    float* kap = (float*)(lds + K2_KAP + cw * K2_KAPSZ);
#pragma unroll
    for (int r = 0; r < 4; ++r) {
      const int c = 4 * g + r;
      const int cX = 128 * (c >> 3) + (c & 7);
      const int cY = c < 8 ? 256 + 128 * (c >> 2) + 2 * (c & 3) : 512 + 128 * ((c - 8) >> 1) + 4 * (c & 1);
      kap[cX + 8 * i16] = kX[r];
      kap[kap_idx(cY + 8 * i16)] = kY[r];
      if (c < 8) kap[512 + 16 * c + i16] = kZ[r];
      if (c == 8 && i16 == 0) kap[640] = kZ[r];
    }
  }
  group_barrier(cnt + 4 * ch, 2, lane);
  char* kp = lds + K2_KP + ch * K2_KPSZ;
  {
    const float* k0 = (const float*)(lds + K2_KAP + (2 * ch) * K2_KAPSZ);
    const float* k1 = (const float*)(lds + K2_KAP + (2 * ch + 1) * K2_KAPSZ);
    {
      const int A = 64 * hf + lane;
      u32x4 o;
      if (hf == 0) {
        if (lane < 32) {
          f32x4 a = *(const f32x4*)(k0 + 8 * A) + *(const f32x4*)(k1 + 8 * A);
          f32x4 b = *(const f32x4*)(k0 + 8 * A + 4) + *(const f32x4*)(k1 + 8 * A + 4);
          o = (u32x4){pkh(a[0], a[1]), pkh(a[2], a[3]), pkh(b[0], b[1]), pkh(b[2], b[3])};
        } else {
          const int i0 = 256 + 4 * (A - 32);
          f32x4 a = *(const f32x4*)(k0 + i0) + *(const f32x4*)(k1 + i0);
          float e4 = k0[i0 + 4] + k1[i0 + 4];
          o = (u32x4){pkh(a[0], a[0]), pkh(a[1], a[2]), pkh(a[2], a[2]), pkh(a[3], e4)};
        }
      } else {
        const int i0 = 384 + 2 * (A - 64);
        f32x2 a = *(const f32x2*)(k0 + i0) + *(const f32x2*)(k1 + i0);
        float e2 = k0[i0 + 2] + k1[i0 + 2];
        o = (u32x4){pkh(a[0], a[0]), pkh(a[0], a[1]), pkh(a[1], a[1]), pkh(e2, e2)};
      }
      *(u32x4*)(kp + 32 + A * 16) = o;
    }
    {
      const int A = 128 + 64 * hf + lane;
      const int i0 = 512 + (A - 128);
      const float e0 = k0[i0] + k1[i0], e1 = k0[i0 + 1] + k1[i0 + 1];
      const unsigned p00 = pkh(e0, e0);
      u32x4 o = {p00, p00, pkh((A & 1) ? e1 : e0, e1), pkh(e1, e1)};
      *(u32x4*)(kp + 32 + A * 16) = o;
    }
    if (hf == 0 && lane < 2) *(u32x4*)(kp + lane * 16) = (u32x4){0u, 0u, 0u, 0u};
  }
  group_barrier(cnt + 4 * ch + 1, 2, lane);
  char* wl = lds + K2_W + ch * K2_WSZ;
  {
#pragma unroll
    for (int rnd = 0; rnd < 3; ++rnd) {
      const int a = rnd < 2 ? 128 * rnd + 64 * hf + lane : 256;
      if (rnd == 2 && !(hf == 0 && lane == 0)) break;
      u32x4 c0 = *(const u32x4*)(kp + a * 16), c1 = *(const u32x4*)(kp + a * 16 + 16);
      unsigned e[8] = {c0[0], c0[1], c0[2], c0[3], c1[0], c1[1], c1[2], c1[3]};
      unsigned o[7];
#pragma unroll
      for (int i = 0; i < 7; ++i) o[i] = __builtin_amdgcn_alignbit(e[i + 1], e[i], 16);
      const int m = (a & 1) | (((a >> 2) & 1) << 1) | (((a >> 1) & 1) << 2);
      char* base = wl + a * 128;
#pragma unroll
      for (int c = 0; c < 8; ++c) {
        u32x4 wv;
        if (c & 1) { const int s = (c + 1) >> 1; wv = (u32x4){e[s], e[s + 1], e[s + 2], e[s + 3]}; }
        else { const int s = c >> 1; wv = (u32x4){o[s], o[s + 1], o[s + 2], o[s + 3]}; }
        *(u32x4*)(base + ((c ^ m) << 4)) = wv;
      }
    }
  }
  group_barrier(cnt + 4 * ch + 2, 2, lane);
  f32x4 acc[8];
#pragma unroll
  for (int i = 0; i < 8; ++i) acc[i] = (f32x4){0.f, 0.f, 0.f, 0.f};
  {
    const int c = i16 + 8 * g;
    int fo[2];
#pragma unroll
    for (int par = 0; par < 2; ++par) {
      const int w = 32 * par + c;
      const int m = ((w >> 3) & 1) | (((w >> 5) & 1) << 1) | (((w >> 4) & 1) << 2);
      fo[par] = (w ^ m) << 4;
    }
    const char* fbase = wl + hf * (128 * 16);
    const char* gbase = ul + (2032 - 16 * i16 + 8 * g + 128 * hf) * 2;
#pragma unroll
    for (int s = 0; s < 4; ++s) {
      f16x8 F[8];
#pragma unroll
      for (int e = 0; e < 8; ++e) F[e] = *(const f16x8*)(fbase + fo[s & 1] + (s >> 1) * 1024 + e * 4096);
#pragma unroll
      for (int mu = 0; mu < 8; ++mu) {
        const f16x8 G = *(const f16x8*)(gbase + s * 64 - mu * 512);
#pragma unroll
        for (int e = 0; e + mu < 8; ++e) acc[e + mu] = __builtin_amdgcn_mfma_f32_16x16x32_f16(F[e], G, acc[e + mu], 0, 0, 0);
      }
    }
  }
  group_barrier(cnt + 4 * ch + 3, 2, lane);
  {
    char* dst = wl + hf * 4096 + lane * 16;
#pragma unroll
    for (int i = 0; i < 4; ++i) *(f32x4*)(dst + i * 1024) = sel(hf, acc[i], acc[4 + i]);
  }
  group_barrier(cnt + 8 + ch, 2, lane);
  {
    const char* src = wl + (hf ^ 1) * 4096 + lane * 16;
    char* sdst = ws + WS_S + (size_t)d * 4096;
#pragma unroll
    for (int i = 0; i < 4; ++i) {
      f32x4 yv = sel(hf, acc[4 + i], acc[i]) + *(const f32x4*)(src + i * 1024);
      const int t = 256 * (4 * hf + i) + 16 * i16 + 4 * g;
      const u32x2 uu = *(const u32x2*)(ul + (2044 - t) * 2);
      const unsigned uu0 = uu[0], uu1 = uu[1];
      const f16x2 ua = __builtin_bit_cast(f16x2, uu0), ub = __builtin_bit_cast(f16x2, uu1);
      const float u0 = (float)ub[1], u1 = (float)ub[0], u2 = (float)ua[1], u3 = (float)ua[0];
      const float o0 = rbf(yv[0] * iscale) + rbf(u0 * dskip), o1 = rbf(yv[1] * iscale) + rbf(u1 * dskip);
      const float o2 = rbf(yv[2] * iscale) + rbf(u2 * dskip), o3 = rbf(yv[3] * iscale) + rbf(u3 * dskip);
      const unsigned long long pk = (unsigned long long)pkbf(o0, o1) | ((unsigned long long)pkbf(o2, o3) << 32);
      __hip_atomic_store((unsigned long long*)(sdst + t * 2), pk, __ATOMIC_RELAXED, __HIP_MEMORY_SCOPE_AGENT);
    }
  }
  asm volatile("s_waitcnt vmcnt(0)" ::: "memory");
  unsigned old = 0;
  if (lane == 0) old = __hip_atomic_fetch_add(cnt + 10, 1u, __ATOMIC_RELAXED, __HIP_MEMORY_SCOPE_WORKGROUP);
  old = __builtin_amdgcn_readfirstlane(old);
  if (old == 3 && lane == 0)
    __hip_atomic_store((unsigned*)(ws + WS_FLAG) + cb * 64 + mb, 1u, __ATOMIC_RELAXED, __HIP_MEMORY_SCOPE_AGENT);
}

#define P_RS 144
DI void k2_gemm(const u16* __restrict__ x, const u16* __restrict__ w2, const u16* __restrict__ b2, u16* __restrict__ out, char* __restrict__ ws,
                char* lds, int mb, int cb, int kq, int lane) {
  const int i16 = lane & 15, g = lane >> 4;
  unsigned* cnt = (unsigned*)(lds + K2_CNT);
  char* ring = lds + K2_RING + kq * (K2_R * 4096);
  const int tid = kq * 64 + lane;
  const u16* xp = x + (size_t)(64 * mb + (tid >> 3)) * D_ + 64 * cb + 8 * (tid & 7);
  const u32x4 xv0 = *(const u32x4*)xp, xv1 = *(const u32x4*)(xp + 32 * D_);
  float bias[4];
#pragma unroll
  for (int dt = 0; dt < 4; ++dt) bias[dt] = bf2f(b2[64 * cb + 16 * dt + i16]);
  asm volatile("" ::: "memory");

  const char* hbase = ws + WS_H + ((size_t)(4 * mb) * 56 + 14 * kq) * 1024;
  unsigned hoff[4];
#pragma unroll
  for (int i = 0; i < 4; ++i) hoff[i] = lane * 16 + i * 57344;
  const u16* wsrc = w2 + (size_t)(14 * kq * 32 + (lane >> 3)) * D_ + 64 * cb + (((lane & 7) ^ (((lane >> 4) & 3) << 1)) << 3);
  int toff[4];
  {
    const int q = i16 >> 2, p = lane & 3;
    const int row = 4 * g + q;
    const int sw = (row >> 1) & 3;
#pragma unroll
    for (int dt = 0; dt < 4; ++dt) toff[dt] = row * 128 + (((2 * dt + (p >> 1)) ^ (sw << 1)) << 4) + 8 * (p & 1);
  }
  u32x4 hb[K2_R][4];
  f32x4 acc[4][4];
#pragma unroll
  for (int i = 0; i < 4; ++i)
#pragma unroll
    for (int jx = 0; jx < 4; ++jx) acc[i][jx] = (f32x4){0.f, 0.f, 0.f, 0.f};

#define K2_ISSUE(S) { \
    _Pragma("unroll") for (int i = 0; i < 4; ++i) gload16(hb[(S) % K2_R][i], hoff[i], hbase + (S) * 1024); \
    _Pragma("unroll") for (int pp = 0; pp < 4; ++pp) dma16(wsrc + (size_t)((S) * 32 + pp * 8) * D_, ring + ((S) % K2_R) * 4096 + pp * 1024); }
#define K2_STEP(S, W) { \
    asm volatile("s_waitcnt vmcnt(" #W ")" : "+v"(hb[(S) % K2_R][0]), "+v"(hb[(S) % K2_R][1]), "+v"(hb[(S) % K2_R][2]), "+v"(hb[(S) % K2_R][3]) :: "memory"); \
    s16x8 wf_[4]; \
    _Pragma("unroll") for (int dt = 0; dt < 4; ++dt) wf_[dt] = tr_frag(ring + ((S) % K2_R) * 4096 + toff[dt], ring + ((S) % K2_R) * 4096 + toff[dt] + 2048); \
    _Pragma("unroll") for (int i = 0; i < 4; ++i) \
    _Pragma("unroll") for (int dt = 0; dt < 4; ++dt) \
      acc[i][dt] = __builtin_amdgcn_mfma_f32_16x16x32_bf16(__builtin_bit_cast(s16x8, hb[(S) % K2_R][i]), wf_[dt], acc[i][dt], 0, 0, 0); \
    __builtin_amdgcn_sched_barrier(0); }

  K2_ISSUE(0); K2_ISSUE(1); K2_ISSUE(2); K2_ISSUE(3);
  K2_STEP(0, 24); K2_ISSUE(4);
  K2_STEP(1, 24); K2_ISSUE(5);
  K2_STEP(2, 24); K2_ISSUE(6);
  K2_STEP(3, 24); K2_ISSUE(7);
  K2_STEP(4, 24); K2_ISSUE(8);
  K2_STEP(5, 24); K2_ISSUE(9);
  K2_STEP(6, 24); K2_ISSUE(10);
  K2_STEP(7, 24); K2_ISSUE(11);
  K2_STEP(8, 24); K2_ISSUE(12);
  K2_STEP(9, 24); K2_ISSUE(13);
  K2_STEP(10, 24);
  K2_STEP(11, 16);
  K2_STEP(12, 8);
  K2_STEP(13, 0);

  {
    char* dst = ring + lane * 16;
#pragma unroll
    for (int i = 0; i < 4; ++i)
      if (i != kq) {
#pragma unroll
        for (int dt = 0; dt < 4; ++dt) *(f32x4*)(dst + i * 4096 + dt * 1024) = acc[i][dt];
      }
  }
  {
    const unsigned* fl = (const unsigned*)(ws + WS_FLAG) + cb * 64 + (lane & 31);
    for (unsigned spin = 0; spin < (1u << 22); ++spin) {
      unsigned v = __hip_atomic_load(fl, __ATOMIC_RELAXED, __HIP_MEMORY_SCOPE_AGENT);
      if (__all(v == 1u)) break;
      __builtin_amdgcn_s_sleep(2);
    }
    asm volatile("" ::: "memory");
  }
  unsigned long long sv[4];
#pragma unroll
  for (int dt = 0; dt < 4; ++dt)
    sv[dt] = __hip_atomic_load((const unsigned long long*)(ws + WS_S + (size_t)(64 * cb + 16 * dt + i16) * 4096 + (64 * mb + 16 * kq + 4 * g) * 2),
                               __ATOMIC_RELAXED, __HIP_MEMORY_SCOPE_AGENT);
  group_barrier(cnt + 12, 4, lane);
  f32x4 sum[4];
#pragma unroll
  for (int dt = 0; dt < 4; ++dt) sum[dt] = sel(kq & 2, sel(kq & 1, acc[3][dt], acc[2][dt]), sel(kq & 1, acc[1][dt], acc[0][dt]));
#pragma unroll
  for (int o = 1; o < 4; ++o) {
    const char* src = lds + K2_RING + ((kq + o) & 3) * (K2_R * 4096) + kq * 4096 + lane * 16;
#pragma unroll
    for (int dt = 0; dt < 4; ++dt) sum[dt] += *(const f32x4*)(src + dt * 1024);
  }
  char* prow = ring + kq * 4096;
#pragma unroll
  for (int dt = 0; dt < 4; ++dt) {
    const unsigned lo = (unsigned)sv[dt], hi = (unsigned)(sv[dt] >> 32);
    const float sf[4] = {bflo(lo), bfhi(lo), bflo(hi), bfhi(hi)};
#pragma unroll
    for (int r = 0; r < 4; ++r) {
      const float v = rbf(rbf(sum[dt][r]) + bias[dt]);
      const float gg = rbf(v * __builtin_amdgcn_rcpf(1.f + __builtin_amdgcn_exp2f(-1.4426950408889634f * v)));
      const __bf16 pv = (__bf16)(sf[r] * gg);
      *(__bf16*)(prow + (4 * g + r) * P_RS + (16 * dt + i16) * 2) = pv;
    }
  }
  group_barrier(cnt + 13, 4, lane);
  {
    const int t = tid >> 3;
    u16* op = out + (size_t)(64 * mb + t) * D_ + 64 * cb + 8 * (tid & 7);
#pragma unroll
    for (int hh = 0; hh < 2; ++hh) {
      const int tt = t + 32 * hh, w = tt >> 4;
      const u32x4 pv = *(const u32x4*)(lds + K2_RING + w * (K2_R * 4096) + w * 4096 + (tt & 15) * P_RS + (tid & 7) * 16);
      const u32x4 xv = hh ? xv1 : xv0;
      u32x4 o;
#pragma unroll
      for (int c = 0; c < 4; ++c) o[c] = pkbf(bflo(xv[c]) + bflo(pv[c]), bfhi(xv[c]) + bfhi(pv[c]));
      *(u32x4*)(op + (size_t)hh * 32 * D_) = o;
    }
  }
}

__global__ __launch_bounds__(512, 2) void k2_kernel(const u16* __restrict__ x, const u16* __restrict__ lar, const u16* __restrict__ aim,
                                                    const u16* __restrict__ cre, const u16* __restrict__ cim, const u16* __restrict__ ldt,
                                                    const u16* __restrict__ dsk, const u16* __restrict__ w2, const u16* __restrict__ b2,
                                                    u16* __restrict__ out, char* __restrict__ ws) {
  extern __shared__ __attribute__((aligned(16))) char lds[];
  const int lane = threadIdx.x & 63;
  const int wid = __builtin_amdgcn_readfirstlane(threadIdx.x >> 6);
  const int b = blockIdx.x, xcd = b & 7, j = b >> 3;
  const int mb = 4 * xcd + (j >> 3), cb = j & 7;
  if (threadIdx.x < 16) ((unsigned*)(lds + K2_CNT))[threadIdx.x] = 0u;
  __syncthreads();
  if (wid < 4) k2_gemm(x, w2, b2, out, ws, lds, mb, cb, wid, lane);
  else k2_conv(lar, aim, cre, cim, ldt, dsk, ws, lds, mb, cb, wid - 4, lane);
}

extern "C" void kernel_launch(void* const* d_in, const int* in_sizes, int n_in, void* d_out, int out_size, void* d_ws, size_t ws_size,
                              hipStream_t stream) {
  const u16* x = (const u16*)d_in[0];
  const u16* lar = (const u16*)d_in[1];
  const u16* aim = (const u16*)d_in[2];
  const u16* cre = (const u16*)d_in[3];
  const u16* cim = (const u16*)d_in[4];
  const u16* ldt = (const u16*)d_in[5];
  const u16* dsk = (const u16*)d_in[6];
  const u16* w1 = (const u16*)d_in[8];
  const u16* b1 = (const u16*)d_in[9];
  const u16* w2 = (const u16*)d_in[10];
  const u16* b2 = (const u16*)d_in[11];
  (void)hipFuncSetAttribute((const void*)k1_kernel, hipFuncAttributeMaxDynamicSharedMemorySize, 163840);
  (void)hipFuncSetAttribute((const void*)k2_kernel, hipFuncAttributeMaxDynamicSharedMemorySize, K2_LDS);
  k1_kernel<<<256, 512, 163840, stream>>>(x, w1, b1, (char*)d_ws);
  k2_kernel<<<256, 512, K2_LDS, stream>>>(x, lar, aim, cre, cim, ldt, dsk, w2, b2, (u16*)d_out, (char*)d_ws);
}
```

```cpp
#include <hip/hip_runtime.h>
#include <stdint.h>

typedef unsigned short u16;
typedef unsigned long long u64;
typedef __attribute__((ext_vector_type(4))) short s16x4;
typedef __attribute__((ext_vector_type(8))) short s16x8;
typedef __attribute__((ext_vector_type(8))) _Float16 f16x8;
typedef __attribute__((ext_vector_type(2))) _Float16 f16x2;
typedef __attribute__((ext_vector_type(2))) __bf16 bf16x2;
typedef __attribute__((ext_vector_type(2))) float f32x2;
typedef __attribute__((ext_vector_type(4))) float f32x4;
typedef __attribute__((ext_vector_type(4))) unsigned u32x4;
typedef __attribute__((ext_vector_type(2))) unsigned u32x2;
#define LDSP __attribute__((address_space(3)))
#define GLBP __attribute__((address_space(1)))
#define DI __device__ __forceinline__

#define NLAUNCH 1

#define L_ 2048
#define D_ 512
#define N_ 128
#define F_ 1792

#define WS_H 0
#define WS_UR (8u << 20)
#define WS_S (10u << 20)
#define WS_STAT (12u << 20)
#define WS_FLAG (13u << 20)
#define WS_KAP (16u << 20)
#define FL_LN 128
#define FL_H 384
#define FL_S 512
#define FL_ST 768

#define STAMP_INIT(kid)
#define STAMP()
#define STAMP0()
#define STAMP_DECL
#define STAMP_FLUSH()
#define TK0_ARG
#define TK0_PASS

DI float bf2f(unsigned v) { return __uint_as_float(v << 16); }
DI float bflo(unsigned v) { return __uint_as_float(v << 16); }
DI float bfhi(unsigned v) { return __uint_as_float(v & 0xffff0000u); }
DI unsigned pkbf(float a, float b) { bf16x2 q = {(__bf16)a, (__bf16)b}; return __builtin_bit_cast(unsigned, q); }
DI float rbf(float a) { __bf16 q = (__bf16)a; return (float)q; }
DI unsigned pkh(float a, float b) { f32x2 v = {a, b}; f16x2 p = __builtin_convertvector(v, f16x2); return __builtin_bit_cast(unsigned, p); }
DI float dot2(unsigned a, unsigned b, float c) { return __builtin_amdgcn_fdot2_f32_bf16(__builtin_bit_cast(bf16x2, a), __builtin_bit_cast(bf16x2, b), c, false); }
template <class T> DI T sel(int c, T a, T b) { return c ? a : b; }
DI void dma16(const void* g, char* l) { __builtin_amdgcn_global_load_lds((const GLBP unsigned*)g, (LDSP unsigned*)l, 16, 0, 0); }
DI s16x8 tr_frag(const char* p0, const char* p1) {
  s16x4 a = __builtin_amdgcn_ds_read_tr16_b64_v4i16((LDSP s16x4*)p0);
  s16x4 b = __builtin_amdgcn_ds_read_tr16_b64_v4i16((LDSP s16x4*)p1);
  return __builtin_shufflevector(a, b, 0, 1, 2, 3, 4, 5, 6, 7);
}
#define SCHED() __builtin_amdgcn_sched_barrier(0)
#define WAITV_BAR(n) do { SCHED(); asm volatile("s_waitcnt vmcnt(" #n ") lgkmcnt(0)\n\ts_barrier" ::: "memory"); SCHED(); } while (0)
#define LGKM_BAR() do { SCHED(); asm volatile("s_waitcnt lgkmcnt(0)\n\ts_barrier" ::: "memory"); SCHED(); } while (0)
#define RLX_AGENT __ATOMIC_RELAXED, __HIP_MEMORY_SCOPE_AGENT
#define RLX_WG __ATOMIC_RELAXED, __HIP_MEMORY_SCOPE_WORKGROUP

template <int CTRL, int RM> DI float dpp_add(float v) {
  return v + __builtin_bit_cast(float, __builtin_amdgcn_update_dpp(0, __builtin_bit_cast(int, v), CTRL, RM, 0xf, true));
}
DI float wave_sum63(float v) {
  v = dpp_add<0x111, 0xf>(v); v = dpp_add<0x112, 0xf>(v); v = dpp_add<0x114, 0xf>(v); v = dpp_add<0x118, 0xf>(v);
  v = dpp_add<0x142, 0xa>(v); v = dpp_add<0x143, 0xc>(v);
  return v;
}
DI float comb32(float a, float b) { auto r = __builtin_amdgcn_permlane32_swap(__float_as_uint(a), __float_as_uint(b), false, false); return __uint_as_float(r[0]) + __uint_as_float(r[1]); }
DI float comb16(float a, float b) { auto r = __builtin_amdgcn_permlane16_swap(__float_as_uint(a), __float_as_uint(b), false, false); return __uint_as_float(r[0]) + __uint_as_float(r[1]); }
DI float row_allsum(float v) { v = dpp_add<0x128, 0xf>(v); v = dpp_add<0x124, 0xf>(v); v = dpp_add<0x122, 0xf>(v); v = dpp_add<0x121, 0xf>(v); return v; }
DI float rdlane(float v, int l) { return __builtin_bit_cast(float, __builtin_amdgcn_readlane(__builtin_bit_cast(int, v), l)); }

DI void group_barrier(unsigned* cnt, unsigned target, int lane) {
  asm volatile("s_waitcnt lgkmcnt(0)" ::: "memory");
  if (lane == 0) __hip_atomic_fetch_add(cnt, 1u, RLX_WG);
  while (__hip_atomic_load(cnt, RLX_WG) < target) __builtin_amdgcn_s_sleep(1);
  asm volatile("" ::: "memory");
}
DI void poll_flags(const u64* p, u64 expect) {
  for (unsigned spin = 0; spin < (1u << 20); ++spin) {
    const u64 v = __hip_atomic_load(p, RLX_AGENT);
    if (__all(v == expect)) break;
    __builtin_amdgcn_s_sleep(2);
  }
  asm volatile("" ::: "memory");
}
DI void lds_set(unsigned* w, int lane) { if (lane == 0) __hip_atomic_store(w, 1u, RLX_WG); }
DI void lds_wait(unsigned* w) { while (__hip_atomic_load(w, RLX_WG) == 0u) __builtin_amdgcn_s_sleep(1); asm volatile("" ::: "memory"); }
DI int blk_of(int mb, int cb) { return (mb >> 2) + 8 * (((mb & 3) << 3) | cb); }

#define LDS_TOTAL 163840
#define L_CNT (LDS_TOTAL - 64)
#define L_KSC (LDS_TOTAL - 128)
#define KAPSZ 2624
#define L_KAP (L_KSC - 8 * KAPSZ)
#define K1_SLOT 32768
#define L_PRM 131072
#define L_COLS 65536
#define K2_R 3
#define L_RING2 0
#define L_W (4 * K2_R * 4096)
#define WSZ 33024
#define L_U (L_W + 2 * WSZ)
#define USZ 4608
#define L_KP (L_U + 2 * USZ)
#define KPSZ 4160
#define L_P L_KP

#define KG_DECL f32x4 kX, kY, kZ; u32x4 fX, fY, fZ, fB; float pX, pY, pZ, pB, isc; int prm;
#define KG_PARAMS f32x4& kX, f32x4& kY, f32x4& kZ, u32x4& fX, u32x4& fY, u32x4& fZ, u32x4& fB, float& pX, float& pY, float& pZ, float& pB, float& isc, int& prm
#define K kX, kY, kZ, fX, fY, fZ, fB, pX, pY, pZ, pB, isc, prm
DI void kgen_prep(KG_PARAMS, unsigned vl, unsigned va, unsigned vr, unsigned vi, float ldtv, char* lds, int wid, int lane) {
  const int i16 = lane & 15, g = lane >> 4;
  const float dt = rbf(__builtin_amdgcn_exp2f(ldtv * 1.4426950408889634f));
  const float ar = rbf(__builtin_amdgcn_exp2f(bf2f(vl) * 1.4426950408889634f));
  const float al = -(ar * dt) * 1.4426950408889634f;
  const float om = (bf2f(va) * dt) * 0.15915494309189535f;
  const float cr = bf2f(vr) * dt, ci = bf2f(vi) * dt;
  const float sabs = rdlane(wave_sum63(fabsf(cr) + fabsf(ci)), 63);
  int ex = (int)((__float_as_uint(sabs) >> 23) & 255);
  ex = ex < 20 ? 20 : (ex > 230 ? 230 : ex);
  const float sc2 = __uint_as_float((unsigned)(268 - ex) << 23);
  isc = __uint_as_float((unsigned)(ex - 13) << 23);
  if (lane < 32) *(f32x4*)(lds + L_PRM + wid * 512 + lane * 16) = (f32x4){al, om, cr * sc2, ci * sc2};
  if (lane == 0) *(float*)(lds + L_KSC + wid * 4) = sabs;
  prm = L_PRM + wid * 512 + g * 64;
  pX = (float)(128 * (i16 >> 3) + (i16 & 7));
  pY = (float)(i16 < 8 ? 256 + 128 * (i16 >> 2) + 2 * (i16 & 3) : 512 + 128 * ((i16 - 8) >> 1) + 4 * (i16 & 1));
  pZ = (float)(i16 < 8 ? 1024 + 128 * i16 : 2048);
  pB = (float)(8 * i16);
  kX = (f32x4){0.f, 0.f, 0.f, 0.f}; kY = kX; kZ = kX;
}
#define KG_PR(C) (*(const f32x4*)(lds + prm + ((C) >> 2) * 256 + ((C) & 3) * 16))
template <int C>
DI void kgen_chunk(KG_PARAMS, const f32x4 pr) {
  constexpr int e = C & 3;
  const float al = pr[0], om = pr[1];
  float m, ph, c, sn;
  m = __builtin_amdgcn_exp2f(al * pX); ph = __builtin_amdgcn_fractf(om * pX);
  fX[e] = pkh(m * __builtin_amdgcn_cosf(ph), m * __builtin_amdgcn_sinf(ph));
  m = __builtin_amdgcn_exp2f(al * pY); ph = __builtin_amdgcn_fractf(om * pY);
  fY[e] = pkh(m * __builtin_amdgcn_cosf(ph), m * __builtin_amdgcn_sinf(ph));
  m = __builtin_amdgcn_exp2f(al * pZ); ph = __builtin_amdgcn_fractf(om * pZ);
  fZ[e] = pkh(m * __builtin_amdgcn_cosf(ph), m * __builtin_amdgcn_sinf(ph));
  m = __builtin_amdgcn_exp2f(al * pB); ph = __builtin_amdgcn_fractf(om * pB);
  c = m * __builtin_amdgcn_cosf(ph); sn = m * __builtin_amdgcn_sinf(ph);
  fB[e] = pkh(pr[2] * c - pr[3] * sn, -(pr[2] * sn + pr[3] * c));
  if (e == 3) {
    const f16x8 hb = __builtin_bit_cast(f16x8, fB);
    kX = __builtin_amdgcn_mfma_f32_16x16x32_f16(__builtin_bit_cast(f16x8, fX), hb, kX, 0, 0, 0);
    kY = __builtin_amdgcn_mfma_f32_16x16x32_f16(__builtin_bit_cast(f16x8, fY), hb, kY, 0, 0, 0);
    kZ = __builtin_amdgcn_mfma_f32_16x16x32_f16(__builtin_bit_cast(f16x8, fZ), hb, kZ, 0, 0, 0);
  }
}
DI void kgen_store(KG_PARAMS, char* lds, int wid, int lane) {
  const int i16 = lane & 15, g = lane >> 4;
  float* kap = (float*)(lds + L_KAP + wid * KAPSZ);
  *(f32x4*)(kap + 128 * (g >> 1) + 4 * (g & 1) + 8 * i16) = kX * isc;
  if (g < 2) {
    *(f32x4*)(kap + 256 + 64 * g + 4 * i16) = kY * isc;
#pragma unroll
    for (int r = 0; r < 4; ++r) kap[512 + 16 * (4 * g + r) + i16] = kZ[r] * isc;
  } else {
    *(f32x2*)(kap + 384 + 64 * (g - 2) + 2 * i16) = (f32x2){kY[0] * isc, kY[1] * isc};
    *(f32x2*)(kap + 384 + 64 * (g - 2) + 32 + 2 * i16) = (f32x2){kY[2] * isc, kY[3] * isc};
    if (g == 2 && i16 == 0) kap[640] = kZ[0] * isc;
  }
}
DI void ld_u16(unsigned& v, const u16* p) { asm volatile("global_load_ushort %0, %1, off" : "=v"(v) : "v"(p) : "memory"); }
DI void ld_u64(u64& v, const void* p) { asm volatile("global_load_dwordx2 %0, %1, off sc1" : "=v"(v) : "v"(p) : "memory"); }
DI void ld_x2(u32x2& v, const void* p) { asm volatile("global_load_dwordx2 %0, %1, off" : "=v"(v) : "v"(p) : "memory"); }

template <int T>
DI void k1_issue(const u16* xs0, const u16* xs1, const u16* ws0, const u16* ws1, char* lds, int wid) {
  char* slot = lds + (T % 4) * K1_SLOT;
  dma16(xs0 + T * 64, slot + wid * 2048);
  dma16(xs1 + T * 64, slot + wid * 2048 + 1024);
  dma16(ws0 + (size_t)T * 64 * F_, slot + 16384 + wid * 2048);
  dma16(ws1 + (size_t)T * 64 * F_, slot + 16384 + wid * 2048 + 1024);
}

DI float gelu_f(float v) {
  const float c0 = -2.302208198f;
  const float c1 = -0.1029432397f;
  const float z = v * fmaf(v * v, c1, c0);
  return v * __builtin_amdgcn_rcpf(1.f + __builtin_amdgcn_exp2f(z));
}

DI void p1_gemm(const u16* __restrict__ x, const u16* __restrict__ w1, const u16* __restrict__ b1, const u16* __restrict__ lar,
                const u16* __restrict__ aim, const u16* __restrict__ cre, const u16* __restrict__ cim, const u16* __restrict__ ldt,
                char* __restrict__ ws, char* lds, int panel, int fs, int dch, int wid, int lane TK0_ARG) {
  STAMP_DECL STAMP_INIT(0) STAMP0();
  const int wm = wid >> 2, wf = (wid >> 1) & 1, kh = wid & 1;
  const int n = lane & 15, g = lane >> 4;
  const int row0 = panel * 128, f0 = fs * 128;
  const int d = dch + (wid >> 2);
  unsigned vl, va, vr, vi;
  {
    const int ni = d * N_ + 32 * (wid & 3) + (lane & 31);
    ld_u16(vl, lar + ni); ld_u16(va, aim + ni); ld_u16(vr, cre + ni); ld_u16(vi, cim + ni);
  }
  unsigned vdt;
  ld_u16(vdt, ldt + d);
  u32x2 bia0, bia1;
  const int fl = 64 * wf + 32 * kh + 4 * g;
  ld_x2(bia0, b1 + f0 + fl); ld_x2(bia1, b1 + f0 + fl + 16);
  u64 aOwn, aCon;
  ld_u64(aOwn, ws + WS_FLAG + (size_t)blockIdx.x * 1024);
  const int cblk = blk_of(2 * panel + ((lane >> 3) & 1), lane & 7);
  ld_u64(aCon, ws + WS_FLAG + (size_t)cblk * 1024);
  const int xr = 16 * wid + (lane >> 3);
  const u16* xs0 = x + (size_t)(row0 + xr) * D_ + (((lane & 7) ^ ((xr >> 1) & 7)) << 3);
  const u16* xs1 = x + (size_t)(row0 + xr + 8) * D_ + (((lane & 7) ^ (((xr + 8) >> 1) & 7)) << 3);
  const int kr = 8 * wid + (lane >> 4);
  const int sw0 = ((kr & 3) << 2) | ((kr >> 2) & 3), sw1 = (((kr + 4) & 3) << 2) | (((kr + 4) >> 2) & 3);
  const u16* ws0 = w1 + (size_t)kr * F_ + f0 + (((lane & 15) ^ sw0) << 3);
  const u16* ws1 = w1 + (size_t)(kr + 4) * F_ + f0 + (((lane & 15) ^ sw1) << 3);
  k1_issue<0>(xs0, xs1, ws0, ws1, lds, wid);
  k1_issue<1>(xs0, xs1, ws0, ws1, lds, wid);
  k1_issue<2>(xs0, xs1, ws0, ws1, lds, wid);
  k1_issue<3>(xs0, xs1, ws0, ws1, lds, wid);
  const int xoff = (64 * wm + n) * 128 + (((4 * kh + g) ^ ((n >> 1) & 7)) << 4);
  const int q = (lane & 15) >> 2, p = lane & 3;
  int woff[4];
  {
    const int r0 = 32 * kh + 8 * g + q;
    const int s0 = (q << 2) | ((2 * g) & 3);
#pragma unroll
    for (int i = 0; i < 4; ++i) {
      const int ft = i ^ (2 * kh + wm);
      const int ch = 2 * (4 * wf + ft) + (p >> 1);
      woff[i] = 16384 + r0 * 256 + ((ch ^ s0) << 4) + 8 * (p & 1);
    }
  }
  f32x4 acc[4][4];
#pragma unroll
  for (int mi = 0; mi < 4; ++mi)
#pragma unroll
    for (int fi = 0; fi < 4; ++fi) acc[mi][fi] = (f32x4){0.f, 0.f, 0.f, 0.f};
  float cs0 = 0.f, cs1 = 0.f;
  STAMP();
  asm volatile("s_waitcnt vmcnt(20)" : "+v"(vl), "+v"(va), "+v"(vr), "+v"(vi), "+v"(vdt) :: "memory");
  KG_DECL
  kgen_prep(K, vl, va, vr, vi, bf2f(vdt), lds, wid, lane);
  kgen_chunk<0>(K, KG_PR(0)); kgen_chunk<1>(K, KG_PR(1));
  STAMP();

  s16x8 xf[4], wfr[4];
  f32x4 pr_;
#define K1_READ(T) { \
    const char* slot = lds + ((T) % 4) * K1_SLOT; \
    _Pragma("unroll") for (int mi = 0; mi < 4; ++mi) xf[mi] = *(const s16x8*)(slot + xoff + mi * 2048); \
    _Pragma("unroll") for (int fi = 0; fi < 4; ++fi) wfr[fi] = tr_frag(slot + woff[fi], slot + (woff[fi] ^ 16) + 1024); }
#define K1_MMA() { \
    _Pragma("unroll") for (int mi = 0; mi < 4; ++mi) \
    _Pragma("unroll") for (int fi = 0; fi < 4; ++fi) acc[mi][fi] = __builtin_amdgcn_mfma_f32_16x16x32_bf16(wfr[fi], xf[mi], acc[mi][fi], 0, 0, 0); \
    const u32x4 wa = __builtin_bit_cast(u32x4, wfr[0]), wb = __builtin_bit_cast(u32x4, wfr[2]); \
    _Pragma("unroll") for (int i = 0; i < 4; ++i) { cs0 = dot2(wa[i], 0x3F803F80u, cs0); cs1 = dot2(wb[i], 0x3F803F80u, cs1); } \
    asm volatile("" : "+v"(cs0), "+v"(cs1)); }
#define K1_ISSUE2(T) { k1_issue<T>(xs0, xs1, ws0, ws1, lds, wid); k1_issue<T>(xs0 + 64 * D_, xs1 + 64 * D_, ws0 + 32 * F_, ws1 + 32 * F_, lds, wid + 4); }
  const u64* lnfl = (const u64*)(ws + WS_FLAG + (size_t)blockIdx.x * 1024 + FL_ST) + (lane & 1);
  u64 lnv;
  u64 st[4];
#define K1_STATS() { \
    if (!__all(lnv == aOwn + 1)) poll_flags(lnfl, aOwn + 1); \
    _Pragma("unroll") for (int mi = 0; mi < 4; ++mi) st[mi] = __hip_atomic_load((const u64*)(ws + WS_STAT) + row0 + 64 * wm + 16 * mi + n, RLX_AGENT); \
    SCHED(); }
  if (wm == 0) {
    WAITV_BAR(12); STAMP(); pr_ = KG_PR(2); SCHED(); K1_READ(0); SCHED(); kgen_chunk<2>(K, pr_); SCHED(); K1_MMA();
    WAITV_BAR(8); K1_ISSUE2(4); pr_ = KG_PR(3); SCHED(); K1_READ(1); SCHED(); kgen_chunk<3>(K, pr_); SCHED(); K1_MMA();
    WAITV_BAR(12); K1_ISSUE2(5); pr_ = KG_PR(4); SCHED(); K1_READ(2); SCHED(); kgen_chunk<4>(K, pr_); SCHED(); K1_MMA();
    WAITV_BAR(16); K1_ISSUE2(6); pr_ = KG_PR(5); SCHED(); K1_READ(3); SCHED(); kgen_chunk<5>(K, pr_); SCHED(); K1_MMA();
    WAITV_BAR(16); STAMP(); K1_ISSUE2(7); pr_ = KG_PR(6); SCHED(); K1_READ(4); SCHED(); kgen_chunk<6>(K, pr_); SCHED(); K1_MMA();
    ld_u64(lnv, lnfl);
    WAITV_BAR(17); pr_ = KG_PR(7); SCHED(); K1_READ(5); SCHED(); kgen_chunk<7>(K, pr_); SCHED(); K1_MMA();
    WAITV_BAR(9); K1_READ(6); SCHED(); kgen_store(K, lds, wid, lane); SCHED(); K1_MMA();
    SCHED();
    asm volatile("s_waitcnt vmcnt(0)" : "+v"(aOwn), "+v"(aCon), "+v"(bia0), "+v"(bia1), "+v"(lnv) :: "memory");
    LGKM_BAR(); STAMP();
    K1_READ(7); K1_STATS(); K1_MMA();
  } else {
    WAITV_BAR(12); STAMP(); pr_ = KG_PR(2); SCHED(); K1_READ(0); SCHED(); kgen_chunk<2>(K, pr_);
    WAITV_BAR(8); K1_MMA(); SCHED(); pr_ = KG_PR(3); SCHED(); K1_READ(1); SCHED(); kgen_chunk<3>(K, pr_);
    WAITV_BAR(4); K1_MMA(); SCHED(); pr_ = KG_PR(4); SCHED(); K1_READ(2); SCHED(); kgen_chunk<4>(K, pr_);
    WAITV_BAR(0); K1_MMA(); SCHED(); pr_ = KG_PR(5); SCHED(); K1_READ(3); SCHED(); kgen_chunk<5>(K, pr_);
    WAITV_BAR(0); STAMP(); K1_MMA(); SCHED(); pr_ = KG_PR(6); SCHED(); K1_READ(4); SCHED(); kgen_chunk<6>(K, pr_);
    ld_u64(lnv, lnfl);
    WAITV_BAR(1); K1_MMA(); SCHED(); pr_ = KG_PR(7); SCHED(); K1_READ(5); SCHED(); kgen_chunk<7>(K, pr_);
    WAITV_BAR(1); K1_MMA(); SCHED(); K1_READ(6); SCHED(); kgen_store(K, lds, wid, lane);
    SCHED();
    asm volatile("s_waitcnt vmcnt(0)" : "+v"(aOwn), "+v"(aCon), "+v"(bia0), "+v"(bia1), "+v"(lnv) :: "memory");
    LGKM_BAR(); STAMP();
    K1_STATS(); K1_MMA(); SCHED(); K1_READ(7); K1_MMA();
  }
  STAMP();

  float* colS = (float*)(lds + L_COLS);
  {
    char* dst = lds + wid * 8192 + lane * 16;
#pragma unroll
    for (int mi = 0; mi < 4; ++mi) {
      *(f32x4*)(dst + (2 * mi) * 1024) = acc[mi][2];
      *(f32x4*)(dst + (2 * mi + 1) * 1024) = acc[mi][3];
    }
    float cc = comb32(cs0, cs1);
    cc = comb16(cc, cc);
    if (!(g & 1)) colS[kh * 128 + 64 * wf + 16 * (2 * (kh ^ (g >> 1)) + wm) + n] = cc;
  }
  LGKM_BAR();
  STAMP();
  f32x4 pa[4][2];
  f32x4 c0, c1;
  {
    const char* src = lds + (wid ^ 1) * 8192 + lane * 16;
    const int fa = fl + 16 * wm, fb = fl + 16 * (wm ^ 1);
    c0 = *(const f32x4*)(colS + fa) + *(const f32x4*)(colS + 128 + fa);
    c1 = *(const f32x4*)(colS + fb) + *(const f32x4*)(colS + 128 + fb);
#pragma unroll
    for (int mi = 0; mi < 4; ++mi) { pa[mi][0] = *(const f32x4*)(src + (2 * mi) * 1024); pa[mi][1] = *(const f32x4*)(src + (2 * mi + 1) * 1024); }
  }
  LGKM_BAR();
  {
    const u32x2 bl = sel(wm, bia1, bia0), bh = sel(wm, bia0, bia1);
    const float bb[8] = {bflo(bl[0]), bfhi(bl[0]), bflo(bl[1]), bfhi(bl[1]), bflo(bh[0]), bfhi(bh[0]), bflo(bh[1]), bfhi(bh[1])};
    const __amdgpu_buffer_rsrc_t hr = __builtin_amdgcn_make_buffer_rsrc(ws + WS_H, 0, L_ * F_ * 2, 0x00020000);
    const unsigned hoff = ((unsigned)(panel * 8 + 4 * wm) * 56 + (4 * fs + 2 * wf + kh)) * 1024 + lane * 16;
#pragma unroll
    for (int mi = 0; mi < 4; ++mi) {
      const float mr = __uint_as_float((unsigned)st[mi]), rinv = __uint_as_float((unsigned)(st[mi] >> 32));
      float o[8];
#pragma unroll
      for (int i = 0; i < 4; ++i) {
        o[i] = gelu_f(fmaf(acc[mi][0][i] + pa[mi][0][i], rinv, fmaf(mr, c0[i], bb[i])));
        o[4 + i] = gelu_f(fmaf(acc[mi][1][i] + pa[mi][1][i], rinv, fmaf(mr, c1[i], bb[4 + i])));
      }
      const u32x2 lo = {pkbf(o[0], o[1]), pkbf(o[2], o[3])}, hi = {pkbf(o[4], o[5]), pkbf(o[6], o[7])};
      const u32x2 a = sel(wm, hi, lo), b = sel(wm, lo, hi);
      const u32x4 pk = {a[0], a[1], b[0], b[1]};
      __builtin_amdgcn_raw_buffer_store_b128(pk, hr, hoff + mi * 57344, 0, 16);
    }
  }
  STAMP();
  asm volatile("s_waitcnt vmcnt(0)" ::: "memory");
  unsigned old = 0;
  if (lane == 0) old = __hip_atomic_fetch_add((unsigned*)(lds + L_CNT) + 14, 1u, RLX_WG);
  old = __builtin_amdgcn_readfirstlane(old);
  if (old == 7 && lane < 16) __hip_atomic_store((u64*)(ws + WS_FLAG + (size_t)cblk * 1024 + FL_H) + fs, aCon + 1, RLX_AGENT);
  STAMP(); STAMP_FLUSH();
}

#define LN_RS 1040
DI void p1_aux(const u16* __restrict__ x, const u16* __restrict__ lar, const u16* __restrict__ aim, const u16* __restrict__ cre,
               const u16* __restrict__ cim, const u16* __restrict__ ldt, char* __restrict__ ws, char* lds, int idx, int dch, int wid,
               int lane TK0_ARG) {
  STAMP_DECL STAMP_INIT(0) STAMP0();
  const int t0 = idx * 64;
  const int d = dch + (wid >> 2);
  u32x4 v[8];
#pragma unroll
  for (int i = 0; i < 8; ++i) v[i] = *(const u32x4*)(x + (size_t)(t0 + 8 * wid + i) * D_ + lane * 8);
  const int ni = d * N_ + 32 * (wid & 3) + (lane & 31);
  const unsigned vl = lar[ni], va = aim[ni], vr = cre[ni], vi = cim[ni];
  const float ldtv = bf2f(ldt[d]);
  const u64 aCon = __hip_atomic_load((const u64*)(ws + WS_FLAG + (size_t)(threadIdx.x & 255) * 1024), RLX_AGENT);
  const int sblk = (idx >> 2) + 8 * (((idx >> 1) & 1) * 14 + (lane < 14 ? lane : 0));
  const u64 aSt = __hip_atomic_load((const u64*)(ws + WS_FLAG + (size_t)sblk * 1024), RLX_AGENT);
  LGKM_BAR();
  SCHED();
  float sA[8], sB[8];
#pragma unroll
  for (int i = 0; i < 8; ++i) {
    float a = dot2(v[i][0], 0x3F803F80u, 0.f), b = dot2(v[i][0], v[i][0], 0.f);
#pragma unroll
    for (int c = 1; c < 4; ++c) { a = dot2(v[i][c], 0x3F803F80u, a); b = dot2(v[i][c], v[i][c], b); }
    sA[i] = a; sB[i] = b;
  }
#pragma unroll
  for (int i = 0; i < 4; ++i) { sA[i] = comb32(sA[i], sA[i + 4]); sB[i] = comb32(sB[i], sB[i + 4]); }
#pragma unroll
  for (int i = 0; i < 2; ++i) { sA[i] = row_allsum(comb16(sA[i], sA[i + 2])); sB[i] = row_allsum(comb16(sB[i], sB[i + 2])); }
  const float mv = ((lane & 1) ? sA[1] : sA[0]) * (1.f / 512.f);
  const float var = fmaxf(fmaf(-mv, mv, ((lane & 1) ? sB[1] : sB[0]) * (1.f / 512.f)), 0.f);
  const float mbv = rbf(mv);
  const float sd = rbf(sqrtf(rbf(rbf(var) + 1e-5f)));
  const float rv = 1.f / sd;
  if ((lane & 14) == 0) {
    const u64 pk = (u64)__float_as_uint(-mbv * rv) | ((u64)__float_as_uint(rv) << 32);
    __hip_atomic_store((u64*)(ws + WS_STAT) + t0 + 8 * wid + (lane & 1) + 2 * (lane >> 4), pk, RLX_AGENT);
  }
  asm volatile("s_waitcnt vmcnt(0)" ::: "memory");
  {
    unsigned old = 0;
    if (lane == 0) old = __hip_atomic_fetch_add((unsigned*)(lds + L_CNT) + 14, 1u, RLX_WG);
    old = __builtin_amdgcn_readfirstlane(old);
    if (old == 7 && lane < 14) __hip_atomic_store((u64*)(ws + WS_FLAG + (size_t)sblk * 1024 + FL_ST) + (idx & 1), aSt + 1, RLX_AGENT);
  }
  STAMP();
#pragma unroll
  for (int i = 0; i < 8; ++i) {
    const float rinv = rdlane(rv, (i & 1) + 16 * (i >> 1)), mb = rdlane(mbv, (i & 1) + 16 * (i >> 1));
    u32x4 pk;
#pragma unroll
    for (int c = 0; c < 4; ++c) {
      const unsigned a = pkbf(bflo(v[i][c]) - mb, bfhi(v[i][c]) - mb);
      const unsigned b = pkbf(bflo(a) * rinv, bfhi(a) * rinv);
      pk[c] = pkh(bflo(b), bfhi(b));
    }
    *(u32x4*)(lds + (63 - (8 * wid + i)) * LN_RS + lane * 16) = pk;
  }
  STAMP();
  LGKM_BAR();
  char* t2 = lds + 64 * LN_RS + wid * 8192;
  const int g = lane >> 4, q = (lane & 15) >> 2, p = lane & 3, dl = lane & 15;
#pragma unroll
  for (int dt = 0; dt < 4; ++dt) {
#pragma unroll
    for (int hh = 0; hh < 2; ++hh) {
      const int rc = 4 * hh + g;
      const char* a = lds + (8 * rc + q) * LN_RS + (64 * wid + 16 * dt + 4 * p) * 2;
      s16x8 f = tr_frag(a, a + 4 * LN_RS);
      const int dd2 = 16 * dt + dl;
      *(s16x8*)(t2 + dd2 * 128 + ((rc ^ (dd2 & 7)) << 4)) = f;
    }
  }
  const __amdgpu_buffer_rsrc_t ur = __builtin_amdgcn_make_buffer_rsrc(ws + WS_UR, 0, L_ * D_ * 2, 0x00020000);
  const int x0 = L_ - 64 - t0;
#pragma unroll
  for (int i = 0; i < 8; ++i) {
    const int dd2 = 8 * i + (lane >> 3), c = lane & 7;
    u32x4 f = *(const u32x4*)(t2 + dd2 * 128 + ((c ^ (dd2 & 7)) << 4));
    __builtin_amdgcn_raw_buffer_store_b128(f, ur, (unsigned)(64 * wid + dd2) * 4096 + x0 * 2 + c * 16, 0, 16);
  }
  STAMP();
  WAITV_BAR(0);
  if (threadIdx.x < 256) __hip_atomic_store((u64*)(ws + WS_FLAG + (size_t)threadIdx.x * 1024 + FL_LN) + idx, aCon + 1, RLX_AGENT);
  STAMP();
  KG_DECL
  kgen_prep(K, vl, va, vr, vi, ldtv, lds, wid, lane);
  kgen_chunk<0>(K, KG_PR(0)); kgen_chunk<1>(K, KG_PR(1)); kgen_chunk<2>(K, KG_PR(2)); kgen_chunk<3>(K, KG_PR(3));
  kgen_chunk<4>(K, KG_PR(4)); kgen_chunk<5>(K, KG_PR(5)); kgen_chunk<6>(K, KG_PR(6)); kgen_chunk<7>(K, KG_PR(7));
  kgen_store(K, lds, wid, lane);
  LGKM_BAR();
  STAMP(); STAMP_FLUSH();
}

DI void gload16(u32x4& v, unsigned voff, const char* sbase) {
  asm volatile("global_load_dwordx4 %0, %1, %2 sc1" : "=v"(v) : "v"(voff), "s"(sbase) : "memory");
}

DI void p2_conv(const u16* __restrict__ dsk, char* __restrict__ ws, char* lds, int mb, int cb, int cw, int lane TK0_ARG) {
  STAMP_DECL STAMP_INIT(1) STAMP0();
  const int ch = cw >> 1, hf = cw & 1;
  const int d = 64 * cb + 2 * mb + ch;
  const int i16 = lane & 15, g = lane >> 4;
  unsigned* cnt = (unsigned*)(lds + L_CNT);
  const u64 aOwn = __hip_atomic_load((const u64*)(ws + WS_FLAG + (size_t)blockIdx.x * 1024), RLX_AGENT);
  const int cblk = blk_of(lane & 31, cb);
  const u64 aCon = __hip_atomic_load((const u64*)(ws + WS_FLAG + (size_t)cblk * 1024), RLX_AGENT);
  const float dskip = bf2f(dsk[d]);
  STAMP();
  __builtin_amdgcn_s_setprio(2);
  if (cw == 0) { poll_flags((const u64*)(ws + WS_FLAG + (size_t)blockIdx.x * 1024 + FL_LN) + (lane & 31), aOwn + 1); lds_set(cnt + 0, lane); }
  else lds_wait(cnt + 0);
  char* ul = lds + L_U + ch * USZ;
  const __amdgpu_buffer_rsrc_t urr = __builtin_amdgcn_make_buffer_rsrc(ws + WS_UR, 0, L_ * D_ * 2, 0x00020000);
  const u32x4 ua_ = __builtin_amdgcn_raw_buffer_load_b128(urr, (unsigned)d * 4096 + hf * 2048 + lane * 16, 0, 16);
  const u32x4 ub_ = __builtin_amdgcn_raw_buffer_load_b128(urr, (unsigned)d * 4096 + hf * 2048 + 1024 + lane * 16, 0, 16);
  SCHED();
  STAMP();
  const f32x4 ks = *(const f32x4*)(lds + L_KSC + ch * 16);
  const float sabs = (ks[0] + ks[1]) + (ks[2] + ks[3]);
  int ex = (int)((__float_as_uint(sabs) >> 23) & 255);
  ex = ex < 20 ? 20 : (ex > 230 ? 230 : ex);
  const float scale = __uint_as_float((unsigned)(267 - ex) << 23);
  const float iscale = __uint_as_float((unsigned)(ex - 13) << 23);
  char* kp = lds + L_KP + ch * KPSZ;
  {
    const float* k0 = (const float*)(lds + L_KAP + (4 * ch) * KAPSZ);
#undef K
#define KSUM4(T, i) (((*(const T*)(k0 + (i)) + *(const T*)(k0 + 656 + (i))) + (*(const T*)(k0 + 1312 + (i)) + *(const T*)(k0 + 1968 + (i)))) * scale)
    {
      const int A = 64 * hf + lane;
      u32x4 o;
      if (hf == 0) {
        if (lane < 32) {
          const f32x4 a = KSUM4(f32x4, 8 * A), b = KSUM4(f32x4, 8 * A + 4);
          o = (u32x4){pkh(a[0], a[1]), pkh(a[2], a[3]), pkh(b[0], b[1]), pkh(b[2], b[3])};
        } else {
          const int i0 = 256 + 4 * (A - 32);
          const f32x4 a = KSUM4(f32x4, i0);
          const float e4 = KSUM4(float, i0 + 4);
          o = (u32x4){pkh(a[0], a[0]), pkh(a[1], a[2]), pkh(a[2], a[2]), pkh(a[3], e4)};
        }
      } else {
        const int i0 = 384 + 2 * (A - 64);
        const f32x2 a = KSUM4(f32x2, i0);
        const float e2 = KSUM4(float, i0 + 2);
        o = (u32x4){pkh(a[0], a[0]), pkh(a[0], a[1]), pkh(a[1], a[1]), pkh(e2, e2)};
      }
      *(u32x4*)(kp + 32 + A * 16) = o;
    }
    {
      const int A = 128 + 64 * hf + lane;
      const int i0 = 512 + (A - 128);
      const float e0 = KSUM4(float, i0), e1 = KSUM4(float, i0 + 1);
      const unsigned p00 = pkh(e0, e0);
      u32x4 o = {p00, p00, pkh((A & 1) ? e1 : e0, e1), pkh(e1, e1)};
      *(u32x4*)(kp + 32 + A * 16) = o;
    }
    if (hf == 0 && lane < 2) *(u32x4*)(kp + lane * 16) = (u32x4){0u, 0u, 0u, 0u};
  }
  group_barrier(cnt + 4 * ch + 1, 2, lane);
  STAMP();
  char* wl = lds + L_W + ch * WSZ;
  {
#pragma unroll
    for (int rnd = 0; rnd < 3; ++rnd) {
      const int a = rnd < 2 ? 128 * rnd + 64 * hf + lane : 256;
      if (rnd == 2 && !(hf == 0 && lane == 0)) break;
      u32x4 c0 = *(const u32x4*)(kp + a * 16), c1 = *(const u32x4*)(kp + a * 16 + 16);
      unsigned e[8] = {c0[0], c0[1], c0[2], c0[3], c1[0], c1[1], c1[2], c1[3]};
      unsigned o[7];
#pragma unroll
      for (int i = 0; i < 7; ++i) o[i] = __builtin_amdgcn_alignbit(e[i + 1], e[i], 16);
      const int m = (a & 1) | (((a >> 2) & 1) << 1) | (((a >> 1) & 1) << 2);
      char* base = wl + a * 128;
#pragma unroll
      for (int c = 0; c < 8; ++c) {
        u32x4 wv;
        if (c & 1) { const int s = (c + 1) >> 1; wv = (u32x4){e[s], e[s + 1], e[s + 2], e[s + 3]}; }
        else { const int s = c >> 1; wv = (u32x4){o[s], o[s + 1], o[s + 2], o[s + 3]}; }
        *(u32x4*)(base + ((c ^ m) << 4)) = wv;
      }
    }
  }
  *(u32x4*)(ul + hf * 2048 + lane * 16) = ua_;
  *(u32x4*)(ul + hf * 2048 + 1024 + lane * 16) = ub_;
  if (hf == 0 && lane < 32) *(u32x4*)(ul + 4096 + lane * 16) = (u32x4){0u, 0u, 0u, 0u};
  group_barrier(cnt + 4 * ch + 2, 2, lane);
  STAMP();
  f32x4 acc[8];
#pragma unroll
  for (int i = 0; i < 8; ++i) acc[i] = (f32x4){0.f, 0.f, 0.f, 0.f};
  {
    const int c = i16 + 8 * g;
    int fo[2];
#pragma unroll
    for (int par = 0; par < 2; ++par) {
      const int w = 32 * par + c;
      const int m = ((w >> 3) & 1) | (((w >> 5) & 1) << 1) | (((w >> 4) & 1) << 2);
      fo[par] = (w ^ m) << 4;
    }
    const char* fbase = wl + hf * (128 * 16);
    const char* gbase = ul + (2032 - 16 * i16 + 8 * g + 128 * hf) * 2 - 7 * 512;
#pragma unroll
    for (int s = 0; s < 4; ++s) {
      f16x8 F[8];
#pragma unroll
      for (int e = 0; e < 8; ++e) F[e] = *(const f16x8*)(fbase + fo[s & 1] + (s >> 1) * 1024 + e * 4096);
#pragma unroll
      for (int mu = 0; mu < 8; ++mu) {
        const f16x8 G = *(const f16x8*)(gbase + s * 64 + (7 - mu) * 512);
#pragma unroll
        for (int e = 0; e + mu < 8; ++e) {
          acc[e + mu] = __builtin_amdgcn_mfma_f32_16x16x32_f16(F[e], G, acc[e + mu], 0, 0, 0);
        }
      }
    }
  }
  STAMP();
  {
    char* dst = lds + L_KAP + ch * (4 * KAPSZ) + hf * 4096 + lane * 16;
#pragma unroll
    for (int i = 0; i < 4; ++i) *(f32x4*)(dst + i * 1024) = sel(hf, acc[i], acc[4 + i]);
  }
  group_barrier(cnt + 8 + ch, 2, lane);
  STAMP();
  {
    const char* src = lds + L_KAP + ch * (4 * KAPSZ) + (hf ^ 1) * 4096 + lane * 16;
    char* sdst = ws + WS_S + (size_t)d * 4096;
#pragma unroll
    for (int i = 0; i < 4; ++i) {
      f32x4 yv = (sel(hf, acc[4 + i], acc[i]) + *(const f32x4*)(src + i * 1024)) * iscale;
      const int t = 256 * (4 * hf + i) + 16 * i16 + 4 * g;
      const u32x2 uu = *(const u32x2*)(ul + (2044 - t) * 2);
      const unsigned uu0 = uu[0], uu1 = uu[1];
      const f16x2 ua = __builtin_bit_cast(f16x2, uu0), ub = __builtin_bit_cast(f16x2, uu1);
      const unsigned y01 = pkbf(yv[0], yv[1]), y23 = pkbf(yv[2], yv[3]);
      const unsigned k01 = pkbf((float)ub[1] * dskip, (float)ub[0] * dskip), k23 = pkbf((float)ua[1] * dskip, (float)ua[0] * dskip);
      const u64 pk = (u64)pkbf(bflo(y01) + bflo(k01), bfhi(y01) + bfhi(k01)) | ((u64)pkbf(bflo(y23) + bflo(k23), bfhi(y23) + bfhi(k23)) << 32);
      __hip_atomic_store((u64*)(sdst + t * 2), pk, RLX_AGENT);
    }
  }
  asm volatile("s_waitcnt vmcnt(0)" ::: "memory");
  STAMP();
  unsigned old = 0;
  if (lane == 0) old = __hip_atomic_fetch_add(cnt + 10, 1u, RLX_WG);
  old = __builtin_amdgcn_readfirstlane(old);
  if (old == 3 && lane < 32) __hip_atomic_store((u64*)(ws + WS_FLAG + (size_t)cblk * 1024 + FL_S) + mb, aCon + 1, RLX_AGENT);
  STAMP_FLUSH();
}

#define P_RS 144
DI void p2_gemm(const u16* __restrict__ x, const u16* __restrict__ w2, const u16* __restrict__ b2, u16* __restrict__ out, char* __restrict__ ws,
                char* lds, int mb, int cb, int kq, int lane TK0_ARG) {
  STAMP_DECL STAMP_INIT(1) STAMP0();
  const int i16 = lane & 15, g = lane >> 4;
  unsigned* cnt = (unsigned*)(lds + L_CNT);
  char* ring = lds + L_RING2 + kq * (K2_R * 4096);
  const int tid = kq * 64 + lane;
  const char* flg = ws + WS_FLAG + (size_t)blockIdx.x * 1024;
  u64 aOwn;
  ld_u64(aOwn, flg);
  const u16* wsrc = w2 + (size_t)(14 * kq * 32 + (lane >> 3)) * D_ + 64 * cb + (((lane & 7) ^ (((lane >> 4) & 3) << 1)) << 3);
#define K2_DMA(S) { _Pragma("unroll") for (int pp = 0; pp < 4; ++pp) dma16(wsrc + (size_t)((S) * 32 + pp * 8) * D_, ring + ((S) % K2_R) * 4096 + pp * 1024); }
  K2_DMA(0); K2_DMA(1); K2_DMA(2);
  const char* hbase = ws + WS_H + ((size_t)(4 * mb) * 56 + 14 * kq) * 1024;
  unsigned hoff[4];
#pragma unroll
  for (int i = 0; i < 4; ++i) hoff[i] = lane * 16 + i * 57344;
  int toff[4];
  {
    const int q = i16 >> 2, p = lane & 3;
    const int row = 4 * g + q;
    const int sw = (row >> 1) & 3;
#pragma unroll
    for (int dt = 0; dt < 4; ++dt) toff[dt] = row * 128 + (((2 * dt + (p >> 1)) ^ (sw << 1)) << 4) + 8 * (p & 1);
  }
  u32x4 hb[3][4];
  f32x4 acc[4][4];
#pragma unroll
  for (int i = 0; i < 4; ++i)
#pragma unroll
    for (int jx = 0; jx < 4; ++jx) acc[i][jx] = (f32x4){0.f, 0.f, 0.f, 0.f};
  STAMP();
  asm volatile("s_waitcnt vmcnt(12)" : "+v"(aOwn) :: "memory");
  if (kq == 0) {
    const u64* fp = (const u64*)(flg + FL_H) + (lane < 14 ? lane : 0);
    for (unsigned spin = 0; spin < (1u << 20); ++spin) {
      u64 v;
      asm volatile("global_load_dwordx2 %0, %1, off sc1\n\ts_waitcnt vmcnt(0)" : "=&v"(v) : "v"(fp) : "memory");
      if (__all(v == aOwn + 1)) break;
      __builtin_amdgcn_s_sleep(2);
    }
    lds_set(cnt + 4, lane);
  } else {
    lds_wait(cnt + 4);
    asm volatile("s_waitcnt vmcnt(0)" ::: "memory");
  }
  STAMP();
#define K2_HLD(S) { _Pragma("unroll") for (int i = 0; i < 4; ++i) gload16(hb[(S) % 3][i], hoff[i], hbase + (S) * 1024); }
#define K2_STEP(S, W) { \
    asm volatile("s_waitcnt vmcnt(" #W ")" : "+v"(hb[(S) % 3][0]), "+v"(hb[(S) % 3][1]), "+v"(hb[(S) % 3][2]), "+v"(hb[(S) % 3][3]) :: "memory"); \
    s16x8 wf_[4]; \
    _Pragma("unroll") for (int dt = 0; dt < 4; ++dt) wf_[dt] = tr_frag(ring + ((S) % K2_R) * 4096 + toff[dt], ring + ((S) % K2_R) * 4096 + toff[dt] + 2048); \
    _Pragma("unroll") for (int i = 0; i < 4; ++i) \
    _Pragma("unroll") for (int dt = 0; dt < 4; ++dt) \
      acc[i][dt] = __builtin_amdgcn_mfma_f32_16x16x32_bf16(__builtin_bit_cast(s16x8, hb[(S) % 3][i]), wf_[dt], acc[i][dt], 0, 0, 0); \
    SCHED(); }
  K2_HLD(0); K2_HLD(1); K2_HLD(2);
  K2_STEP(0, 8); STAMP(); K2_HLD(3); K2_DMA(3);
  K2_STEP(1, 12); K2_HLD(4); K2_DMA(4);
  K2_STEP(2, 16); K2_HLD(5); K2_DMA(5);
  K2_STEP(3, 16); K2_HLD(6); K2_DMA(6);
  K2_STEP(4, 16); K2_HLD(7); K2_DMA(7);
  K2_STEP(5, 16); K2_HLD(8); K2_DMA(8);
  K2_STEP(6, 16); K2_HLD(9); K2_DMA(9);
  K2_STEP(7, 16); STAMP(); K2_HLD(10); K2_DMA(10);
  K2_STEP(8, 16); K2_HLD(11); K2_DMA(11);
  K2_STEP(9, 16); K2_HLD(12); K2_DMA(12);
  K2_STEP(10, 16); K2_HLD(13); K2_DMA(13);
  const u16* xp = x + (size_t)(64 * mb + (tid >> 3)) * D_ + 64 * cb + 8 * (tid & 7);
  u32x4 xv0, xv1;
  asm volatile("global_load_dwordx4 %0, %1, off" : "=v"(xv0) : "v"(xp) : "memory");
  asm volatile("global_load_dwordx4 %0, %1, off" : "=v"(xv1) : "v"(xp + 32 * D_) : "memory");
  unsigned braw[4];
#pragma unroll
  for (int dt = 0; dt < 4; ++dt) ld_u16(braw[dt], b2 + 64 * cb + 16 * dt + i16);
  K2_STEP(11, 22);
  K2_STEP(12, 14);
  K2_STEP(13, 6);
  STAMP();
  {
    char* dst = ring + lane * 16;
#pragma unroll
    for (int i = 0; i < 4; ++i)
      if (i != kq) {
        const int sl = (i - kq - 1) & 3;
#pragma unroll
        for (int dt = 0; dt < 4; ++dt) *(f32x4*)(dst + sl * 4096 + dt * 1024) = acc[i][dt];
      }
  }
  if (kq == 0) { poll_flags((const u64*)(flg + FL_S) + (lane & 31), aOwn + 1); lds_set(cnt + 11, lane); }
  else lds_wait(cnt + 11);
  STAMP();
  u64 sv[4];
#pragma unroll
  for (int dt = 0; dt < 4; ++dt)
    sv[dt] = __hip_atomic_load((const u64*)(ws + WS_S + (size_t)(64 * cb + 16 * dt + i16) * 4096 + (64 * mb + 16 * kq + 4 * g) * 2), RLX_AGENT);
  group_barrier(cnt + 12, 4, lane);
  STAMP();
  f32x4 sum[4];
#pragma unroll
  for (int dt = 0; dt < 4; ++dt) sum[dt] = sel(kq & 2, sel(kq & 1, acc[3][dt], acc[2][dt]), sel(kq & 1, acc[1][dt], acc[0][dt]));
#pragma unroll
  for (int o = 1; o < 4; ++o) {
    const char* src = lds + L_RING2 + ((kq + o) & 3) * (K2_R * 4096) + (3 - o) * 4096 + lane * 16;
#pragma unroll
    for (int dt = 0; dt < 4; ++dt) sum[dt] += *(const f32x4*)(src + dt * 1024);
  }
  asm volatile("s_waitcnt vmcnt(0)" : "+v"(xv0), "+v"(xv1), "+v"(braw[0]), "+v"(braw[1]), "+v"(braw[2]), "+v"(braw[3]) :: "memory");
  char* prow = lds + L_P + kq * (16 * P_RS);
#pragma unroll
  for (int dt = 0; dt < 4; ++dt) {
    const unsigned lo = (unsigned)sv[dt], hi = (unsigned)(sv[dt] >> 32);
    const float sf[4] = {bflo(lo), bfhi(lo), bflo(hi), bfhi(hi)};
    const float bias = bf2f(braw[dt]);
#pragma unroll
    for (int r = 0; r < 4; ++r) {
      const float v = sum[dt][r] + bias;
      const float gg = v * __builtin_amdgcn_rcpf(1.f + __builtin_amdgcn_exp2f(-1.4426950408889634f * v));
      const __bf16 pv = (__bf16)(sf[r] * gg);
      *(__bf16*)(prow + (4 * g + r) * P_RS + (16 * dt + i16) * 2) = pv;
    }
  }
  group_barrier(cnt + 13, 4, lane);
  STAMP();
  {
    const int t = tid >> 3;
    u16* op = out + (size_t)(64 * mb + t) * D_ + 64 * cb + 8 * (tid & 7);
#pragma unroll
    for (int hh = 0; hh < 2; ++hh) {
      const u32x4 pv = *(const u32x4*)(lds + L_P + (t + 32 * hh) * P_RS + (tid & 7) * 16);
      const u32x4 xv = hh ? xv1 : xv0;
      u32x4 o;
#pragma unroll
      for (int c = 0; c < 4; ++c) o[c] = pkbf(bflo(xv[c]) + bflo(pv[c]), bfhi(xv[c]) + bfhi(pv[c]));
      *(u32x4*)(op + (size_t)hh * 32 * D_) = o;
    }
  }
  if (tid == 0) __hip_atomic_store((u64*)(ws + WS_FLAG + (size_t)blockIdx.x * 1024), aOwn + 1, RLX_AGENT);
  STAMP(); STAMP_FLUSH();
}

template <int PH>
__global__ __launch_bounds__(512, 2) void fused_kernel(const u16* __restrict__ x, const u16* __restrict__ lar, const u16* __restrict__ aim,
                                                       const u16* __restrict__ cre, const u16* __restrict__ cim, const u16* __restrict__ ldt,
                                                       const u16* __restrict__ dsk, const u16* __restrict__ w1, const u16* __restrict__ b1,
                                                       const u16* __restrict__ w2, const u16* __restrict__ b2, u16* __restrict__ out,
                                                       char* __restrict__ ws) {
  extern __shared__ __attribute__((aligned(16))) char lds[];
  const int lane = threadIdx.x & 63;
  const int wid = __builtin_amdgcn_readfirstlane(threadIdx.x >> 6);
  const int b = blockIdx.x, xcd = b & 7, j = b >> 3;
  const int mb = 4 * xcd + (j >> 3), cb = j & 7;
  const int dch = 64 * cb + 2 * mb;
  if (threadIdx.x < 16) ((unsigned*)(lds + L_CNT))[threadIdx.x] = 0u;
  if (PH & 1) {
    if (j < 28) p1_gemm(x, w1, b1, lar, aim, cre, cim, ldt, ws, lds, 2 * xcd + (j >= 14 ? 1 : 0), j >= 14 ? j - 14 : j, dch, wid, lane TK0_PASS);
    else p1_aux(x, lar, aim, cre, cim, ldt, ws, lds, xcd * 4 + (j - 28), dch, wid, lane TK0_PASS);
  }
  if (PH == 1) {
    __syncthreads();
    for (int i = threadIdx.x; i < (8 * KAPSZ + 64) / 16; i += 512) *(u32x4*)(ws + WS_KAP + (size_t)b * 32768 + i * 16) = *(const u32x4*)(lds + L_KAP + i * 16);
  }
  if (PH == 2) {
    for (int i = threadIdx.x; i < (8 * KAPSZ + 64) / 16; i += 512) *(u32x4*)(lds + L_KAP + i * 16) = *(const u32x4*)(ws + WS_KAP + (size_t)b * 32768 + i * 16);
    __syncthreads();
  }
  if (PH & 2) {
    if (wid < 4) p2_gemm(x, w2, b2, out, ws, lds, mb, cb, wid, lane TK0_PASS);
    else p2_conv(dsk, ws, lds, mb, cb, wid - 4, lane TK0_PASS);
  }
}

extern "C" void kernel_launch(void* const* d_in, const int* in_sizes, int n_in, void* d_out, int out_size, void* d_ws, size_t ws_size,
                              hipStream_t stream) {
  const u16* x = (const u16*)d_in[0];
  const u16* lar = (const u16*)d_in[1];
  const u16* aim = (const u16*)d_in[2];
  const u16* cre = (const u16*)d_in[3];
  const u16* cim = (const u16*)d_in[4];
  const u16* ldt = (const u16*)d_in[5];
  const u16* dsk = (const u16*)d_in[6];
  const u16* w1 = (const u16*)d_in[8];
  const u16* b1 = (const u16*)d_in[9];
  const u16* w2 = (const u16*)d_in[10];
  const u16* b2 = (const u16*)d_in[11];
  (void)hipFuncSetAttribute((const void*)fused_kernel<3>, hipFuncAttributeMaxDynamicSharedMemorySize, LDS_TOTAL);
  fused_kernel<3><<<256, 512, LDS_TOTAL, stream>>>(x, lar, aim, cre, cim, ldt, dsk, w1, b1, w2, b2, (u16*)d_out, (char*)d_ws);
}
```

```cpp
#include <hip/hip_runtime.h>
#include <stdint.h>

typedef unsigned short u16;
typedef unsigned long long u64;
typedef __attribute__((ext_vector_type(4))) short s16x4;
typedef __attribute__((ext_vector_type(8))) short s16x8;
typedef __attribute__((ext_vector_type(8))) _Float16 f16x8;
typedef __attribute__((ext_vector_type(2))) _Float16 f16x2;
typedef __attribute__((ext_vector_type(2))) __bf16 bf16x2;
typedef __attribute__((ext_vector_type(2))) float f32x2;
typedef __attribute__((ext_vector_type(4))) float f32x4;
typedef __attribute__((ext_vector_type(16))) float f32x16;
typedef __attribute__((ext_vector_type(4))) unsigned u32x4;
typedef __attribute__((ext_vector_type(2))) unsigned u32x2;
#define LDSP __attribute__((address_space(3)))
#define GLBP __attribute__((address_space(1)))
#define DI __device__ __forceinline__

#define NLAUNCH 1

#define XPRIO 3
#define L_ 2048
#define D_ 512
#define N_ 128
#define F_ 1792

#define WS_H 0
#define WS_UR (8u << 20)
#define WS_S (10u << 20)
#define WS_STAT (12u << 20)
#define WS_FLAG (13u << 20)
#define WS_KAP (16u << 20)
#define FL_LN 128
#define FL_H 384
#define FL_S 512
#define FL_ST 768

#define STAMP_INIT(kid)
#define STAMP()
#define STAMP0()
#define STAMP_DECL
#define STAMP_FLUSH()
#define TK0_ARG
#define TK0_PASS

DI float bf2f(unsigned v) { return __uint_as_float(v << 16); }
DI float bflo(unsigned v) { return __uint_as_float(v << 16); }
DI float bfhi(unsigned v) { return __uint_as_float(v & 0xffff0000u); }
DI unsigned pkbf(float a, float b) { bf16x2 q = {(__bf16)a, (__bf16)b}; return __builtin_bit_cast(unsigned, q); }
DI float rbf(float a) { __bf16 q = (__bf16)a; return (float)q; }
DI unsigned pkh(float a, float b) { f32x2 v = {a, b}; f16x2 p = __builtin_convertvector(v, f16x2); return __builtin_bit_cast(unsigned, p); }
DI float dot2(unsigned a, unsigned b, float c) { return __builtin_amdgcn_fdot2_f32_bf16(__builtin_bit_cast(bf16x2, a), __builtin_bit_cast(bf16x2, b), c, false); }
template <class T> DI T sel(int c, T a, T b) { return c ? a : b; }
DI void dma16(const void* g, char* l) { __builtin_amdgcn_global_load_lds((const GLBP unsigned*)g, (LDSP unsigned*)l, 16, 0, 0); }
DI void dma16s(unsigned voff, const void* sbase, unsigned ldsaddr) {
  asm volatile("s_mov_b32 m0, %2\n\ts_nop 0\n\tglobal_load_lds_dwordx4 %0, %1" :: "v"(voff), "s"(sbase), "s"(ldsaddr) : "memory");
}
DI unsigned lds_base(char* lds) { return __builtin_amdgcn_readfirstlane((unsigned)(size_t)(LDSP char*)lds); }
DI s16x8 tr_frag(const char* p0, const char* p1) {
  s16x4 a = __builtin_amdgcn_ds_read_tr16_b64_v4i16((LDSP s16x4*)p0);
  s16x4 b = __builtin_amdgcn_ds_read_tr16_b64_v4i16((LDSP s16x4*)p1);
  return __builtin_shufflevector(a, b, 0, 1, 2, 3, 4, 5, 6, 7);
}
#define SCHED() __builtin_amdgcn_sched_barrier(0)
#define WAITV_BAR(n) do { SCHED(); asm volatile("s_waitcnt vmcnt(" #n ") lgkmcnt(0)\n\ts_barrier" ::: "memory"); SCHED(); } while (0)
#define LGKM_BAR() do { SCHED(); asm volatile("s_waitcnt lgkmcnt(0)\n\ts_barrier" ::: "memory"); SCHED(); } while (0)
#define RLX_AGENT __ATOMIC_RELAXED, __HIP_MEMORY_SCOPE_AGENT
#define RLX_WG __ATOMIC_RELAXED, __HIP_MEMORY_SCOPE_WORKGROUP

template <int CTRL, int RM> DI float dpp_add(float v) {
  return v + __builtin_bit_cast(float, __builtin_amdgcn_update_dpp(0, __builtin_bit_cast(int, v), CTRL, RM, 0xf, true));
}
DI float wave_sum63(float v) {
  v = dpp_add<0x111, 0xf>(v); v = dpp_add<0x112, 0xf>(v); v = dpp_add<0x114, 0xf>(v); v = dpp_add<0x118, 0xf>(v);
  v = dpp_add<0x142, 0xa>(v); v = dpp_add<0x143, 0xc>(v);
  return v;
}
DI float comb32(float a, float b) { auto r = __builtin_amdgcn_permlane32_swap(__float_as_uint(a), __float_as_uint(b), false, false); return __uint_as_float(r[0]) + __uint_as_float(r[1]); }
DI float comb16(float a, float b) { auto r = __builtin_amdgcn_permlane16_swap(__float_as_uint(a), __float_as_uint(b), false, false); return __uint_as_float(r[0]) + __uint_as_float(r[1]); }
DI float row_allsum(float v) { v = dpp_add<0x128, 0xf>(v); v = dpp_add<0x124, 0xf>(v); v = dpp_add<0x122, 0xf>(v); v = dpp_add<0x121, 0xf>(v); return v; }
DI float rdlane(float v, int l) { return __builtin_bit_cast(float, __builtin_amdgcn_readlane(__builtin_bit_cast(int, v), l)); }

DI void group_barrier(unsigned* cnt, unsigned target, int lane) {
  asm volatile("s_waitcnt lgkmcnt(0)" ::: "memory");
  if (lane == 0) __hip_atomic_fetch_add(cnt, 1u, RLX_WG);
  while (__hip_atomic_load(cnt, RLX_WG) < target) __builtin_amdgcn_s_sleep(1);
  asm volatile("" ::: "memory");
}
DI void poll_flags(const u64* p, u64 expect) {
  for (unsigned spin = 0; spin < (1u << 20); ++spin) {
    const u64 v = __hip_atomic_load(p, RLX_AGENT);
    if (__all(v == expect)) break;
    __builtin_amdgcn_s_sleep(2);
  }
  asm volatile("" ::: "memory");
}
DI void lds_set(unsigned* w, int lane) { if (lane == 0) __hip_atomic_store(w, 1u, RLX_WG); }
DI void lds_wait(unsigned* w) { while (__hip_atomic_load(w, RLX_WG) == 0u) __builtin_amdgcn_s_sleep(1); asm volatile("" ::: "memory"); }
DI int blk_of(int mb, int cb) { return (mb >> 2) + 8 * (((mb & 3) << 3) | cb); }

#define LDS_TOTAL 163840
#define L_STAT 162304
#define L_BIAS 163328
#define L_ACON 163584
#define L_KSC 163712
#define L_CNT 163744
#define KAPSZ 2576
#define L_KAP 141696
#define L_ASC 140800
#define K1_SLOT 32768
#define L_COLS 65536
#define L_TB 98304
#define L_KP 131584
#define KPSZ 4160
#define L_RING2 0
#define L_W 65536
#define WSZ 33024
#define L_U L_KAP
#define USZ 4608
#define L_RED 0
#define RED_RS 272
#define L_ST L_KAP
#define ST_RS 144
#define C_PUB 0
#define C_SP 1
#define C_UR 2
#define C_H 3
#define C_ST 4

struct cf { float x, y; };
DI cf cmul(cf a, cf b) { return cf{fmaf(a.x, b.x, -(a.y * b.y)), fmaf(a.x, b.y, a.y * b.x)}; }
DI unsigned pkc(cf a) { return pkh(a.x, a.y); }
DI unsigned pkcc(cf a) { return pkh(a.x, -a.y); }
DI cf zpow(float al, float om, float p) {
  const float m = __builtin_amdgcn_exp2f(al * p), ph = __builtin_amdgcn_fractf(om * p);
  return cf{m * __builtin_amdgcn_cosf(ph), m * __builtin_amdgcn_sinf(ph)};
}
DI unsigned cmulh(unsigned a, unsigned b) {
  const f16x2 z = __builtin_bit_cast(f16x2, a), w = __builtin_bit_cast(f16x2, b);
  const f16x2 t = z * (f16x2){w[0], w[0]};
  const f16x2 r = __builtin_elementwise_fma((f16x2){-z[1], z[0]}, (f16x2){w[1], w[1]}, t);
  return __builtin_bit_cast(unsigned, r);
}
DI u32x4 cmulh4(u32x4 a, u32x4 b) { return (u32x4){cmulh(a[0], b[0]), cmulh(a[1], b[1]), cmulh(a[2], b[2]), cmulh(a[3], b[3])}; }
#define TB_RS 144
#define TB_N 33
DI void kgen_tables(unsigned (&T)[TB_N], float& isc, unsigned raw, float ldtv, char* lds, int cw, int lane) {
  const int src = ((lane & 31) >> 1) * 4, sh = (lane & 1) ? 0 : 16;
  unsigned pv[4];
#pragma unroll
  for (int a = 0; a < 4; ++a) {
    pv[a] = ((unsigned)__builtin_amdgcn_ds_bpermute(src + 64 * a, (int)raw) << sh) & 0xffff0000u;
  }
  const float dt = rbf(__builtin_amdgcn_exp2f(ldtv * 1.4426950408889634f));
  const float ar = rbf(__builtin_amdgcn_exp2f(__uint_as_float(pv[0]) * 1.4426950408889634f));
  const float al = -(ar * dt) * 1.4426950408889634f;
  const float om = (__uint_as_float(pv[1]) * dt) * 0.15915494309189535f;
  const float cr = __uint_as_float(pv[2]) * dt, ci = __uint_as_float(pv[3]) * dt;
  const float sabs = rdlane(wave_sum63(fabsf(cr) + fabsf(ci)), 63);
  int ex = (int)((__float_as_uint(sabs) >> 23) & 255);
  ex = ex < 20 ? 20 : (ex > 230 ? 230 : ex);
  const float sc2 = __uint_as_float((unsigned)(268 - ex) << 23);
  isc = __uint_as_float((unsigned)(ex - 13) << 23);
  if (lane == 0) *(float*)(lds + L_KSC + cw * 4) = sabs;
  const cf z1 = zpow(al, om, 1.f), z8 = zpow(al, om, 8.f), z32 = zpow(al, om, 32.f), a1 = zpow(al, om, 128.f), a4 = zpow(al, om, 512.f);
  const unsigned one = 0x00003C00u;
  const cf a2 = cmul(a1, a1), a3 = cmul(a2, a1), a8 = cmul(a4, a4), a12 = cmul(a8, a4);
  T[0] = one; T[1] = pkc(a1); T[2] = pkc(a2); T[3] = pkc(a3);
  T[4] = pkc(a4); T[5] = pkc(cmul(a4, a1)); T[6] = pkc(cmul(a4, a2)); T[7] = pkc(cmul(a4, a3));
  T[8] = pkc(a8); T[9] = pkc(cmul(a8, a1)); T[10] = pkc(cmul(a8, a2)); T[11] = pkc(cmul(a8, a3));
  T[12] = pkc(a12); T[13] = pkc(cmul(a12, a1)); T[14] = pkc(cmul(a12, a2)); T[15] = pkc(cmul(a12, a3));
  T[16] = pkc(cmul(a8, a8));
  const cf e2 = cmul(z1, z1), e3 = cmul(e2, z1), e4 = cmul(e2, e2);
  T[17] = one; T[18] = pkc(z1); T[19] = pkc(e2); T[20] = pkc(e3); T[21] = pkc(e4);
  T[22] = pkc(cmul(e4, z1)); T[23] = pkc(cmul(e4, e2)); T[24] = pkc(cmul(e4, e3));
  const cf c0 = cf{cr * sc2, ci * sc2}, c2 = cmul(c0, cmul(z32, z32));
  T[25] = pkcc(c0); T[26] = pkcc(cmul(c0, z32)); T[27] = pkcc(c2); T[28] = pkcc(cmul(c2, z32));
  const cf z16 = cmul(z8, z8);
  T[29] = one; T[30] = pkcc(z8); T[31] = pkcc(z16); T[32] = pkcc(cmul(z16, z8));
}
#define KG_PARAMS f32x4& kX, f32x4& kY, f32x4& kZ, float isc
DI void kgen_store(KG_PARAMS, char* lds, int wid, int lane) {
  const int i16 = lane & 15, g = lane >> 4;
  float* kap = (float*)(lds + L_KAP + wid * KAPSZ);
  *(f32x4*)(kap + 128 * (g >> 1) + 4 * (g & 1) + 8 * i16) = kX * isc;
  if (g < 2) {
    *(f32x4*)(kap + 256 + 64 * g + 4 * i16) = kY * isc;
#pragma unroll
    for (int r = 0; r < 4; ++r) kap[512 + 16 * (4 * g + r) + i16] = kZ[r] * isc;
  } else {
    *(f32x2*)(kap + 384 + 64 * (g - 2) + 2 * i16) = (f32x2){kY[0] * isc, kY[1] * isc};
    *(f32x2*)(kap + 384 + 64 * (g - 2) + 32 + 2 * i16) = (f32x2){kY[2] * isc, kY[3] * isc};
    if (g == 2 && i16 == 0) kap[640] = kZ[0] * isc;
  }
}
DI const u16* prm_ptr(const u16* lar, const u16* aim, const u16* cre, const u16* cim, int d, int q4, int lane) {
  const int g = lane >> 4;
  const u16* p = g == 0 ? lar : (g == 1 ? aim : (g == 2 ? cre : cim));
  return p + d * N_ + 32 * q4 + 2 * (lane & 15);
}
DI void ld_u32(unsigned& v, const void* p) { asm volatile("global_load_dword %0, %1, off" : "=v"(v) : "v"(p) : "memory"); }
DI void sld_u32(unsigned& v, const void* p) { asm volatile("s_load_dword %0, %1, 0x0" : "=s"(v) : "s"(p) : "memory"); }
DI void sld_u64(u64& v, const void* p) { asm volatile("s_load_dwordx2 %0, %1, 0x0" : "=s"(v) : "s"(p) : "memory"); }
DI void ld_u64(u64& v, const void* p) { asm volatile("global_load_dwordx2 %0, %1, off sc1" : "=v"(v) : "v"(p) : "memory"); }
DI void ld_u64p(u64& v, const void* p) { asm volatile("global_load_dwordx2 %0, %1, off" : "=v"(v) : "v"(p) : "memory"); }
DI void gload16(u32x4& v, unsigned voff, const char* sbase) {
  asm volatile("global_load_dwordx4 %0, %1, %2 sc1" : "=v"(v) : "v"(voff), "s"(sbase) : "memory");
}
DI bool last_of(char* lds, int c, unsigned n, int lane) {
  unsigned old = 0;
  if (lane == 0) old = __hip_atomic_fetch_add((unsigned*)(lds + L_CNT) + c, 1u, RLX_WG);
  return (unsigned)__builtin_amdgcn_readfirstlane(old) == n - 1;
}

DI void kgen_all(unsigned raw, float ldtv, char* lds, int wid, int lane) {
  const int i16 = lane & 15, g = lane >> 4;
  unsigned T[TB_N];
  float isc;
  kgen_tables(T, isc, raw, ldtv, lds, wid, lane);
  char* tb = lds + L_TB + wid * (TB_N * TB_RS);
  if (lane < 32) {
#pragma unroll
    for (int i = 0; i < TB_N; ++i) *(unsigned*)(tb + i * TB_RS + lane * 4) = T[i];
  }
  const char* tr = tb + g * 16;
  const int rXa = (i16 >> 3) * TB_RS, rXe = (17 + (i16 & 7)) * TB_RS;
  const int rYa = (i16 < 8 ? 2 + (i16 >> 2) : 4 + ((i16 - 8) >> 1)) * TB_RS, rYe = (17 + (i16 < 8 ? 2 * (i16 & 3) : 4 * (i16 & 1))) * TB_RS;
  const int rZa = (i16 < 8 ? 8 + i16 : 16) * TB_RS;
  const int rB1 = (25 + (i16 >> 2)) * TB_RS, rB0 = (29 + (i16 & 3)) * TB_RS;
  f32x4 kX = {0.f, 0.f, 0.f, 0.f}, kY = kX, kZ = kX;
#pragma unroll
  for (int s = 0; s < 2; ++s) {
    const u32x4 fB = cmulh4(*(const u32x4*)(tr + rB1 + s * 64), *(const u32x4*)(tr + rB0 + s * 64));
    const u32x4 fX = cmulh4(*(const u32x4*)(tr + rXa + s * 64), *(const u32x4*)(tr + rXe + s * 64));
    const u32x4 fY = cmulh4(*(const u32x4*)(tr + rYa + s * 64), *(const u32x4*)(tr + rYe + s * 64));
    const u32x4 fZ = *(const u32x4*)(tr + rZa + s * 64);
    const f16x8 hb = __builtin_bit_cast(f16x8, fB);
    kX = __builtin_amdgcn_mfma_f32_16x16x32_f16(__builtin_bit_cast(f16x8, fX), hb, kX, 0, 0, 0);
    kY = __builtin_amdgcn_mfma_f32_16x16x32_f16(__builtin_bit_cast(f16x8, fY), hb, kY, 0, 0, 0);
    kZ = __builtin_amdgcn_mfma_f32_16x16x32_f16(__builtin_bit_cast(f16x8, fZ), hb, kZ, 0, 0, 0);
  }
  kgen_store(kX, kY, kZ, isc, lds, wid, lane);
}
DI int chan_ex(const char* lds, int ch) {
  const f32x4 ks = *(const f32x4*)(lds + L_KSC + ch * 16);
  const float sabs = (ks[0] + ks[1]) + (ks[2] + ks[3]);
  const int ex = (int)((__float_as_uint(sabs) >> 23) & 255);
  return ex < 20 ? 20 : (ex > 230 ? 230 : ex);
}
DI void kgen_expand(char* lds, int wid, int lane) {
  const int ch = wid >> 2, q4 = wid & 3;
  const float scale = __uint_as_float((unsigned)(267 - chan_ex(lds, ch)) << 23);
  const float* k0 = (const float*)(lds + L_KAP + (4 * ch) * KAPSZ);
  char* kp = lds + L_KP + ch * KPSZ;
#define KSUM4(T, i) (((*(const T*)(k0 + (i)) + *(const T*)(k0 + 644 + (i))) + (*(const T*)(k0 + 1288 + (i)) + *(const T*)(k0 + 1932 + (i)))) * scale)
  const int A = 64 * q4 + lane;
  u32x4 o;
  if (q4 == 0) {
    if (lane < 32) {
      const f32x4 a = KSUM4(f32x4, 8 * A), b = KSUM4(f32x4, 8 * A + 4);
      o = (u32x4){pkh(a[0], a[1]), pkh(a[2], a[3]), pkh(b[0], b[1]), pkh(b[2], b[3])};
    } else {
      const int i0 = 256 + 4 * (A - 32);
      const f32x4 a = KSUM4(f32x4, i0);
      const float e4 = KSUM4(float, i0 + 4);
      o = (u32x4){pkh(a[0], a[0]), pkh(a[1], a[2]), pkh(a[2], a[2]), pkh(a[3], e4)};
    }
    if (lane < 2) *(u32x4*)(kp + lane * 16) = (u32x4){0u, 0u, 0u, 0u};
  } else if (q4 == 1) {
    const int i0 = 384 + 2 * lane;
    const f32x2 a = KSUM4(f32x2, i0);
    const float e2 = KSUM4(float, i0 + 2);
    o = (u32x4){pkh(a[0], a[0]), pkh(a[0], a[1]), pkh(a[1], a[1]), pkh(e2, e2)};
  } else {
    const int i0 = 512 + (A - 128);
    const float e0 = KSUM4(float, i0), e1 = KSUM4(float, i0 + 1);
    const unsigned p00 = pkh(e0, e0);
    o = (u32x4){p00, p00, pkh((A & 1) ? e1 : e0, e1), pkh(e1, e1)};
  }
  *(u32x4*)(kp + 32 + A * 16) = o;
}

DI float gelu_f(float v) {
  const float c0 = -2.302208198f;
  const float c1 = -0.1029432397f;
  const float z = v * fmaf(v * v, c1, c0);
  return v * __builtin_amdgcn_rcpf(1.f + __builtin_amdgcn_exp2f(z));
}

#define K2_DMA(S) { _Pragma("unroll") for (int pp = 0; pp < 4; ++pp) dma16s(wvoff, wsrc + (size_t)((S) * 32 + pp * 8) * D_, ringa + ((S) & 1) * 4096 + pp * 1024); }
#define K2_SETUP() int lane2_ = lane; asm volatile("" : "+v"(lane2_)); \
  const unsigned wvoff = (lane2_ >> 3) * (D_ * 2) + (((lane2_ & 7) ^ (((lane2_ >> 4) & 1) << 2)) << 4); \
  const unsigned ringa = __builtin_amdgcn_readfirstlane(lds_base(lds) + L_RING2 + wid * 8192);
#define HMT 114688

DI void p1_gemm(const u16* __restrict__ x, const u16* __restrict__ w1, const u16* __restrict__ b1, const u16* prm, const u16* wsrc,
                u64& aOwn, unsigned sdt, char* __restrict__ ws, char* lds, int panel, int fs, int cb, int dodd, int wid, int lane TK0_ARG) {
  STAMP_DECL STAMP_INIT(0) STAMP0();
  const int wm = wid >> 2, wf = (wid >> 1) & 1, kh = wid & 1;
  const int r = lane & 31, h = lane >> 5;
  const int row0 = panel * 128, f0 = fs * 128;
  unsigned raw;
  u64 misc;
  ld_u32(raw, prm);
  ld_u64(misc, ws + WS_FLAG + (size_t)(lane >= 32 ? (int)blockIdx.x : ((wid & 1) ? blk_of(2 * panel + ((lane >> 3) & 1), lane & 7) : blk_of(lane, cb))) * 1024);
  if (wid == 0) asm volatile("s_mov_b32 m0, %2\n\ts_nop 0\n\tglobal_load_lds_dword %0, %1" :: "v"(lane * 4), "s"(b1 + f0), "s"(lds_base(lds) + L_BIAS) : "memory");
  const unsigned xv0 = (lane >> 3) * 1024 + (((lane & 7) ^ ((lane >> 4) & 3)) << 4);
  const unsigned xv1 = xv0 ^ 64;
  const unsigned wv = (lane >> 4) * (F_ * 2) + (((lane & 15) ^ (((lane >> 4) & 3) << 2)) << 4);
  const u16* xsb = x + (size_t)(row0 + 16 * wid) * D_;
  const u16* wsb = w1 + (size_t)(8 * wid) * F_ + f0;
  const unsigned la = lds_base(lds) + wid * 2048;
#define K1_D0(S) { dma16s(xv0, xsb + (S) * 64, la + ((S) % 4) * K1_SLOT); SCHED(); }
#define K1_D1(S) { dma16s(xv1, xsb + 8 * D_ + (S) * 64, la + ((S) % 4) * K1_SLOT + 1024); SCHED(); }
#define K1_D2(S) { dma16s(wv, wsb + (size_t)(S) * 64 * F_, la + ((S) % 4) * K1_SLOT + 16384); SCHED(); }
#define K1_D3(S) { dma16s(wv, wsb + (size_t)((S) * 64 + 4) * F_, la + ((S) % 4) * K1_SLOT + 16384 + 1024); SCHED(); }
#define K1_ISSUE(S) { K1_D0(S); K1_D1(S); K1_D2(S); K1_D3(S); }
  K1_ISSUE(0); K1_ISSUE(1); K1_ISSUE(2);
  if (wid == 0 && lane < 24) ((unsigned*)(lds + L_CNT))[lane] = 0u;
  STAMP();
  asm volatile("s_waitcnt vmcnt(13) lgkmcnt(0)" : "+v"(raw) :: "memory");
  STAMP();
  kgen_all(raw, dodd ? bfhi(sdt) : bflo(sdt), lds, wid, lane);
  const int xoff = (64 * wm + r) * 128 + (((4 * kh + h) ^ ((r >> 1) & 7)) << 4);
  const int q = (lane & 15) >> 2, p = lane & 3;
  const int woff0 = 16384 + (32 * kh + 8 * h + q) * 256 + (((2 * wf + kh) ^ q) << 6) + ((lane >> 4) & 1) * 32 + 8 * p;
  f32x16 acc[2][2];
#pragma unroll
  for (int i = 0; i < 16; ++i) { acc[0][0][i] = 0.f; acc[0][1][i] = 0.f; acc[1][0][i] = 0.f; acc[1][1][i] = 0.f; }
  float cs = 0.f;
  STAMP();
  s16x8 xf[2][2][2], wfr[2][2][2];
#define K1_M(T, s, mi, fi) { acc[mi][fi] = __builtin_amdgcn_mfma_f32_32x32x16_bf16(wfr[(T) & 1][s][fi], xf[(T) & 1][s][mi], acc[mi][fi], 0, 0, 0); SCHED(); }
#define K1_RX(T, s, mi) { xf[(T) & 1][s][mi] = *(const s16x8*)(lds + (xa_ ^ ((s) << 5)) + (mi) * 4096); SCHED(); }
#define K1_RW(T, s, fi) { wfr[(T) & 1][s][fi] = tr_frag(lds + (wa_ ^ ((fi) << 6)) + (s) * 4096, lds + (wa_ ^ ((fi) << 6)) + (s) * 4096 + 1024); SCHED(); }
#define K1_ADDR(T) int xa_ = xoff + ((T) % 4) * K1_SLOT, wa_ = woff0 + ((T) % 4) * K1_SLOT; asm volatile("" : "+v"(xa_), "+v"(wa_));
#define K1_CS1(T, fi) { const u32x4 wa = __builtin_bit_cast(u32x4, wfr[(T) & 1][0][fi]), wb = __builtin_bit_cast(u32x4, wfr[(T) & 1][1][fi]); \
    float c2_ = dot2(wb[0], 0x3F803F80u, 0.f); cs = dot2(wa[0], 0x3F803F80u, cs); \
    _Pragma("unroll") for (int i = 1; i < 4; ++i) { cs = dot2(wa[i], 0x3F803F80u, cs); c2_ = dot2(wb[i], 0x3F803F80u, c2_); } cs += c2_; asm volatile("" : "+v"(cs)); }
#define K1_CS(T) { if (wm == 0) K1_CS1(T, 0) else K1_CS1(T, 1) SCHED(); }
#define K1_READ(T) { K1_ADDR(T) K1_RX(T,0,0); K1_RW(T,0,0); K1_RW(T,0,1); K1_RX(T,0,1); K1_RX(T,1,0); K1_RW(T,1,0); K1_RW(T,1,1); K1_RX(T,1,1); }
#define K1_MMA(T) { K1_M(T,0,0,0); K1_M(T,0,0,1); K1_M(T,0,1,0); K1_M(T,0,1,1); K1_M(T,1,0,0); K1_M(T,1,0,1); K1_M(T,1,1,0); K1_M(T,1,1,1); K1_CS(T); }
#define K1_STAGE(T, DM0, DM1, DM2, DM3) { K1_ADDR(T) \
    K1_M((T)-1,0,0,0); K1_RX(T,0,0); K1_RW(T,0,0); K1_M((T)-1,0,0,1); K1_RW(T,0,1); K1_RX(T,0,1); \
    K1_M((T)-1,0,1,0); K1_RX(T,1,0); K1_RW(T,1,0); K1_M((T)-1,0,1,1); K1_RW(T,1,1); K1_RX(T,1,1); \
    K1_M((T)-1,1,0,0); DM0; K1_M((T)-1,1,0,1); DM1; K1_M((T)-1,1,1,0); DM2; K1_M((T)-1,1,1,1); DM3; K1_CS((T)-1); }
#define K1_STAGE_D(T, S) K1_STAGE(T, K1_D0(S), K1_D1(S), K1_D2(S), K1_D3(S))
  SCHED();
  asm volatile("s_waitcnt vmcnt(8) lgkmcnt(0)\n\ts_barrier" : "+v"(misc) :: "memory");
  SCHED(); STAMP();
  K1_ISSUE(3);
  K1_READ(0);
  aOwn = (u64)(unsigned)__builtin_amdgcn_readlane((int)(unsigned)misc, 32) | ((u64)(unsigned)__builtin_amdgcn_readlane((int)(unsigned)(misc >> 32), 32) << 32);
  if (wid == 1 && lane < 16) *(u64*)(lds + L_ACON + lane * 8) = misc;
  if (wid == 0 && lane < 32) *(u64*)(lds + L_ASC + lane * 8) = misc;
  kgen_expand(lds, wid, lane);
  WAITV_BAR(8); K1_STAGE_D(1, 4);
  WAITV_BAR(8); K1_STAGE_D(2, 5);
  WAITV_BAR(8); K1_STAGE_D(3, 6);
  WAITV_BAR(8); STAMP(); K1_STAGE_D(4, 7);
  if (wid == 2) {
    const u64* stfl = (const u64*)(ws + WS_FLAG + (size_t)blockIdx.x * 1024 + FL_ST) + (lane & 1);
    const char* stp = ws + WS_STAT + (size_t)panel * 1024;
    u64 fv; u32x4 stv;
    WAITV_BAR(8); K1_STAGE(5, , , , );
    WAITV_BAR(4); K1_STAGE(6, ld_u64(fv, stfl), , , );
    K1_MMA(6);
    SCHED(); asm volatile("s_waitcnt vmcnt(0) lgkmcnt(0)\n\ts_barrier" : "+v"(fv) :: "memory"); SCHED();
    if (!__all(fv == aOwn + 1)) poll_flags(stfl, aOwn + 1);
    gload16(stv, lane * 16, stp);
    K1_READ(7); K1_MMA(7);
    asm volatile("s_waitcnt vmcnt(0)" : "+v"(stv) :: "memory");
    *(u32x4*)(lds + L_STAT + lane * 16) = stv;
  } else {
    WAITV_BAR(8); K1_STAGE(5, , , , );
    WAITV_BAR(4); K1_STAGE(6, , , , );
    K1_MMA(6);
    WAITV_BAR(0);
    K1_READ(7); K1_MMA(7);
  }
  STAMP();

  float* colS = (float*)(lds + L_COLS);
  {
    char* dst = lds + wid * 8192 + lane * 16;
#pragma unroll
    for (int mi = 0; mi < 2; ++mi)
#pragma unroll
      for (int rq = 0; rq < 4; ++rq)
        *(f32x4*)(dst + (mi * 4 + rq) * 1024) = (f32x4){acc[mi][1][4 * rq], acc[mi][1][4 * rq + 1], acc[mi][1][4 * rq + 2], acc[mi][1][4 * rq + 3]};
    const float cc = comb32(cs, cs);
    if (h == 0) colS[kh * 128 + 64 * wf + 32 * (wm ^ kh) + r] = cc;
  }
  LGKM_BAR();
  STAMP();
  f32x4 pa[2][4], cq[4];
  f32x2 st[2];
  u32x2 bq[4];
  {
    const char* src = lds + (wid ^ 1) * 8192 + lane * 16;
    const int fl = 64 * wf + 32 * kh + 4 * h;
#pragma unroll
    for (int rq = 0; rq < 4; ++rq) {
      cq[rq] = *(const f32x4*)(colS + fl + 8 * rq) + *(const f32x4*)(colS + 128 + fl + 8 * rq);
      bq[rq] = *(const u32x2*)(lds + L_BIAS + (fl + 8 * rq) * 2);
      pa[0][rq] = *(const f32x4*)(src + rq * 1024); pa[1][rq] = *(const f32x4*)(src + (4 + rq) * 1024);
    }
    st[0] = *(const f32x2*)(lds + L_STAT + (64 * wm + r) * 8); st[1] = *(const f32x2*)(lds + L_STAT + (64 * wm + 32 + r) * 8);
  }
  LGKM_BAR();
  {
    K2_SETUP();
    K2_DMA(0); K2_DMA(1);
    SCHED();
  }
  {
    const __amdgpu_buffer_rsrc_t hr = __builtin_amdgcn_make_buffer_rsrc(ws + WS_H, 0, L_ * F_ * 2, 0x00020000);
    const unsigned hoff = (unsigned)(panel * 4 + 2 * wm) * HMT + (8 * fs + 4 * wf + 2 * kh) * 1024 + lane * 16;
#pragma unroll
    for (int mi = 0; mi < 2; ++mi) {
      const float mr = st[mi][0], rinv = st[mi][1];
#pragma unroll
      for (int s2 = 0; s2 < 2; ++s2) {
        u32x4 pk;
#pragma unroll
        for (int qq = 0; qq < 2; ++qq) {
          const int rq = 2 * s2 + qq;
          const float b0 = bflo(bq[rq][0]), b1_ = bfhi(bq[rq][0]), b2_ = bflo(bq[rq][1]), b3 = bfhi(bq[rq][1]);
          const float o0 = gelu_f(fmaf(acc[mi][0][4 * rq] + pa[mi][rq][0], rinv, fmaf(mr, cq[rq][0], b0)));
          const float o1 = gelu_f(fmaf(acc[mi][0][4 * rq + 1] + pa[mi][rq][1], rinv, fmaf(mr, cq[rq][1], b1_)));
          const float o2 = gelu_f(fmaf(acc[mi][0][4 * rq + 2] + pa[mi][rq][2], rinv, fmaf(mr, cq[rq][2], b2_)));
          const float o3 = gelu_f(fmaf(acc[mi][0][4 * rq + 3] + pa[mi][rq][3], rinv, fmaf(mr, cq[rq][3], b3)));
          pk[2 * qq] = pkbf(o0, o1); pk[2 * qq + 1] = pkbf(o2, o3);
        }
        __builtin_amdgcn_raw_buffer_store_b128(pk, hr, hoff + mi * HMT + s2 * 1024, 0, 16);
      }
    }
  }
  STAMP();
  asm volatile("s_waitcnt vmcnt(0)" ::: "memory");
  if (last_of(lds, C_PUB, 8, lane) && lane < 16) {
    const u64 ac = *(const u64*)(lds + L_ACON + lane * 8);
    int l2 = lane; asm volatile("" : "+v"(l2));
    __hip_atomic_store((u64*)(ws + WS_FLAG + (size_t)blk_of(2 * panel + ((l2 >> 3) & 1), l2 & 7) * 1024 + FL_H) + fs, ac + 1, RLX_AGENT);
  }
  STAMP(); STAMP_FLUSH();
}

#define LN_RS 1040
DI void p1_aux(const u16* __restrict__ x, const u16* prm, const u16* wsrc, u64& aOwn, unsigned sdt, char* __restrict__ ws, char* lds, int idx,
               int cb, int dodd, int wid, int lane TK0_ARG) {
  STAMP_DECL STAMP_INIT(0) STAMP0();
  const int t0 = idx * 64;
  u32x4 v[8];
#pragma unroll
  for (int i = 0; i < 8; ++i) v[i] = *(const u32x4*)(x + (size_t)(t0 + 8 * wid + i) * D_ + lane * 8);
  const u64 aCon = __hip_atomic_load((const u64*)(ws + WS_FLAG + (size_t)((wid * 64 + lane) & 255) * 1024), RLX_AGENT);
  const int sfs = lane < 14 ? lane : 0;
  const int sblk = 2 * (idx >> 3) + (sfs >= 7 ? 1 : 0) + 8 * (((idx >> 1) & 3) * 7 + (sfs >= 7 ? sfs - 7 : sfs));
  const u64 aSt = __hip_atomic_load((const u64*)(ws + WS_FLAG + (size_t)sblk * 1024), RLX_AGENT);
  const u64 aS = __hip_atomic_load((const u64*)(ws + WS_FLAG + (size_t)(lane >= 32 ? (int)blockIdx.x : blk_of(lane, cb)) * 1024), RLX_AGENT);
  const unsigned raw = *(const unsigned*)prm;
  if (wid == 0 && lane < 24) ((unsigned*)(lds + L_CNT))[lane] = 0u;
  SCHED();
  asm volatile("s_waitcnt lgkmcnt(0)\n\ts_barrier" ::: "memory");
  SCHED();
  float sA[8], sB[8];
#pragma unroll
  for (int i = 0; i < 8; ++i) {
    float a = dot2(v[i][0], 0x3F803F80u, 0.f), b = dot2(v[i][0], v[i][0], 0.f);
#pragma unroll
    for (int c = 1; c < 4; ++c) { a = dot2(v[i][c], 0x3F803F80u, a); b = dot2(v[i][c], v[i][c], b); }
    sA[i] = a; sB[i] = b;
  }
#pragma unroll
  for (int i = 0; i < 4; ++i) { sA[i] = comb32(sA[i], sA[i + 4]); sB[i] = comb32(sB[i], sB[i + 4]); }
#pragma unroll
  for (int i = 0; i < 2; ++i) { sA[i] = row_allsum(comb16(sA[i], sA[i + 2])); sB[i] = row_allsum(comb16(sB[i], sB[i + 2])); }
  const float mv = ((lane & 1) ? sA[1] : sA[0]) * (1.f / 512.f);
  const float var = fmaxf(fmaf(-mv, mv, ((lane & 1) ? sB[1] : sB[0]) * (1.f / 512.f)), 0.f);
  const float mbv = rbf(mv);
  const float sd = rbf(sqrtf(rbf(rbf(var) + 1e-5f)));
  const float rv = 1.f / sd;
  if ((lane & 14) == 0) {
    const u64 pk = (u64)__float_as_uint(-mbv * rv) | ((u64)__float_as_uint(rv) << 32);
    __hip_atomic_store((u64*)(ws + WS_STAT) + t0 + 8 * wid + (lane & 1) + 2 * (lane >> 4), pk, RLX_AGENT);
  }
  if (wid == 0 && lane < 32) *(u64*)(lds + L_ASC + lane * 8) = aS;
  aOwn = (u64)(unsigned)__builtin_amdgcn_readlane((int)(unsigned)aS, 32) | ((u64)(unsigned)__builtin_amdgcn_readlane((int)(unsigned)(aS >> 32), 32) << 32);
#define LN_ROW(i) { const float rinv = rdlane(rv, ((i) & 1) + 16 * ((i) >> 1)), mb = rdlane(mbv, ((i) & 1) + 16 * ((i) >> 1)); u32x4 pk; \
    _Pragma("unroll") for (int c = 0; c < 4; ++c) { const unsigned a = pkbf(bflo(v[i][c]) - mb, bfhi(v[i][c]) - mb); \
      const unsigned b = pkbf(bflo(a) * rinv, bfhi(a) * rinv); pk[c] = pkh(bflo(b), bfhi(b)); } \
    *(u32x4*)(lds + (63 - (8 * wid + (i))) * LN_RS + lane * 16) = pk; }
  LN_ROW(0); LN_ROW(1);
  SCHED();
  asm volatile("s_waitcnt vmcnt(0)" ::: "memory");
  if (last_of(lds, C_PUB, 8, lane) && lane < 14) __hip_atomic_store((u64*)(ws + WS_FLAG + (size_t)sblk * 1024 + FL_ST) + (idx & 1), aSt + 1, RLX_AGENT);
  STAMP();
  LN_ROW(2); LN_ROW(3); LN_ROW(4); LN_ROW(5); LN_ROW(6); LN_ROW(7);
  STAMP();
  LGKM_BAR();
  char* t2 = lds + 64 * LN_RS + wid * 8192;
  const int g = lane >> 4, q = (lane & 15) >> 2, p = lane & 3, dl = lane & 15;
  s16x8 f[8];
#pragma unroll
  for (int i = 0; i < 8; ++i) {
    const char* a = lds + (8 * (4 * (i & 1) + g) + q) * LN_RS + (64 * wid + 16 * (i >> 1) + 4 * p) * 2;
    f[i] = tr_frag(a, a + 4 * LN_RS);
  }
  SCHED();
#pragma unroll
  for (int i = 0; i < 8; ++i) {
    const int dd2 = 16 * (i >> 1) + dl, rc = 4 * (i & 1) + g;
    *(s16x8*)(t2 + dd2 * 128 + ((rc ^ (dd2 & 7)) << 4)) = f[i];
  }
  SCHED();
  const __amdgpu_buffer_rsrc_t ur = __builtin_amdgcn_make_buffer_rsrc(ws + WS_UR, 0, L_ * D_ * 2, 0x00020000);
  const int x0 = L_ - 64 - t0;
  u32x4 o[8];
#pragma unroll
  for (int i = 0; i < 8; ++i) {
    const int dd2 = 8 * i + (lane >> 3), c = lane & 7;
    o[i] = *(const u32x4*)(t2 + dd2 * 128 + ((c ^ (dd2 & 7)) << 4));
  }
  SCHED();
#pragma unroll
  for (int i = 0; i < 8; ++i)
    __builtin_amdgcn_raw_buffer_store_b128(o[i], ur, (unsigned)(64 * wid + 8 * i + (lane >> 3)) * 4096 + x0 * 2 + (lane & 7) * 16, 0, 16);
  STAMP();
  WAITV_BAR(0);
  if (wid < 4) __hip_atomic_store((u64*)(ws + WS_FLAG + (size_t)(wid * 64 + lane) * 1024 + FL_LN) + idx, aCon + 1, RLX_AGENT);
  STAMP();
  {
    K2_SETUP();
    K2_DMA(0); K2_DMA(1);
    SCHED();
  }
  kgen_all(raw, dodd ? bfhi(sdt) : bflo(sdt), lds, wid, lane);
  LGKM_BAR();
  kgen_expand(lds, wid, lane);
  WAITV_BAR(0);
  STAMP(); STAMP_FLUSH();
}

DI void phase2(const u16* __restrict__ x, const u16* wsrc, u64 aOwn, float dskip, const u16* __restrict__ b2, u16* __restrict__ out,
               char* __restrict__ ws, char* lds, int mb, int cb, int wid, int lane TK0_ARG) {
  STAMP_DECL STAMP_INIT(1) STAMP0();
  const int ch = wid >> 2, q4 = wid & 3;
  const int d = 64 * cb + 2 * mb + ch;
  const int i16 = lane & 15, g = lane >> 4, r = lane & 31, h = lane >> 5;
  const int tid = wid * 64 + lane;
  unsigned* cnt = (unsigned*)(lds + L_CNT);
  const char* flg = ws + WS_FLAG + (size_t)blockIdx.x * 1024;
  K2_SETUP();
  const char* ring = lds + L_RING2 + wid * 8192;
  const int toff0 = (4 * h + (i16 >> 2)) * 128 + ((i16 >> 3) << 6) + (g & 1) * 32 + 8 * (lane & 3), toff1 = toff0 ^ 64;
  s16x8 wp[7][4];
#define PARK(S) { const char* sl_ = ring + ((S) & 1) * 4096; \
    wp[S][0] = tr_frag(sl_ + toff0, sl_ + toff0 + 1024); wp[S][1] = tr_frag(sl_ + toff1, sl_ + toff1 + 1024); \
    wp[S][2] = tr_frag(sl_ + toff0 + 2048, sl_ + toff0 + 3072); wp[S][3] = tr_frag(sl_ + toff1 + 2048, sl_ + toff1 + 3072); }
#define PARK_PIN(S) asm volatile("s_waitcnt lgkmcnt(0)" : "+v"(wp[S][0]), "+v"(wp[S][1]), "+v"(wp[S][2]), "+v"(wp[S][3]) :: "memory")
  STAMP();
  PARK(0); PARK(1); PARK_PIN(0); PARK_PIN(1);
  K2_DMA(2); K2_DMA(3);
  if (wid == 0) { poll_flags((const u64*)(flg + FL_LN) + (lane & 31), aOwn + 1); lds_set(cnt + C_UR, lane); }
  const float iscale = __uint_as_float((unsigned)(chan_ex(lds, ch) - 13) << 23);
  char* wl = lds + L_W + ch * WSZ;
  const char* kp = lds + L_KP + ch * KPSZ;
#pragma unroll
  for (int rnd = 0; rnd < 2; ++rnd) {
    const int a = rnd == 0 ? 64 * q4 + lane : 256;
    if (rnd == 1 && !(q4 == 3 && lane == 0)) break;
    const u32x4 c0 = *(const u32x4*)(kp + a * 16), c1 = *(const u32x4*)(kp + a * 16 + 16);
    const unsigned e[8] = {c0[0], c0[1], c0[2], c0[3], c1[0], c1[1], c1[2], c1[3]};
    unsigned o[7];
#pragma unroll
    for (int i = 0; i < 7; ++i) o[i] = __builtin_amdgcn_alignbit(e[i + 1], e[i], 16);
    const int m = (a & 1) | (((a >> 2) & 1) << 1) | (((a >> 1) & 1) << 2);
    char* base = wl + a * 128;
#pragma unroll
    for (int c = 0; c < 8; ++c) {
      u32x4 wv;
      if (c & 1) { const int s = (c + 1) >> 1; wv = (u32x4){e[s], e[s + 1], e[s + 2], e[s + 3]}; }
      else { const int s = c >> 1; wv = (u32x4){o[s], o[s + 1], o[s + 2], o[s + 3]}; }
      *(u32x4*)(base + ((c ^ m) << 4)) = wv;
    }
  }
  STAMP();
  lds_wait(cnt + C_UR);
  char* ul = lds + L_U + ch * USZ;
  u32x4 uv;
  gload16(uv, lane * 16, ws + WS_UR + (size_t)d * 4096 + q4 * 1024);
  asm volatile("s_waitcnt vmcnt(1)" ::: "memory");
  PARK(2); PARK(3); PARK_PIN(2); PARK_PIN(3);
  K2_DMA(4); K2_DMA(5);
  asm volatile("s_waitcnt vmcnt(8)" : "+v"(uv) :: "memory");
  *(u32x4*)(ul + q4 * 1024 + lane * 16) = uv;
  if (q4 == 0 && lane < 32) *(u32x4*)(ul + 4096 + lane * 16) = (u32x4){0u, 0u, 0u, 0u};
  LGKM_BAR();
  STAMP();
  f32x4 ca[8];
#pragma unroll
  for (int i = 0; i < 8; ++i) ca[i] = (f32x4){0.f, 0.f, 0.f, 0.f};
  {
    const int c = i16 + 8 * g;
    int fo[2];
#pragma unroll
    for (int par = 0; par < 2; ++par) {
      const int w = 32 * par + c;
      const int m = ((w >> 3) & 1) | (((w >> 5) & 1) << 1) | (((w >> 4) & 1) << 2);
      fo[par] = (w ^ m) << 4;
    }
    const char* fbase = wl + q4 * 1024;
    const char* gbase = ul + (2032 - 16 * i16 + 8 * g + 64 * q4) * 2 - 7 * 512;
#define CF(s, e) (*(const f16x8*)(fbase + fo[s] + (e) * 4096))
#define CG(s, mu) (*(const f16x8*)(gbase + (s) * 64 + (7 - (mu)) * 512))
    f16x8 F[2][8], G[2];
#pragma unroll
    for (int e = 0; e < 8; ++e) F[0][e] = CF(0, e);
    G[0] = CG(0, 0);
#pragma unroll
    for (int s = 0; s < 2; ++s) {
#pragma unroll
      for (int mu = 0; mu < 8; ++mu) {
        const int it = s * 8 + mu;
        SCHED();
        if (it + 1 < 16) G[(it + 1) & 1] = CG((it + 1) >> 3, (it + 1) & 7);
        if (s == 0) F[1][mu] = CF(1, mu);
        SCHED();
#pragma unroll
        for (int e = 0; e + mu < 8; ++e) ca[e + mu] = __builtin_amdgcn_mfma_f32_16x16x32_f16(F[s][e], G[it & 1], ca[e + mu], 0, 0, 0);
      }
    }
  }
  STAMP();
  asm volatile("s_waitcnt vmcnt(0)" ::: "memory");
  PARK(4); PARK(5); PARK_PIN(4); PARK_PIN(5);
  K2_DMA(6);
  LGKM_BAR();
  {
    char* dst = wl + q4 * 8192 + lane * 16;
#pragma unroll
    for (int i = 0; i < 8; ++i) *(f32x4*)(dst + i * 1024) = ca[i];
  }
  LGKM_BAR();
  STAMP();
  {
    const char* src = wl + (2 * q4) * 1024 + lane * 16;
    char* sdst = ws + WS_S + (size_t)d * 4096;
#pragma unroll
    for (int i = 0; i < 2; ++i) {
      const f32x4 yv = ((*(const f32x4*)(src + i * 1024) + *(const f32x4*)(src + 8192 + i * 1024)) +
                        (*(const f32x4*)(src + 16384 + i * 1024) + *(const f32x4*)(src + 24576 + i * 1024))) * iscale;
      const int t = 256 * (2 * q4 + i) + 16 * i16 + 4 * g;
      const u32x2 uu = *(const u32x2*)(ul + (2044 - t) * 2);
      const unsigned uu0 = uu[0], uu1 = uu[1];
      const f16x2 ua = __builtin_bit_cast(f16x2, uu0), ub = __builtin_bit_cast(f16x2, uu1);
      const unsigned y01 = pkbf(yv[0], yv[1]), y23 = pkbf(yv[2], yv[3]);
      const unsigned k01 = pkbf((float)ub[1] * dskip, (float)ub[0] * dskip), k23 = pkbf((float)ua[1] * dskip, (float)ua[0] * dskip);
      const u64 pk = (u64)pkbf(bflo(y01) + bflo(k01), bfhi(y01) + bfhi(k01)) | ((u64)pkbf(bflo(y23) + bflo(k23), bfhi(y23) + bfhi(k23)) << 32);
      __hip_atomic_store((u64*)(sdst + t * 2), pk, RLX_AGENT);
    }
  }
  SCHED();
  if (wid == 1) { poll_flags((const u64*)(flg + FL_H) + (lane < 14 ? lane : 0), aOwn + 1); lds_set(cnt + C_H, lane); }
  asm volatile("s_waitcnt vmcnt(0)" ::: "memory");
  if (last_of(lds, C_SP, 8, lane) && lane < 32) {
    const u64 ac = *(const u64*)(lds + L_ASC + lane * 8);
    __hip_atomic_store((u64*)(ws + WS_FLAG + (size_t)blk_of(lane, cb) * 1024 + FL_S) + mb, ac + 1, RLX_AGENT);
  }
  PARK(6); PARK_PIN(6);
  STAMP();
  const char* hbase = ws + WS_H + (size_t)(2 * mb) * HMT + 14 * wid * 1024;
  const unsigned hoff0 = lane * 16, hoff1 = lane * 16 + HMT;
  u32x4 hb[7][4];
  f32x16 acc[2][2];
#pragma unroll
  for (int i = 0; i < 16; ++i) { acc[0][0][i] = 0.f; acc[0][1][i] = 0.f; acc[1][0][i] = 0.f; acc[1][1][i] = 0.f; }
  u32x4 xv, bv, sv;
  u64 sf;
  asm volatile("global_load_dwordx4 %0, %1, off" : "=v"(xv) : "v"(x + (size_t)(64 * mb + (tid >> 3)) * D_ + 64 * cb + 8 * (tid & 7)) : "memory");
  asm volatile("global_load_dwordx4 %0, %1, off" : "=v"(bv) : "v"(b2 + 64 * cb + 8 * (tid & 7)) : "memory");
  lds_wait(cnt + C_H);
  STAMP();
#define K2_HLD(S) { gload16(hb[S][0], hoff0, hbase + (S) * 2048); gload16(hb[S][1], hoff1, hbase + (S) * 2048); \
                    gload16(hb[S][2], hoff0, hbase + (S) * 2048 + 1024); gload16(hb[S][3], hoff1, hbase + (S) * 2048 + 1024); }
#define K2_STEP(S, W) { \
    asm volatile("s_waitcnt vmcnt(" #W ")" : "+v"(hb[S][0]), "+v"(hb[S][1]), "+v"(hb[S][2]), "+v"(hb[S][3]) :: "memory"); \
    _Pragma("unroll") for (int s2 = 0; s2 < 2; ++s2) { \
      const s16x8 h0 = __builtin_bit_cast(s16x8, hb[S][2 * s2]), h1 = __builtin_bit_cast(s16x8, hb[S][2 * s2 + 1]); \
      acc[0][0] = __builtin_amdgcn_mfma_f32_32x32x16_bf16(wp[S][2 * s2], h0, acc[0][0], 0, 0, 0); \
      acc[1][0] = __builtin_amdgcn_mfma_f32_32x32x16_bf16(wp[S][2 * s2], h1, acc[1][0], 0, 0, 0); \
      acc[0][1] = __builtin_amdgcn_mfma_f32_32x32x16_bf16(wp[S][2 * s2 + 1], h0, acc[0][1], 0, 0, 0); \
      acc[1][1] = __builtin_amdgcn_mfma_f32_32x32x16_bf16(wp[S][2 * s2 + 1], h1, acc[1][1], 0, 0, 0); } \
    SCHED(); }
  const u64* sfl = (const u64*)(flg + FL_S) + (lane & 31);
  const char* sp = ws + WS_S + (size_t)(64 * cb + 8 * wid) * 4096 + 64 * mb * 2;
  const unsigned svo = (lane >> 3) * 4096 + (lane & 7) * 16;
  K2_HLD(0); K2_HLD(1); K2_HLD(2);
  K2_STEP(0, 8); STAMP(); K2_HLD(3); K2_HLD(4);
  K2_STEP(1, 12); K2_HLD(5); K2_HLD(6);
  ld_u64(sf, sfl);
  K2_STEP(2, 17);
  K2_STEP(3, 13);
  K2_STEP(4, 9);
  K2_STEP(5, 5);
  K2_STEP(6, 1);
  STAMP();
  asm volatile("s_waitcnt vmcnt(0)" : "+v"(sf), "+v"(xv), "+v"(bv) :: "memory");
  if (!__all(sf == aOwn + 1)) poll_flags(sfl, aOwn + 1);
  gload16(sv, svo, sp);
  LGKM_BAR();
  {
    char* dst = lds + L_RED + (wid * 64 + r) * RED_RS + h * 16;
#pragma unroll
    for (int mi = 0; mi < 2; ++mi)
#pragma unroll
      for (int di = 0; di < 2; ++di)
#pragma unroll
        for (int rq = 0; rq < 4; ++rq)
          *(f32x4*)(dst + mi * 32 * RED_RS + di * 128 + rq * 32) = (f32x4){acc[mi][di][4 * rq], acc[mi][di][4 * rq + 1], acc[mi][di][4 * rq + 2], acc[mi][di][4 * rq + 3]};
  }
  SCHED();
  asm volatile("s_waitcnt vmcnt(0)" : "+v"(sv) :: "memory");
  {
    char* dst = lds + L_ST + (8 * (lane & 7)) * ST_RS + (8 * wid + (lane >> 3)) * 2;
#pragma unroll
    for (int c = 0; c < 4; ++c) { *(u16*)(dst + (2 * c) * ST_RS) = (u16)sv[c]; *(u16*)(dst + (2 * c + 1) * ST_RS) = (u16)(sv[c] >> 16); }
  }
  LGKM_BAR();
  STAMP();
  {
    const int t = tid >> 3, dq = tid & 7;
    const char* src = lds + L_RED + t * RED_RS + dq * 32;
    f32x4 a = *(const f32x4*)src, b = *(const f32x4*)(src + 16);
#pragma unroll
    for (int k = 1; k < 8; ++k) { a += *(const f32x4*)(src + k * 64 * RED_RS); b += *(const f32x4*)(src + k * 64 * RED_RS + 16); }
    const u32x4 sq = *(const u32x4*)(lds + L_ST + t * ST_RS + dq * 16);
    u32x4 o;
#pragma unroll
    for (int c = 0; c < 4; ++c) {
      const float v0 = (c < 2 ? a[2 * c] : b[2 * c - 4]) + bflo(bv[c]), v1 = (c < 2 ? a[2 * c + 1] : b[2 * c - 3]) + bfhi(bv[c]);
      const float g0 = v0 * __builtin_amdgcn_rcpf(1.f + __builtin_amdgcn_exp2f(-1.4426950408889634f * v0));
      const float g1 = v1 * __builtin_amdgcn_rcpf(1.f + __builtin_amdgcn_exp2f(-1.4426950408889634f * v1));
      const unsigned pv = pkbf(bflo(sq[c]) * g0, bfhi(sq[c]) * g1);
      o[c] = pkbf(bflo(xv[c]) + bflo(pv), bfhi(xv[c]) + bfhi(pv));
    }
    *(u32x4*)(out + (size_t)(64 * mb + t) * D_ + 64 * cb + 8 * dq) = o;
  }
  if (tid == 0) __hip_atomic_store((u64*)(ws + WS_FLAG + (size_t)blockIdx.x * 1024), aOwn + 1, RLX_AGENT);
  STAMP(); STAMP_FLUSH();
}

template <int PH>
__global__ __launch_bounds__(512, 2) void fused_kernel(const u16* __restrict__ x, const u16* __restrict__ lar, const u16* __restrict__ aim,
                                                       const u16* __restrict__ cre, const u16* __restrict__ cim, const u16* __restrict__ ldt,
                                                       const u16* __restrict__ dsk, const u16* __restrict__ w1, const u16* __restrict__ b1,
                                                       const u16* __restrict__ w2, const u16* __restrict__ b2, u16* __restrict__ out,
                                                       char* __restrict__ ws) {
  extern __shared__ __attribute__((aligned(16))) char lds[];
  const int lane = threadIdx.x & 63;
  const int wid = __builtin_amdgcn_readfirstlane(threadIdx.x >> 6);
  const int b = blockIdx.x, xcd = b & 7, j = b >> 3;
  const int mb = 4 * xcd + (j >> 3), cb = j & 7;
  const int d = 64 * cb + 2 * mb + (wid >> 2);
  u64 aOwn;
  const unsigned sdt = *(const __attribute__((address_space(4))) unsigned*)((const char*)ldt + ((d * 2) & ~3));
  const unsigned sds = *(const __attribute__((address_space(4))) unsigned*)((const char*)dsk + ((d * 2) & ~3));
  const u16* prm = prm_ptr(lar, aim, cre, cim, d, wid & 3, lane);
  const u16* wsrc = w2 + (size_t)(7 * wid * 32) * D_ + 64 * cb;
  if (j < 28) p1_gemm(x, w1, b1, prm, wsrc, aOwn, sdt, ws, lds, 4 * (xcd >> 1) + j / 7, 7 * (xcd & 1) + j % 7, cb, d & 1, wid, lane TK0_PASS);
  else p1_aux(x, prm, wsrc, aOwn, sdt, ws, lds, xcd * 4 + (j - 28), cb, d & 1, wid, lane TK0_PASS);
  phase2(x, wsrc, aOwn, (d & 1) ? bfhi(sds) : bflo(sds), b2, out, ws, lds, mb, cb, wid, lane TK0_PASS);
}

extern "C" void kernel_launch(void* const* d_in, const int* in_sizes, int n_in, void* d_out, int out_size, void* d_ws, size_t ws_size,
                              hipStream_t stream) {
  const u16* x = (const u16*)d_in[0];
  const u16* lar = (const u16*)d_in[1];
  const u16* aim = (const u16*)d_in[2];
  const u16* cre = (const u16*)d_in[3];
  const u16* cim = (const u16*)d_in[4];
  const u16* ldt = (const u16*)d_in[5];
  const u16* dsk = (const u16*)d_in[6];
  const u16* w1 = (const u16*)d_in[8];
  const u16* b1 = (const u16*)d_in[9];
  const u16* w2 = (const u16*)d_in[10];
  const u16* b2 = (const u16*)d_in[11];
  (void)hipFuncSetAttribute((const void*)fused_kernel<3>, hipFuncAttributeMaxDynamicSharedMemorySize, LDS_TOTAL);
  fused_kernel<3><<<256, 512, LDS_TOTAL, stream>>>(x, lar, aim, cre, cim, ldt, dsk, w1, b1, w2, b2, (u16*)d_out, (char*)d_ws);
}
```

```cpp
#include <hip/hip_runtime.h>
#include <stdint.h>

typedef unsigned short u16;
typedef unsigned long long u64;
typedef __attribute__((ext_vector_type(4))) short s16x4;
typedef __attribute__((ext_vector_type(8))) short s16x8;
typedef __attribute__((ext_vector_type(8))) _Float16 f16x8;
typedef __attribute__((ext_vector_type(2))) _Float16 f16x2;
typedef __attribute__((ext_vector_type(2))) __bf16 bf16x2;
typedef __attribute__((ext_vector_type(2))) float f32x2;
typedef __attribute__((ext_vector_type(4))) float f32x4;
typedef __attribute__((ext_vector_type(16))) float f32x16;
typedef __attribute__((ext_vector_type(4))) unsigned u32x4;
typedef __attribute__((ext_vector_type(2))) unsigned u32x2;
#define LDSP __attribute__((address_space(3)))
#define GLBP __attribute__((address_space(1)))
#define DI __device__ __forceinline__

#define NLAUNCH 1

#define XPRIO 3
#define L_ 2048
#define D_ 512
#define N_ 128
#define F_ 1792

#define WS_H 0
#define WS_UR (8u << 20)
#define WS_S (10u << 20)
#define WS_STAT (12u << 20)
#define WS_FLAG (13u << 20)
#define WS_KAP (16u << 20)
#define FL_LN 128
#define FL_H 384
#define FL_S 512
#define FL_ST 768

#define STAMP_INIT(kid)
#define STAMP()
#define STAMP0()
#define STAMP_DECL
#define STAMP_FLUSH()
#define TK0_ARG
#define TK0_PASS

DI float bf2f(unsigned v) { return __uint_as_float(v << 16); }
DI float bflo(unsigned v) { return __uint_as_float(v << 16); }
DI float bfhi(unsigned v) { return __uint_as_float(v & 0xffff0000u); }
DI unsigned pkbf(float a, float b) { bf16x2 q = {(__bf16)a, (__bf16)b}; return __builtin_bit_cast(unsigned, q); }
DI float rbf(float a) { __bf16 q = (__bf16)a; return (float)q; }
DI unsigned pkh(float a, float b) { f32x2 v = {a, b}; f16x2 p = __builtin_convertvector(v, f16x2); return __builtin_bit_cast(unsigned, p); }
DI float dot2(unsigned a, unsigned b, float c) { return __builtin_amdgcn_fdot2_f32_bf16(__builtin_bit_cast(bf16x2, a), __builtin_bit_cast(bf16x2, b), c, false); }
template <class T> DI T sel(int c, T a, T b) { return c ? a : b; }
DI void dma16(const void* g, char* l) { __builtin_amdgcn_global_load_lds((const GLBP unsigned*)g, (LDSP unsigned*)l, 16, 0, 0); }
DI void dma16s(unsigned voff, const void* sbase, unsigned ldsaddr) {
  asm volatile("s_mov_b32 m0, %2\n\ts_nop 0\n\tglobal_load_lds_dwordx4 %0, %1" :: "v"(voff), "s"(sbase), "s"(ldsaddr) : "memory");
}
DI unsigned lds_base(char* lds) { return __builtin_amdgcn_readfirstlane((unsigned)(size_t)(LDSP char*)lds); }
DI s16x8 tr_frag(const char* p0, const char* p1) {
  s16x4 a = __builtin_amdgcn_ds_read_tr16_b64_v4i16((LDSP s16x4*)p0);
  s16x4 b = __builtin_amdgcn_ds_read_tr16_b64_v4i16((LDSP s16x4*)p1);
  return __builtin_shufflevector(a, b, 0, 1, 2, 3, 4, 5, 6, 7);
}
#define SCHED() __builtin_amdgcn_sched_barrier(0)
#define WAITV_BAR(n) do { SCHED(); asm volatile("s_waitcnt vmcnt(" #n ") lgkmcnt(0)\n\ts_barrier" ::: "memory"); SCHED(); } while (0)
#define LGKM_BAR() do { SCHED(); asm volatile("s_waitcnt lgkmcnt(0)\n\ts_barrier" ::: "memory"); SCHED(); } while (0)
#define RLX_AGENT __ATOMIC_RELAXED, __HIP_MEMORY_SCOPE_AGENT
#define RLX_WG __ATOMIC_RELAXED, __HIP_MEMORY_SCOPE_WORKGROUP

template <int CTRL, int RM> DI float dpp_add(float v) {
  return v + __builtin_bit_cast(float, __builtin_amdgcn_update_dpp(0, __builtin_bit_cast(int, v), CTRL, RM, 0xf, true));
}
DI float wave_sum63(float v) {
  v = dpp_add<0x111, 0xf>(v); v = dpp_add<0x112, 0xf>(v); v = dpp_add<0x114, 0xf>(v); v = dpp_add<0x118, 0xf>(v);
  v = dpp_add<0x142, 0xa>(v); v = dpp_add<0x143, 0xc>(v);
  return v;
}
DI float comb32(float a, float b) { auto r = __builtin_amdgcn_permlane32_swap(__float_as_uint(a), __float_as_uint(b), false, false); return __uint_as_float(r[0]) + __uint_as_float(r[1]); }
DI float comb16(float a, float b) { auto r = __builtin_amdgcn_permlane16_swap(__float_as_uint(a), __float_as_uint(b), false, false); return __uint_as_float(r[0]) + __uint_as_float(r[1]); }
DI float row_allsum(float v) { v = dpp_add<0x128, 0xf>(v); v = dpp_add<0x124, 0xf>(v); v = dpp_add<0x122, 0xf>(v); v = dpp_add<0x121, 0xf>(v); return v; }
DI float rdlane(float v, int l) { return __builtin_bit_cast(float, __builtin_amdgcn_readlane(__builtin_bit_cast(int, v), l)); }

DI void group_barrier(unsigned* cnt, unsigned target, int lane) {
  asm volatile("s_waitcnt lgkmcnt(0)" ::: "memory");
  if (lane == 0) __hip_atomic_fetch_add(cnt, 1u, RLX_WG);
  while (__hip_atomic_load(cnt, RLX_WG) < target) __builtin_amdgcn_s_sleep(1);
  asm volatile("" ::: "memory");
}
DI void poll_flags(const u64* p, u64 expect) {
  for (unsigned spin = 0; spin < (1u << 20); ++spin) {
    const u64 v = __hip_atomic_load(p, RLX_AGENT);
    if (__all(v == expect)) break;
    __builtin_amdgcn_s_sleep(2);
  }
  asm volatile("" ::: "memory");
}
DI void lds_set(unsigned* w, int lane) { if (lane == 0) __hip_atomic_store(w, 1u, RLX_WG); }
DI void lds_wait(unsigned* w) { while (__hip_atomic_load(w, RLX_WG) == 0u) __builtin_amdgcn_s_sleep(1); asm volatile("" ::: "memory"); }
DI int blk_of(int mb, int cb) { return (mb >> 2) + 8 * (((mb & 3) << 3) | cb); }

#define LDS_TOTAL 163840
#define L_STAT 162304
#define L_BIAS 163328
#define L_ACON 163584
#define L_KSC 163712
#define L_CNT 163744
#define KAPSZ 2576
#define L_KAP 141696
#define L_ASC 140800
#define L_DUMMY 140160
#define K1_SLOT 32768
#define L_COLS 65536
#define L_TB 98304
#define L_KP 131584
#define KPSZ 4160
#define L_RING2 0
#define L_W 65536
#define WSZ 33024
#define L_U L_KAP
#define USZ 4608
#define L_RED 0
#define RED_RS 272
#define L_ST L_KAP
#define ST_RS 144
#define C_PUB 0
#define C_SP 1
#define C_UR 2
#define C_H 3
#define C_ST 4

struct cf { float x, y; };
DI cf cmul(cf a, cf b) { return cf{fmaf(a.x, b.x, -(a.y * b.y)), fmaf(a.x, b.y, a.y * b.x)}; }
DI unsigned pkc(cf a) { return pkh(a.x, a.y); }
DI unsigned pkcc(cf a) { return pkh(a.x, -a.y); }
DI cf zpow(float al, float om, float p) {
  const float m = __builtin_amdgcn_exp2f(al * p), ph = __builtin_amdgcn_fractf(om * p);
  return cf{m * __builtin_amdgcn_cosf(ph), m * __builtin_amdgcn_sinf(ph)};
}
DI unsigned cmulh(unsigned a, unsigned b) {
  const f16x2 z = __builtin_bit_cast(f16x2, a), w = __builtin_bit_cast(f16x2, b);
  const f16x2 t = z * (f16x2){w[0], w[0]};
  const f16x2 r = __builtin_elementwise_fma((f16x2){-z[1], z[0]}, (f16x2){w[1], w[1]}, t);
  return __builtin_bit_cast(unsigned, r);
}
DI u32x4 cmulh4(u32x4 a, u32x4 b) { return (u32x4){cmulh(a[0], b[0]), cmulh(a[1], b[1]), cmulh(a[2], b[2]), cmulh(a[3], b[3])}; }
#define TB_RS 144
#define TB_N 33
DI void kgen_tables(unsigned (&T)[TB_N], float& isc, unsigned raw, float ldtv, char* lds, int cw, int lane) {
  const int src = ((lane & 31) >> 1) * 4, sh = (lane & 1) ? 0 : 16;
  unsigned pv[4];
#pragma unroll
  for (int a = 0; a < 4; ++a) {
    pv[a] = ((unsigned)__builtin_amdgcn_ds_bpermute(src + 64 * a, (int)raw) << sh) & 0xffff0000u;
  }
  const float dt = rbf(__builtin_amdgcn_exp2f(ldtv * 1.4426950408889634f));
  const float ar = rbf(__builtin_amdgcn_exp2f(__uint_as_float(pv[0]) * 1.4426950408889634f));
  const float al = -(ar * dt) * 1.4426950408889634f;
  const float om = (__uint_as_float(pv[1]) * dt) * 0.15915494309189535f;
  const float cr = __uint_as_float(pv[2]) * dt, ci = __uint_as_float(pv[3]) * dt;
  const float sabs = rdlane(wave_sum63(fabsf(cr) + fabsf(ci)), 63);
  int ex = (int)((__float_as_uint(sabs) >> 23) & 255);
  ex = ex < 20 ? 20 : (ex > 230 ? 230 : ex);
  const float sc2 = __uint_as_float((unsigned)(268 - ex) << 23);
  isc = __uint_as_float((unsigned)(ex - 13) << 23);
  if (lane == 0) *(float*)(lds + L_KSC + cw * 4) = sabs;
  const cf z1 = zpow(al, om, 1.f), z8 = zpow(al, om, 8.f), z32 = zpow(al, om, 32.f), a1 = zpow(al, om, 128.f), a4 = zpow(al, om, 512.f);
  const unsigned one = 0x00003C00u;
  const cf a2 = cmul(a1, a1), a3 = cmul(a2, a1), a8 = cmul(a4, a4), a12 = cmul(a8, a4);
  T[0] = one; T[1] = pkc(a1); T[2] = pkc(a2); T[3] = pkc(a3);
  T[4] = pkc(a4); T[5] = pkc(cmul(a4, a1)); T[6] = pkc(cmul(a4, a2)); T[7] = pkc(cmul(a4, a3));
  T[8] = pkc(a8); T[9] = pkc(cmul(a8, a1)); T[10] = pkc(cmul(a8, a2)); T[11] = pkc(cmul(a8, a3));
  T[12] = pkc(a12); T[13] = pkc(cmul(a12, a1)); T[14] = pkc(cmul(a12, a2)); T[15] = pkc(cmul(a12, a3));
  T[16] = pkc(cmul(a8, a8));
  const cf e2 = cmul(z1, z1), e3 = cmul(e2, z1), e4 = cmul(e2, e2);
  T[17] = one; T[18] = pkc(z1); T[19] = pkc(e2); T[20] = pkc(e3); T[21] = pkc(e4);
  T[22] = pkc(cmul(e4, z1)); T[23] = pkc(cmul(e4, e2)); T[24] = pkc(cmul(e4, e3));
  const cf c0 = cf{cr * sc2, ci * sc2}, c2 = cmul(c0, cmul(z32, z32));
  T[25] = pkcc(c0); T[26] = pkcc(cmul(c0, z32)); T[27] = pkcc(c2); T[28] = pkcc(cmul(c2, z32));
  const cf z16 = cmul(z8, z8);
  T[29] = one; T[30] = pkcc(z8); T[31] = pkcc(z16); T[32] = pkcc(cmul(z16, z8));
}
#define KG_PARAMS f32x4& kX, f32x4& kY, f32x4& kZ, float isc
DI void kgen_store(KG_PARAMS, char* lds, int wid, int lane) {
  const int i16 = lane & 15, g = lane >> 4;
  float* kap = (float*)(lds + L_KAP + wid * KAPSZ);
  *(f32x4*)(kap + 128 * (g >> 1) + 4 * (g & 1) + 8 * i16) = kX * isc;
  if (g < 2) {
    *(f32x4*)(kap + 256 + 64 * g + 4 * i16) = kY * isc;
#pragma unroll
    for (int r = 0; r < 4; ++r) kap[512 + 16 * (4 * g + r) + i16] = kZ[r] * isc;
  } else {
    *(f32x2*)(kap + 384 + 64 * (g - 2) + 2 * i16) = (f32x2){kY[0] * isc, kY[1] * isc};
    *(f32x2*)(kap + 384 + 64 * (g - 2) + 32 + 2 * i16) = (f32x2){kY[2] * isc, kY[3] * isc};
    if (g == 2 && i16 == 0) kap[640] = kZ[0] * isc;
  }
}
DI const u16* prm_ptr(const u16* lar, const u16* aim, const u16* cre, const u16* cim, int d, int q4, int lane) {
  const int g = lane >> 4;
  const u16* p = g == 0 ? lar : (g == 1 ? aim : (g == 2 ? cre : cim));
  return p + d * N_ + 32 * q4 + 2 * (lane & 15);
}
DI void ld_u32(unsigned& v, const void* p) { asm volatile("global_load_dword %0, %1, off" : "=v"(v) : "v"(p) : "memory"); }
DI void sld_u32(unsigned& v, const void* p) { asm volatile("s_load_dword %0, %1, 0x0" : "=s"(v) : "s"(p) : "memory"); }
DI void sld_u64(u64& v, const void* p) { asm volatile("s_load_dwordx2 %0, %1, 0x0" : "=s"(v) : "s"(p) : "memory"); }
DI void ld_u64(u64& v, const void* p) { asm volatile("global_load_dwordx2 %0, %1, off sc1" : "=v"(v) : "v"(p) : "memory"); }
DI void ld_u64p(u64& v, const void* p) { asm volatile("global_load_dwordx2 %0, %1, off" : "=v"(v) : "v"(p) : "memory"); }
DI void gload16(u32x4& v, unsigned voff, const char* sbase) {
  asm volatile("global_load_dwordx4 %0, %1, %2 sc1" : "=v"(v) : "v"(voff), "s"(sbase) : "memory");
}
DI bool last_of(char* lds, int c, unsigned n, int lane) {
  unsigned old = 0;
  if (lane == 0) old = __hip_atomic_fetch_add((unsigned*)(lds + L_CNT) + c, 1u, RLX_WG);
  return (unsigned)__builtin_amdgcn_readfirstlane(old) == n - 1;
}

DI void kgen_all(unsigned raw, float ldtv, char* lds, int wid, int lane) {
  const int i16 = lane & 15, g = lane >> 4;
  unsigned T[TB_N];
  float isc;
  kgen_tables(T, isc, raw, ldtv, lds, wid, lane);
  char* tb = lds + L_TB + wid * (TB_N * TB_RS);
  if (lane < 32) {
#pragma unroll
    for (int i = 0; i < TB_N; ++i) *(unsigned*)(tb + i * TB_RS + lane * 4) = T[i];
  }
  const char* tr = tb + g * 16;
  const int rXa = (i16 >> 3) * TB_RS, rXe = (17 + (i16 & 7)) * TB_RS;
  const int rYa = (i16 < 8 ? 2 + (i16 >> 2) : 4 + ((i16 - 8) >> 1)) * TB_RS, rYe = (17 + (i16 < 8 ? 2 * (i16 & 3) : 4 * (i16 & 1))) * TB_RS;
  const int rZa = (i16 < 8 ? 8 + i16 : 16) * TB_RS;
  const int rB1 = (25 + (i16 >> 2)) * TB_RS, rB0 = (29 + (i16 & 3)) * TB_RS;
  f32x4 kX = {0.f, 0.f, 0.f, 0.f}, kY = kX, kZ = kX;
#pragma unroll
  for (int s = 0; s < 2; ++s) {
    const u32x4 fB = cmulh4(*(const u32x4*)(tr + rB1 + s * 64), *(const u32x4*)(tr + rB0 + s * 64));
    const u32x4 fX = cmulh4(*(const u32x4*)(tr + rXa + s * 64), *(const u32x4*)(tr + rXe + s * 64));
    const u32x4 fY = cmulh4(*(const u32x4*)(tr + rYa + s * 64), *(const u32x4*)(tr + rYe + s * 64));
    const u32x4 fZ = *(const u32x4*)(tr + rZa + s * 64);
    const f16x8 hb = __builtin_bit_cast(f16x8, fB);
    kX = __builtin_amdgcn_mfma_f32_16x16x32_f16(__builtin_bit_cast(f16x8, fX), hb, kX, 0, 0, 0);
    kY = __builtin_amdgcn_mfma_f32_16x16x32_f16(__builtin_bit_cast(f16x8, fY), hb, kY, 0, 0, 0);
    kZ = __builtin_amdgcn_mfma_f32_16x16x32_f16(__builtin_bit_cast(f16x8, fZ), hb, kZ, 0, 0, 0);
  }
  kgen_store(kX, kY, kZ, isc, lds, wid, lane);
}
DI int chan_ex(const char* lds, int ch) {
  const f32x4 ks = *(const f32x4*)(lds + L_KSC + ch * 16);
  const float sabs = (ks[0] + ks[1]) + (ks[2] + ks[3]);
  const int ex = (int)((__float_as_uint(sabs) >> 23) & 255);
  return ex < 20 ? 20 : (ex > 230 ? 230 : ex);
}
DI void kgen_expand(char* lds, int wid, int lane) {
  const int ch = wid >> 2, q4 = wid & 3;
  const float scale = __uint_as_float((unsigned)(267 - chan_ex(lds, ch)) << 23);
  const float* k0 = (const float*)(lds + L_KAP + (4 * ch) * KAPSZ);
  char* kp = lds + L_KP + ch * KPSZ;
#define KSUM4(T, i) (((*(const T*)(k0 + (i)) + *(const T*)(k0 + 644 + (i))) + (*(const T*)(k0 + 1288 + (i)) + *(const T*)(k0 + 1932 + (i)))) * scale)
  const int A = 64 * q4 + lane;
  u32x4 o;
  if (q4 == 0) {
    if (lane < 32) {
      const f32x4 a = KSUM4(f32x4, 8 * A), b = KSUM4(f32x4, 8 * A + 4);
      o = (u32x4){pkh(a[0], a[1]), pkh(a[2], a[3]), pkh(b[0], b[1]), pkh(b[2], b[3])};
    } else {
      const int i0 = 256 + 4 * (A - 32);
      const f32x4 a = KSUM4(f32x4, i0);
      const float e4 = KSUM4(float, i0 + 4);
      o = (u32x4){pkh(a[0], a[0]), pkh(a[1], a[2]), pkh(a[2], a[2]), pkh(a[3], e4)};
    }
    if (lane < 2) *(u32x4*)(kp + lane * 16) = (u32x4){0u, 0u, 0u, 0u};
  } else if (q4 == 1) {
    const int i0 = 384 + 2 * lane;
    const f32x2 a = KSUM4(f32x2, i0);
    const float e2 = KSUM4(float, i0 + 2);
    o = (u32x4){pkh(a[0], a[0]), pkh(a[0], a[1]), pkh(a[1], a[1]), pkh(e2, e2)};
  } else {
    const int i0 = 512 + (A - 128);
    const float e0 = KSUM4(float, i0), e1 = KSUM4(float, i0 + 1);
    const unsigned p00 = pkh(e0, e0);
    o = (u32x4){p00, p00, pkh((A & 1) ? e1 : e0, e1), pkh(e1, e1)};
  }
  *(u32x4*)(kp + 32 + A * 16) = o;
}

DI float gelu_f(float v) {
  const float c0 = -2.302208198f;
  const float c1 = -0.1029432397f;
  const float z = v * fmaf(v * v, c1, c0);
  return v * __builtin_amdgcn_rcpf(1.f + __builtin_amdgcn_exp2f(z));
}

#define K2_DMA(S) { _Pragma("unroll") for (int pp = 0; pp < 4; ++pp) dma16s(wvoff, wsrc + (size_t)((S) * 32 + pp * 8) * D_, ringa + ((S) & 1) * 4096 + pp * 1024); }
#define K2_SETUP() int lane2_ = lane; asm volatile("" : "+v"(lane2_)); \
  const unsigned wvoff = (lane2_ >> 3) * (D_ * 2) + (((lane2_ & 7) ^ (((lane2_ >> 4) & 1) << 2)) << 4); \
  const unsigned ringa = __builtin_amdgcn_readfirstlane(lds_base(lds) + L_RING2 + wid * 8192);
#define HMT 114688

DI void pretouch(const u16* w2, const u16* b2, const u16* x, const u16* out, const char* ws, char* lds, int mb, int cb, int panel, int fs, int lane) {
  const char* p;
  if (lane < 28) p = (const char*)w2 + (size_t)lane * 65536 + cb * 128;
  else if (lane < 32) p = ws + WS_H + (size_t)(panel * 4 + (lane - 28)) * HMT + fs * 8192;
  else if (lane < 36) p = ws + WS_H + (size_t)(2 * mb) * HMT + (lane - 32) * 57344;
  else if (lane < 40) p = ws + WS_S + (size_t)(64 * cb + 16 * (lane - 36)) * 4096;
  else if (lane == 40) p = ws + WS_UR + (size_t)(64 * cb + 2 * mb) * 4096;
  else if (lane == 41) p = ws + WS_STAT;
  else if (lane == 42) p = (const char*)(out + (size_t)64 * mb * D_);
  else if (lane == 43) p = (const char*)(x + (size_t)64 * mb * D_);
  else p = (const char*)b2;
  asm volatile("s_mov_b32 m0, %1\n\ts_nop 0\n\tglobal_load_lds_dword %0, off" :: "v"(p), "s"(lds_base(lds) + L_DUMMY) : "memory");
}
DI void p1_gemm(const u16* w2_, const u16* b2_, const u16* out_, int mb_, const u16* __restrict__ x, const u16* __restrict__ w1, const u16* __restrict__ b1, const u16* prm, const u16* wsrc,
                u64& aOwn, unsigned sdt, char* __restrict__ ws, char* lds, int panel, int fs, int cb, int dodd, int wid, int lane TK0_ARG) {
  STAMP_DECL STAMP_INIT(0) STAMP0();
  const int wm = wid >> 2, wf = (wid >> 1) & 1, kh = wid & 1;
  const int r = lane & 31, h = lane >> 5;
  const int row0 = panel * 128, f0 = fs * 128;
  unsigned raw;
  u64 misc;
  ld_u32(raw, prm);
  ld_u64(misc, ws + WS_FLAG + (size_t)(lane >= 32 ? (int)blockIdx.x : ((wid & 1) ? blk_of(2 * panel + ((lane >> 3) & 1), lane & 7) : blk_of(lane, cb))) * 1024);
  if (wid == 0) asm volatile("s_mov_b32 m0, %2\n\ts_nop 0\n\tglobal_load_lds_dword %0, %1" :: "v"(lane * 4), "s"(b1 + f0), "s"(lds_base(lds) + L_BIAS) : "memory");
  const unsigned xv0 = (lane >> 3) * 1024 + (((lane & 7) ^ ((lane >> 4) & 3)) << 4);
  const unsigned xv1 = xv0 ^ 64;
  const unsigned wv = (lane >> 4) * (F_ * 2) + (((lane & 15) ^ (((lane >> 4) & 3) << 2)) << 4);
  const u16* xsb = x + (size_t)(row0 + 16 * wid) * D_;
  const u16* wsb = w1 + (size_t)(8 * wid) * F_ + f0;
  const unsigned la = lds_base(lds) + wid * 2048;
#define K1_D0(S) { dma16s(xv0, xsb + (S) * 64, la + ((S) % 4) * K1_SLOT); SCHED(); }
#define K1_D1(S) { dma16s(xv1, xsb + 8 * D_ + (S) * 64, la + ((S) % 4) * K1_SLOT + 1024); SCHED(); }
#define K1_D2(S) { dma16s(wv, wsb + (size_t)(S) * 64 * F_, la + ((S) % 4) * K1_SLOT + 16384); SCHED(); }
#define K1_D3(S) { dma16s(wv, wsb + (size_t)((S) * 64 + 4) * F_, la + ((S) % 4) * K1_SLOT + 16384 + 1024); SCHED(); }
#define K1_ISSUE(S) { K1_D0(S); K1_D1(S); K1_D2(S); K1_D3(S); }
  K1_ISSUE(0); K1_ISSUE(1);
  if (wid == 7) pretouch(w2_, b2_, x, out_, ws, lds, mb_, cb, panel, fs, lane);
  K1_ISSUE(2);
  if (wid == 0 && lane < 24) ((unsigned*)(lds + L_CNT))[lane] = 0u;
  STAMP();
  asm volatile("s_waitcnt vmcnt(13) lgkmcnt(0)" : "+v"(raw) :: "memory");
  STAMP();
  kgen_all(raw, dodd ? bfhi(sdt) : bflo(sdt), lds, wid, lane);
  const int xoff = (64 * wm + r) * 128 + (((4 * kh + h) ^ ((r >> 1) & 7)) << 4);
  const int q = (lane & 15) >> 2, p = lane & 3;
  const int woff0 = 16384 + (32 * kh + 8 * h + q) * 256 + (((2 * wf + kh) ^ q) << 6) + ((lane >> 4) & 1) * 32 + 8 * p;
  f32x16 acc[2][2];
#pragma unroll
  for (int i = 0; i < 16; ++i) { acc[0][0][i] = 0.f; acc[0][1][i] = 0.f; acc[1][0][i] = 0.f; acc[1][1][i] = 0.f; }
  float cs = 0.f;
  STAMP();
  s16x8 xf[2][2][2], wfr[2][2][2];
#define K1_M(T, s, mi, fi) { acc[mi][fi] = __builtin_amdgcn_mfma_f32_32x32x16_bf16(wfr[(T) & 1][s][fi], xf[(T) & 1][s][mi], acc[mi][fi], 0, 0, 0); SCHED(); }
#define K1_RX(T, s, mi) { xf[(T) & 1][s][mi] = *(const s16x8*)(lds + (xa_ ^ ((s) << 5)) + (mi) * 4096); SCHED(); }
#define K1_RW(T, s, fi) { wfr[(T) & 1][s][fi] = tr_frag(lds + (wa_ ^ ((fi) << 6)) + (s) * 4096, lds + (wa_ ^ ((fi) << 6)) + (s) * 4096 + 1024); SCHED(); }
#define K1_ADDR(T) int xa_ = xoff + ((T) % 4) * K1_SLOT, wa_ = woff0 + ((T) % 4) * K1_SLOT; asm volatile("" : "+v"(xa_), "+v"(wa_));
#define K1_CS1(T, fi) { const u32x4 wa = __builtin_bit_cast(u32x4, wfr[(T) & 1][0][fi]), wb = __builtin_bit_cast(u32x4, wfr[(T) & 1][1][fi]); \
    float c2_ = dot2(wb[0], 0x3F803F80u, 0.f); cs = dot2(wa[0], 0x3F803F80u, cs); \
    _Pragma("unroll") for (int i = 1; i < 4; ++i) { cs = dot2(wa[i], 0x3F803F80u, cs); c2_ = dot2(wb[i], 0x3F803F80u, c2_); } cs += c2_; asm volatile("" : "+v"(cs)); }
#define K1_CS(T) { if (wm == 0) K1_CS1(T, 0) else K1_CS1(T, 1) SCHED(); }
#define K1_READ(T) { K1_ADDR(T) K1_RX(T,0,0); K1_RW(T,0,0); K1_RW(T,0,1); K1_RX(T,0,1); K1_RX(T,1,0); K1_RW(T,1,0); K1_RW(T,1,1); K1_RX(T,1,1); }
#define K1_MMA(T) { K1_M(T,0,0,0); K1_M(T,0,0,1); K1_M(T,0,1,0); K1_M(T,0,1,1); K1_M(T,1,0,0); K1_M(T,1,0,1); K1_M(T,1,1,0); K1_M(T,1,1,1); K1_CS(T); }
#define K1_STAGE(T, DM0, DM1, DM2, DM3) { K1_ADDR(T) \
    K1_M((T)-1,0,0,0); K1_RX(T,0,0); K1_RW(T,0,0); K1_M((T)-1,0,0,1); K1_RW(T,0,1); K1_RX(T,0,1); \
    K1_M((T)-1,0,1,0); K1_RX(T,1,0); K1_RW(T,1,0); K1_M((T)-1,0,1,1); K1_RW(T,1,1); K1_RX(T,1,1); \
    K1_M((T)-1,1,0,0); DM0; K1_M((T)-1,1,0,1); DM1; K1_M((T)-1,1,1,0); DM2; K1_M((T)-1,1,1,1); DM3; K1_CS((T)-1); }
#define K1_STAGE_D(T, S) K1_STAGE(T, K1_D0(S), K1_D1(S), K1_D2(S), K1_D3(S))
  SCHED();
  asm volatile("s_waitcnt vmcnt(8) lgkmcnt(0)\n\ts_barrier" : "+v"(misc) :: "memory");
  SCHED(); STAMP();
  K1_ISSUE(3);
  K1_READ(0);
  aOwn = (u64)(unsigned)__builtin_amdgcn_readlane((int)(unsigned)misc, 32) | ((u64)(unsigned)__builtin_amdgcn_readlane((int)(unsigned)(misc >> 32), 32) << 32);
  if (wid == 1 && lane < 16) *(u64*)(lds + L_ACON + lane * 8) = misc;
  if (wid == 0 && lane < 32) *(u64*)(lds + L_ASC + lane * 8) = misc;
  kgen_expand(lds, wid, lane);
  WAITV_BAR(8); K1_STAGE_D(1, 4);
  WAITV_BAR(8); K1_STAGE_D(2, 5);
  WAITV_BAR(8); K1_STAGE_D(3, 6);
  WAITV_BAR(8); STAMP(); K1_STAGE_D(4, 7);
  if (wid == 2) {
    const u64* stfl = (const u64*)(ws + WS_FLAG + (size_t)blockIdx.x * 1024 + FL_ST) + (lane & 1);
    const char* stp = ws + WS_STAT + (size_t)panel * 1024;
    u64 fv; u32x4 stv;
    WAITV_BAR(8); K1_STAGE(5, , , , );
    WAITV_BAR(4); K1_STAGE(6, ld_u64(fv, stfl), , , );
    K1_MMA(6);
    SCHED(); asm volatile("s_waitcnt vmcnt(0) lgkmcnt(0)\n\ts_barrier" : "+v"(fv) :: "memory"); SCHED();
    if (!__all(fv == aOwn + 1)) poll_flags(stfl, aOwn + 1);
    gload16(stv, lane * 16, stp);
    K1_READ(7); K1_MMA(7);
    asm volatile("s_waitcnt vmcnt(0)" : "+v"(stv) :: "memory");
    *(u32x4*)(lds + L_STAT + lane * 16) = stv;
  } else {
    WAITV_BAR(8); K1_STAGE(5, , , , );
    WAITV_BAR(4); K1_STAGE(6, , , , );
    K1_MMA(6);
    WAITV_BAR(0);
    K1_READ(7); K1_MMA(7);
  }
  STAMP();

  float* colS = (float*)(lds + L_COLS);
  {
    char* dst = lds + wid * 8192 + lane * 16;
#pragma unroll
    for (int mi = 0; mi < 2; ++mi)
#pragma unroll
      for (int rq = 0; rq < 4; ++rq)
        *(f32x4*)(dst + (mi * 4 + rq) * 1024) = (f32x4){acc[mi][1][4 * rq], acc[mi][1][4 * rq + 1], acc[mi][1][4 * rq + 2], acc[mi][1][4 * rq + 3]};
    const float cc = comb32(cs, cs);
    if (h == 0) colS[kh * 128 + 64 * wf + 32 * (wm ^ kh) + r] = cc;
  }
  LGKM_BAR();
  STAMP();
  f32x4 pa[2][4], cq[4];
  f32x2 st[2];
  u32x2 bq[4];
  {
    const char* src = lds + (wid ^ 1) * 8192 + lane * 16;
    const int fl = 64 * wf + 32 * kh + 4 * h;
#pragma unroll
    for (int rq = 0; rq < 4; ++rq) {
      cq[rq] = *(const f32x4*)(colS + fl + 8 * rq) + *(const f32x4*)(colS + 128 + fl + 8 * rq);
      bq[rq] = *(const u32x2*)(lds + L_BIAS + (fl + 8 * rq) * 2);
      pa[0][rq] = *(const f32x4*)(src + rq * 1024); pa[1][rq] = *(const f32x4*)(src + (4 + rq) * 1024);
    }
    st[0] = *(const f32x2*)(lds + L_STAT + (64 * wm + r) * 8); st[1] = *(const f32x2*)(lds + L_STAT + (64 * wm + 32 + r) * 8);
  }
  LGKM_BAR();
  {
    K2_SETUP();
    K2_DMA(0); K2_DMA(1);
    SCHED();
  }
  {
    const __amdgpu_buffer_rsrc_t hr = __builtin_amdgcn_make_buffer_rsrc(ws + WS_H, 0, L_ * F_ * 2, 0x00020000);
    const unsigned hoff = (unsigned)(panel * 4 + 2 * wm) * HMT + (8 * fs + 4 * wf + 2 * kh) * 1024 + lane * 16;
#pragma unroll
    for (int mi = 0; mi < 2; ++mi) {
      const float mr = st[mi][0], rinv = st[mi][1];
#pragma unroll
      for (int s2 = 0; s2 < 2; ++s2) {
        u32x4 pk;
#pragma unroll
        for (int qq = 0; qq < 2; ++qq) {
          const int rq = 2 * s2 + qq;
          const float b0 = bflo(bq[rq][0]), b1_ = bfhi(bq[rq][0]), b2_ = bflo(bq[rq][1]), b3 = bfhi(bq[rq][1]);
          const float o0 = gelu_f(fmaf(acc[mi][0][4 * rq] + pa[mi][rq][0], rinv, fmaf(mr, cq[rq][0], b0)));
          const float o1 = gelu_f(fmaf(acc[mi][0][4 * rq + 1] + pa[mi][rq][1], rinv, fmaf(mr, cq[rq][1], b1_)));
          const float o2 = gelu_f(fmaf(acc[mi][0][4 * rq + 2] + pa[mi][rq][2], rinv, fmaf(mr, cq[rq][2], b2_)));
          const float o3 = gelu_f(fmaf(acc[mi][0][4 * rq + 3] + pa[mi][rq][3], rinv, fmaf(mr, cq[rq][3], b3)));
          pk[2 * qq] = pkbf(o0, o1); pk[2 * qq + 1] = pkbf(o2, o3);
        }
        __builtin_amdgcn_raw_buffer_store_b128(pk, hr, hoff + mi * HMT + s2 * 1024, 0, 16);
      }
    }
  }
  STAMP();
  asm volatile("s_waitcnt vmcnt(0)" ::: "memory");
  if (last_of(lds, C_PUB, 8, lane) && lane < 16) {
    const u64 ac = *(const u64*)(lds + L_ACON + lane * 8);
    int l2 = lane; asm volatile("" : "+v"(l2));
    __hip_atomic_store((u64*)(ws + WS_FLAG + (size_t)blk_of(2 * panel + ((l2 >> 3) & 1), l2 & 7) * 1024 + FL_H) + fs, ac + 1, RLX_AGENT);
  }
  STAMP(); STAMP_FLUSH();
}

#define LN_RS 1040
DI void p1_aux(const u16* __restrict__ x, const u16* prm, const u16* wsrc, u64& aOwn, unsigned sdt, char* __restrict__ ws, char* lds, int idx,
               int cb, int dodd, int wid, int lane TK0_ARG) {
  STAMP_DECL STAMP_INIT(0) STAMP0();
  const int t0 = idx * 64;
  u32x4 v[8];
#pragma unroll
  for (int i = 0; i < 8; ++i) v[i] = *(const u32x4*)(x + (size_t)(t0 + 8 * wid + i) * D_ + lane * 8);
  const u64 aCon = __hip_atomic_load((const u64*)(ws + WS_FLAG + (size_t)((wid * 64 + lane) & 255) * 1024), RLX_AGENT);
  const int sfs = lane < 14 ? lane : 0;
  const int sblk = 2 * (idx >> 3) + (sfs >= 7 ? 1 : 0) + 8 * (((idx >> 1) & 3) * 7 + (sfs >= 7 ? sfs - 7 : sfs));
  const u64 aSt = __hip_atomic_load((const u64*)(ws + WS_FLAG + (size_t)sblk * 1024), RLX_AGENT);
  const u64 aS = __hip_atomic_load((const u64*)(ws + WS_FLAG + (size_t)(lane >= 32 ? (int)blockIdx.x : blk_of(lane, cb)) * 1024), RLX_AGENT);
  const unsigned raw = *(const unsigned*)prm;
  if (wid == 0 && lane < 24) ((unsigned*)(lds + L_CNT))[lane] = 0u;
  SCHED();
  asm volatile("s_waitcnt lgkmcnt(0)\n\ts_barrier" ::: "memory");
  SCHED();
  float sA[8], sB[8];
#pragma unroll
  for (int i = 0; i < 8; ++i) {
    float a = dot2(v[i][0], 0x3F803F80u, 0.f), b = dot2(v[i][0], v[i][0], 0.f);
#pragma unroll
    for (int c = 1; c < 4; ++c) { a = dot2(v[i][c], 0x3F803F80u, a); b = dot2(v[i][c], v[i][c], b); }
    sA[i] = a; sB[i] = b;
  }
#pragma unroll
  for (int i = 0; i < 4; ++i) { sA[i] = comb32(sA[i], sA[i + 4]); sB[i] = comb32(sB[i], sB[i + 4]); }
#pragma unroll
  for (int i = 0; i < 2; ++i) { sA[i] = row_allsum(comb16(sA[i], sA[i + 2])); sB[i] = row_allsum(comb16(sB[i], sB[i + 2])); }
  const float mv = ((lane & 1) ? sA[1] : sA[0]) * (1.f / 512.f);
  const float var = fmaxf(fmaf(-mv, mv, ((lane & 1) ? sB[1] : sB[0]) * (1.f / 512.f)), 0.f);
  const float mbv = rbf(mv);
  const float sd = rbf(sqrtf(rbf(rbf(var) + 1e-5f)));
  const float rv = 1.f / sd;
  if ((lane & 14) == 0) {
    const u64 pk = (u64)__float_as_uint(-mbv * rv) | ((u64)__float_as_uint(rv) << 32);
    __hip_atomic_store((u64*)(ws + WS_STAT) + t0 + 8 * wid + (lane & 1) + 2 * (lane >> 4), pk, RLX_AGENT);
  }
  if (wid == 0 && lane < 32) *(u64*)(lds + L_ASC + lane * 8) = aS;
  aOwn = (u64)(unsigned)__builtin_amdgcn_readlane((int)(unsigned)aS, 32) | ((u64)(unsigned)__builtin_amdgcn_readlane((int)(unsigned)(aS >> 32), 32) << 32);
#define LN_ROW(i) { const float rinv = rdlane(rv, ((i) & 1) + 16 * ((i) >> 1)), mb = rdlane(mbv, ((i) & 1) + 16 * ((i) >> 1)); u32x4 pk; \
    _Pragma("unroll") for (int c = 0; c < 4; ++c) { const unsigned a = pkbf(bflo(v[i][c]) - mb, bfhi(v[i][c]) - mb); \
      const unsigned b = pkbf(bflo(a) * rinv, bfhi(a) * rinv); pk[c] = pkh(bflo(b), bfhi(b)); } \
    *(u32x4*)(lds + (63 - (8 * wid + (i))) * LN_RS + lane * 16) = pk; }
  LN_ROW(0); LN_ROW(1);
  SCHED();
  asm volatile("s_waitcnt vmcnt(0)" ::: "memory");
  if (last_of(lds, C_PUB, 8, lane) && lane < 14) __hip_atomic_store((u64*)(ws + WS_FLAG + (size_t)sblk * 1024 + FL_ST) + (idx & 1), aSt + 1, RLX_AGENT);
  STAMP();
  LN_ROW(2); LN_ROW(3); LN_ROW(4); LN_ROW(5); LN_ROW(6); LN_ROW(7);
  STAMP();
  LGKM_BAR();
  char* t2 = lds + 64 * LN_RS + wid * 8192;
  const int g = lane >> 4, q = (lane & 15) >> 2, p = lane & 3, dl = lane & 15;
  s16x8 f[8];
#pragma unroll
  for (int i = 0; i < 8; ++i) {
    const char* a = lds + (8 * (4 * (i & 1) + g) + q) * LN_RS + (64 * wid + 16 * (i >> 1) + 4 * p) * 2;
    f[i] = tr_frag(a, a + 4 * LN_RS);
  }
  SCHED();
#pragma unroll
  for (int i = 0; i < 8; ++i) {
    const int dd2 = 16 * (i >> 1) + dl, rc = 4 * (i & 1) + g;
    *(s16x8*)(t2 + dd2 * 128 + ((rc ^ (dd2 & 7)) << 4)) = f[i];
  }
  SCHED();
  const __amdgpu_buffer_rsrc_t ur = __builtin_amdgcn_make_buffer_rsrc(ws + WS_UR, 0, L_ * D_ * 2, 0x00020000);
  const int x0 = L_ - 64 - t0;
  u32x4 o[8];
#pragma unroll
  for (int i = 0; i < 8; ++i) {
    const int dd2 = 8 * i + (lane >> 3), c = lane & 7;
    o[i] = *(const u32x4*)(t2 + dd2 * 128 + ((c ^ (dd2 & 7)) << 4));
  }
  SCHED();
#pragma unroll
  for (int i = 0; i < 8; ++i)
    __builtin_amdgcn_raw_buffer_store_b128(o[i], ur, (unsigned)(64 * wid + 8 * i + (lane >> 3)) * 4096 + x0 * 2 + (lane & 7) * 16, 0, 16);
  STAMP();
  WAITV_BAR(0);
  if (wid < 4) __hip_atomic_store((u64*)(ws + WS_FLAG + (size_t)(wid * 64 + lane) * 1024 + FL_LN) + idx, aCon + 1, RLX_AGENT);
  STAMP();
  {
    K2_SETUP();
    K2_DMA(0); K2_DMA(1);
    SCHED();
  }
  kgen_all(raw, dodd ? bfhi(sdt) : bflo(sdt), lds, wid, lane);
  LGKM_BAR();
  kgen_expand(lds, wid, lane);
  WAITV_BAR(0);
  STAMP(); STAMP_FLUSH();
}

DI void phase2(const u16* __restrict__ x, const u16* wsrc, u64 aOwn, float dskip, const u16* __restrict__ b2, u16* __restrict__ out,
               char* __restrict__ ws, char* lds, int mb, int cb, int wid, int lane TK0_ARG) {
  STAMP_DECL STAMP_INIT(1) STAMP0();
  const int ch = wid >> 2, q4 = wid & 3;
  const int d = 64 * cb + 2 * mb + ch;
  const int i16 = lane & 15, g = lane >> 4, r = lane & 31, h = lane >> 5;
  const int tid = wid * 64 + lane;
  unsigned* cnt = (unsigned*)(lds + L_CNT);
  const char* flg = ws + WS_FLAG + (size_t)blockIdx.x * 1024;
  K2_SETUP();
  const char* ring = lds + L_RING2 + wid * 8192;
  const int toff0 = (4 * h + (i16 >> 2)) * 128 + ((i16 >> 3) << 6) + (g & 1) * 32 + 8 * (lane & 3), toff1 = toff0 ^ 64;
  s16x8 wp[7][4];
#define PARK(S) { const char* sl_ = ring + ((S) & 1) * 4096; \
    wp[S][0] = tr_frag(sl_ + toff0, sl_ + toff0 + 1024); wp[S][1] = tr_frag(sl_ + toff1, sl_ + toff1 + 1024); \
    wp[S][2] = tr_frag(sl_ + toff0 + 2048, sl_ + toff0 + 3072); wp[S][3] = tr_frag(sl_ + toff1 + 2048, sl_ + toff1 + 3072); }
#define PARK_PIN(S) asm volatile("s_waitcnt lgkmcnt(0)" : "+v"(wp[S][0]), "+v"(wp[S][1]), "+v"(wp[S][2]), "+v"(wp[S][3]) :: "memory")
  STAMP();
  PARK(0); PARK(1); PARK_PIN(0); PARK_PIN(1);
  K2_DMA(2); K2_DMA(3);
  if (wid == 0) { poll_flags((const u64*)(flg + FL_LN) + (lane & 31), aOwn + 1); lds_set(cnt + C_UR, lane); }
  const float iscale = __uint_as_float((unsigned)(chan_ex(lds, ch) - 13) << 23);
  char* wl = lds + L_W + ch * WSZ;
  const char* kp = lds + L_KP + ch * KPSZ;
#pragma unroll
  for (int rnd = 0; rnd < 2; ++rnd) {
    const int a = rnd == 0 ? 64 * q4 + lane : 256;
    if (rnd == 1 && !(q4 == 3 && lane == 0)) break;
    const u32x4 c0 = *(const u32x4*)(kp + a * 16), c1 = *(const u32x4*)(kp + a * 16 + 16);
    const unsigned e[8] = {c0[0], c0[1], c0[2], c0[3], c1[0], c1[1], c1[2], c1[3]};
    unsigned o[7];
#pragma unroll
    for (int i = 0; i < 7; ++i) o[i] = __builtin_amdgcn_alignbit(e[i + 1], e[i], 16);
    const int m = (a & 1) | (((a >> 2) & 1) << 1) | (((a >> 1) & 1) << 2);
    char* base = wl + a * 128;
#pragma unroll
    for (int c = 0; c < 8; ++c) {
      u32x4 wv;
      if (c & 1) { const int s = (c + 1) >> 1; wv = (u32x4){e[s], e[s + 1], e[s + 2], e[s + 3]}; }
      else { const int s = c >> 1; wv = (u32x4){o[s], o[s + 1], o[s + 2], o[s + 3]}; }
      *(u32x4*)(base + ((c ^ m) << 4)) = wv;
    }
  }
  STAMP();
  lds_wait(cnt + C_UR);
  char* ul = lds + L_U + ch * USZ;
  u32x4 uv;
  gload16(uv, lane * 16, ws + WS_UR + (size_t)d * 4096 + q4 * 1024);
  asm volatile("s_waitcnt vmcnt(1)" ::: "memory");
  PARK(2); PARK(3); PARK_PIN(2); PARK_PIN(3);
  K2_DMA(4); K2_DMA(5);
  asm volatile("s_waitcnt vmcnt(8)" : "+v"(uv) :: "memory");
  *(u32x4*)(ul + q4 * 1024 + lane * 16) = uv;
  if (q4 == 0 && lane < 32) *(u32x4*)(ul + 4096 + lane * 16) = (u32x4){0u, 0u, 0u, 0u};
  LGKM_BAR();
  STAMP();
  f32x4 ca[8];
#pragma unroll
  for (int i = 0; i < 8; ++i) ca[i] = (f32x4){0.f, 0.f, 0.f, 0.f};
  {
    const int c = i16 + 8 * g;
    int fo[2];
#pragma unroll
    for (int par = 0; par < 2; ++par) {
      const int w = 32 * par + c;
      const int m = ((w >> 3) & 1) | (((w >> 5) & 1) << 1) | (((w >> 4) & 1) << 2);
      fo[par] = (w ^ m) << 4;
    }
    const char* fbase = wl + q4 * 1024;
    const char* gbase = ul + (2032 - 16 * i16 + 8 * g + 64 * q4) * 2 - 7 * 512;
#define CF(s, e) (*(const f16x8*)(fbase + fo[s] + (e) * 4096))
#define CG(s, mu) (*(const f16x8*)(gbase + (s) * 64 + (7 - (mu)) * 512))
    f16x8 F[2][8], G[2];
#pragma unroll
    for (int e = 0; e < 8; ++e) F[0][e] = CF(0, e);
    G[0] = CG(0, 0);
#pragma unroll
    for (int s = 0; s < 2; ++s) {
#pragma unroll
      for (int mu = 0; mu < 8; ++mu) {
        const int it = s * 8 + mu;
        SCHED();
        if (it + 1 < 16) G[(it + 1) & 1] = CG((it + 1) >> 3, (it + 1) & 7);
        if (s == 0) F[1][mu] = CF(1, mu);
        SCHED();
#pragma unroll
        for (int e = 0; e + mu < 8; ++e) ca[e + mu] = __builtin_amdgcn_mfma_f32_16x16x32_f16(F[s][e], G[it & 1], ca[e + mu], 0, 0, 0);
      }
    }
  }
  STAMP();
  asm volatile("s_waitcnt vmcnt(0)" ::: "memory");
  PARK(4); PARK(5); PARK_PIN(4); PARK_PIN(5);
  K2_DMA(6);
  LGKM_BAR();
  {
    char* dst = wl + q4 * 8192 + lane * 16;
#pragma unroll
    for (int i = 0; i < 8; ++i) *(f32x4*)(dst + i * 1024) = ca[i];
  }
  LGKM_BAR();
  STAMP();
  {
    const char* src = wl + (2 * q4) * 1024 + lane * 16;
    char* sdst = ws + WS_S + (size_t)d * 4096;
#pragma unroll
    for (int i = 0; i < 2; ++i) {
      const f32x4 yv = ((*(const f32x4*)(src + i * 1024) + *(const f32x4*)(src + 8192 + i * 1024)) +
                        (*(const f32x4*)(src + 16384 + i * 1024) + *(const f32x4*)(src + 24576 + i * 1024))) * iscale;
      const int t = 256 * (2 * q4 + i) + 16 * i16 + 4 * g;
      const u32x2 uu = *(const u32x2*)(ul + (2044 - t) * 2);
      const unsigned uu0 = uu[0], uu1 = uu[1];
      const f16x2 ua = __builtin_bit_cast(f16x2, uu0), ub = __builtin_bit_cast(f16x2, uu1);
      const unsigned y01 = pkbf(yv[0], yv[1]), y23 = pkbf(yv[2], yv[3]);
      const unsigned k01 = pkbf((float)ub[1] * dskip, (float)ub[0] * dskip), k23 = pkbf((float)ua[1] * dskip, (float)ua[0] * dskip);
      const u64 pk = (u64)pkbf(bflo(y01) + bflo(k01), bfhi(y01) + bfhi(k01)) | ((u64)pkbf(bflo(y23) + bflo(k23), bfhi(y23) + bfhi(k23)) << 32);
      __hip_atomic_store((u64*)(sdst + t * 2), pk, RLX_AGENT);
    }
  }
  SCHED();
  if (wid == 1) { poll_flags((const u64*)(flg + FL_H) + (lane < 14 ? lane : 0), aOwn + 1); lds_set(cnt + C_H, lane); }
  asm volatile("s_waitcnt vmcnt(0)" ::: "memory");
  if (last_of(lds, C_SP, 8, lane) && lane < 32) {
    const u64 ac = *(const u64*)(lds + L_ASC + lane * 8);
    __hip_atomic_store((u64*)(ws + WS_FLAG + (size_t)blk_of(lane, cb) * 1024 + FL_S) + mb, ac + 1, RLX_AGENT);
  }
  PARK(6); PARK_PIN(6);
  STAMP();
  const char* hbase = ws + WS_H + (size_t)(2 * mb) * HMT + 14 * wid * 1024;
  const unsigned hoff0 = lane * 16, hoff1 = lane * 16 + HMT;
  u32x4 hb[7][4];
  f32x16 acc[2][2];
#pragma unroll
  for (int i = 0; i < 16; ++i) { acc[0][0][i] = 0.f; acc[0][1][i] = 0.f; acc[1][0][i] = 0.f; acc[1][1][i] = 0.f; }
  u32x4 xv, bv, sv;
  u64 sf;
  asm volatile("global_load_dwordx4 %0, %1, off" : "=v"(xv) : "v"(x + (size_t)(64 * mb + (tid >> 3)) * D_ + 64 * cb + 8 * (tid & 7)) : "memory");
  asm volatile("global_load_dwordx4 %0, %1, off" : "=v"(bv) : "v"(b2 + 64 * cb + 8 * (tid & 7)) : "memory");
  lds_wait(cnt + C_H);
  STAMP();
#define K2_HLD(S) { gload16(hb[S][0], hoff0, hbase + (S) * 2048); gload16(hb[S][1], hoff1, hbase + (S) * 2048); \
                    gload16(hb[S][2], hoff0, hbase + (S) * 2048 + 1024); gload16(hb[S][3], hoff1, hbase + (S) * 2048 + 1024); }
#define K2_STEP(S, W) { \
    asm volatile("s_waitcnt vmcnt(" #W ")" : "+v"(hb[S][0]), "+v"(hb[S][1]), "+v"(hb[S][2]), "+v"(hb[S][3]) :: "memory"); \
    _Pragma("unroll") for (int s2 = 0; s2 < 2; ++s2) { \
      const s16x8 h0 = __builtin_bit_cast(s16x8, hb[S][2 * s2]), h1 = __builtin_bit_cast(s16x8, hb[S][2 * s2 + 1]); \
      acc[0][0] = __builtin_amdgcn_mfma_f32_32x32x16_bf16(wp[S][2 * s2], h0, acc[0][0], 0, 0, 0); \
      acc[1][0] = __builtin_amdgcn_mfma_f32_32x32x16_bf16(wp[S][2 * s2], h1, acc[1][0], 0, 0, 0); \
      acc[0][1] = __builtin_amdgcn_mfma_f32_32x32x16_bf16(wp[S][2 * s2 + 1], h0, acc[0][1], 0, 0, 0); \
      acc[1][1] = __builtin_amdgcn_mfma_f32_32x32x16_bf16(wp[S][2 * s2 + 1], h1, acc[1][1], 0, 0, 0); } \
    SCHED(); }
  const u64* sfl = (const u64*)(flg + FL_S) + (lane & 31);
  const char* sp = ws + WS_S + (size_t)(64 * cb + 8 * wid) * 4096 + 64 * mb * 2;
  const unsigned svo = (lane >> 3) * 4096 + (lane & 7) * 16;
  K2_HLD(0); K2_HLD(1); K2_HLD(2);
  K2_STEP(0, 8); STAMP(); K2_HLD(3); K2_HLD(4);
  K2_STEP(1, 12); K2_HLD(5); K2_HLD(6);
  ld_u64(sf, sfl);
  K2_STEP(2, 17);
  K2_STEP(3, 13);
  K2_STEP(4, 9);
  K2_STEP(5, 5);
  K2_STEP(6, 1);
  STAMP();
  asm volatile("s_waitcnt vmcnt(0)" : "+v"(sf), "+v"(xv), "+v"(bv) :: "memory");
  if (!__all(sf == aOwn + 1)) poll_flags(sfl, aOwn + 1);
  gload16(sv, svo, sp);
  LGKM_BAR();
  {
    char* dst = lds + L_RED + (wid * 64 + r) * RED_RS + h * 16;
#pragma unroll
    for (int mi = 0; mi < 2; ++mi)
#pragma unroll
      for (int di = 0; di < 2; ++di)
#pragma unroll
        for (int rq = 0; rq < 4; ++rq)
          *(f32x4*)(dst + mi * 32 * RED_RS + di * 128 + rq * 32) = (f32x4){acc[mi][di][4 * rq], acc[mi][di][4 * rq + 1], acc[mi][di][4 * rq + 2], acc[mi][di][4 * rq + 3]};
  }
  SCHED();
  asm volatile("s_waitcnt vmcnt(0)" : "+v"(sv) :: "memory");
  {
    char* dst = lds + L_ST + (8 * (lane & 7)) * ST_RS + (8 * wid + (lane >> 3)) * 2;
#pragma unroll
    for (int c = 0; c < 4; ++c) { *(u16*)(dst + (2 * c) * ST_RS) = (u16)sv[c]; *(u16*)(dst + (2 * c + 1) * ST_RS) = (u16)(sv[c] >> 16); }
  }
  LGKM_BAR();
  STAMP();
  {
    const int t = tid >> 3, dq = tid & 7;
    const char* src = lds + L_RED + t * RED_RS + dq * 32;
    f32x4 a = *(const f32x4*)src, b = *(const f32x4*)(src + 16);
#pragma unroll
    for (int k = 1; k < 8; ++k) { a += *(const f32x4*)(src + k * 64 * RED_RS); b += *(const f32x4*)(src + k * 64 * RED_RS + 16); }
    const u32x4 sq = *(const u32x4*)(lds + L_ST + t * ST_RS + dq * 16);
    u32x4 o;
#pragma unroll
    for (int c = 0; c < 4; ++c) {
      const float v0 = (c < 2 ? a[2 * c] : b[2 * c - 4]) + bflo(bv[c]), v1 = (c < 2 ? a[2 * c + 1] : b[2 * c - 3]) + bfhi(bv[c]);
      const float g0 = v0 * __builtin_amdgcn_rcpf(1.f + __builtin_amdgcn_exp2f(-1.4426950408889634f * v0));
      const float g1 = v1 * __builtin_amdgcn_rcpf(1.f + __builtin_amdgcn_exp2f(-1.4426950408889634f * v1));
      const unsigned pv = pkbf(bflo(sq[c]) * g0, bfhi(sq[c]) * g1);
      o[c] = pkbf(bflo(xv[c]) + bflo(pv), bfhi(xv[c]) + bfhi(pv));
    }
    *(u32x4*)(out + (size_t)(64 * mb + t) * D_ + 64 * cb + 8 * dq) = o;
  }
  if (tid == 0) __hip_atomic_store((u64*)(ws + WS_FLAG + (size_t)blockIdx.x * 1024), aOwn + 1, RLX_AGENT);
  STAMP(); STAMP_FLUSH();
}

template <int PH>
__global__ __launch_bounds__(512, 2) void fused_kernel(const u16* __restrict__ x, const u16* __restrict__ lar, const u16* __restrict__ aim,
                                                       const u16* __restrict__ cre, const u16* __restrict__ cim, const u16* __restrict__ ldt,
                                                       const u16* __restrict__ dsk, const u16* __restrict__ w1, const u16* __restrict__ b1,
                                                       const u16* __restrict__ w2, const u16* __restrict__ b2, u16* __restrict__ out,
                                                       char* __restrict__ ws) {
  extern __shared__ __attribute__((aligned(16))) char lds[];
  const int lane = threadIdx.x & 63;
  const int wid = __builtin_amdgcn_readfirstlane(threadIdx.x >> 6);
  const int b = blockIdx.x, xcd = b & 7, j = b >> 3;
  const int mb = 4 * xcd + (j >> 3), cb = j & 7;
  const int d = 64 * cb + 2 * mb + (wid >> 2);
  u64 aOwn;
  const unsigned sdt = *(const __attribute__((address_space(4))) unsigned*)((const char*)ldt + ((d * 2) & ~3));
  const unsigned sds = *(const __attribute__((address_space(4))) unsigned*)((const char*)dsk + ((d * 2) & ~3));
  const u16* prm = prm_ptr(lar, aim, cre, cim, d, wid & 3, lane);
  const u16* wsrc = w2 + (size_t)(7 * wid * 32) * D_ + 64 * cb;
  if (j < 28) p1_gemm(w2, b2, out, mb, x, w1, b1, prm, wsrc, aOwn, sdt, ws, lds, 4 * (xcd >> 1) + j / 7, 7 * (xcd & 1) + j % 7, cb, d & 1, wid, lane TK0_PASS);
  else p1_aux(x, prm, wsrc, aOwn, sdt, ws, lds, xcd * 4 + (j - 28), cb, d & 1, wid, lane TK0_PASS);
  phase2(x, wsrc, aOwn, (d & 1) ? bfhi(sds) : bflo(sds), b2, out, ws, lds, mb, cb, wid, lane TK0_PASS);
}

extern "C" void kernel_launch(void* const* d_in, const int* in_sizes, int n_in, void* d_out, int out_size, void* d_ws, size_t ws_size,
                              hipStream_t stream) {
  const u16* x = (const u16*)d_in[0];
  const u16* lar = (const u16*)d_in[1];
  const u16* aim = (const u16*)d_in[2];
  const u16* cre = (const u16*)d_in[3];
  const u16* cim = (const u16*)d_in[4];
  const u16* ldt = (const u16*)d_in[5];
  const u16* dsk = (const u16*)d_in[6];
  const u16* w1 = (const u16*)d_in[8];
  const u16* b1 = (const u16*)d_in[9];
  const u16* w2 = (const u16*)d_in[10];
  const u16* b2 = (const u16*)d_in[11];
  (void)hipFuncSetAttribute((const void*)fused_kernel<3>, hipFuncAttributeMaxDynamicSharedMemorySize, LDS_TOTAL);
  fused_kernel<3><<<256, 512, LDS_TOTAL, stream>>>(x, lar, aim, cre, cim, ldt, dsk, w1, b1, w2, b2, (u16*)d_out, (char*)d_ws);
}
```

```cpp
#include <hip/hip_runtime.h>
#include <stdint.h>

typedef unsigned short u16;
typedef unsigned long long u64;
typedef __attribute__((ext_vector_type(4))) short s16x4;
typedef __attribute__((ext_vector_type(8))) short s16x8;
typedef __attribute__((ext_vector_type(8))) _Float16 f16x8;
typedef __attribute__((ext_vector_type(2))) _Float16 f16x2;
typedef __attribute__((ext_vector_type(2))) __bf16 bf16x2;
typedef __attribute__((ext_vector_type(2))) float f32x2;
typedef __attribute__((ext_vector_type(4))) float f32x4;
typedef __attribute__((ext_vector_type(16))) float f32x16;
typedef __attribute__((ext_vector_type(4))) unsigned u32x4;
typedef __attribute__((ext_vector_type(2))) unsigned u32x2;
#define LDSP __attribute__((address_space(3)))
#define GLBP __attribute__((address_space(1)))
#define DI __device__ __forceinline__

#define NLAUNCH 1

#define XPRIO 3
#define L_ 2048
#define D_ 512
#define N_ 128
#define F_ 1792

#define WS_H 0
#define WS_UR (8u << 20)
#define WS_S (10u << 20)
#define WS_STAT (12u << 20)
#define WS_CS ((12u << 20) + 65536)
#define WS_ST2 ((12u << 20) + 131072)
#define WS_FLAG (13u << 20)
#define WS_KAP (16u << 20)
#define FL_LN 128
#define FL_H 384
#define FL_S 512
#define FL_ST 768

#define STAMP_INIT(kid)
#define STAMP()
#define STAMP0()
#define STAMP_DECL
#define STAMP_FLUSH()
#define TK0_ARG
#define TK0_PASS

DI float bf2f(unsigned v) { return __uint_as_float(v << 16); }
DI float bflo(unsigned v) { return __uint_as_float(v << 16); }
DI float bfhi(unsigned v) { return __uint_as_float(v & 0xffff0000u); }
DI unsigned pkbf(float a, float b) { bf16x2 q = {(__bf16)a, (__bf16)b}; return __builtin_bit_cast(unsigned, q); }
DI float rbf(float a) { __bf16 q = (__bf16)a; return (float)q; }
DI unsigned pkh(float a, float b) { f32x2 v = {a, b}; f16x2 p = __builtin_convertvector(v, f16x2); return __builtin_bit_cast(unsigned, p); }
DI float dot2(unsigned a, unsigned b, float c) { return __builtin_amdgcn_fdot2_f32_bf16(__builtin_bit_cast(bf16x2, a), __builtin_bit_cast(bf16x2, b), c, false); }
template <class T> DI T sel(int c, T a, T b) { return c ? a : b; }
DI void dma16(const void* g, char* l) { __builtin_amdgcn_global_load_lds((const GLBP unsigned*)g, (LDSP unsigned*)l, 16, 0, 0); }
DI void dma16s(unsigned voff, const void* sbase, unsigned ldsaddr) {
  asm volatile("s_mov_b32 m0, %2\n\ts_nop 0\n\tglobal_load_lds_dwordx4 %0, %1" :: "v"(voff), "s"(sbase), "s"(ldsaddr) : "memory");
}
DI unsigned lds_base(char* lds) { return __builtin_amdgcn_readfirstlane((unsigned)(size_t)(LDSP char*)lds); }
DI s16x8 tr_frag(const char* p0, const char* p1) {
  s16x4 a = __builtin_amdgcn_ds_read_tr16_b64_v4i16((LDSP s16x4*)p0);
  s16x4 b = __builtin_amdgcn_ds_read_tr16_b64_v4i16((LDSP s16x4*)p1);
  return __builtin_shufflevector(a, b, 0, 1, 2, 3, 4, 5, 6, 7);
}
#define SCHED() __builtin_amdgcn_sched_barrier(0)
#define WAITV_BAR(n) do { SCHED(); asm volatile("s_waitcnt vmcnt(" #n ") lgkmcnt(0)\n\ts_barrier" ::: "memory"); SCHED(); } while (0)
#define LGKM_BAR() do { SCHED(); asm volatile("s_waitcnt lgkmcnt(0)\n\ts_barrier" ::: "memory"); SCHED(); } while (0)
#define RLX_AGENT __ATOMIC_RELAXED, __HIP_MEMORY_SCOPE_AGENT
#define RLX_WG __ATOMIC_RELAXED, __HIP_MEMORY_SCOPE_WORKGROUP

template <int CTRL, int RM> DI float dpp_add(float v) {
  return v + __builtin_bit_cast(float, __builtin_amdgcn_update_dpp(0, __builtin_bit_cast(int, v), CTRL, RM, 0xf, true));
}
DI float wave_sum63(float v) {
  v = dpp_add<0x111, 0xf>(v); v = dpp_add<0x112, 0xf>(v); v = dpp_add<0x114, 0xf>(v); v = dpp_add<0x118, 0xf>(v);
  v = dpp_add<0x142, 0xa>(v); v = dpp_add<0x143, 0xc>(v);
  return v;
}
DI float comb32(float a, float b) { auto r = __builtin_amdgcn_permlane32_swap(__float_as_uint(a), __float_as_uint(b), false, false); return __uint_as_float(r[0]) + __uint_as_float(r[1]); }
DI float comb16(float a, float b) { auto r = __builtin_amdgcn_permlane16_swap(__float_as_uint(a), __float_as_uint(b), false, false); return __uint_as_float(r[0]) + __uint_as_float(r[1]); }
DI float row_allsum(float v) { v = dpp_add<0x128, 0xf>(v); v = dpp_add<0x124, 0xf>(v); v = dpp_add<0x122, 0xf>(v); v = dpp_add<0x121, 0xf>(v); return v; }
DI float rdlane(float v, int l) { return __builtin_bit_cast(float, __builtin_amdgcn_readlane(__builtin_bit_cast(int, v), l)); }

DI void group_barrier(unsigned* cnt, unsigned target, int lane) {
  asm volatile("s_waitcnt lgkmcnt(0)" ::: "memory");
  if (lane == 0) __hip_atomic_fetch_add(cnt, 1u, RLX_WG);
  while (__hip_atomic_load(cnt, RLX_WG) < target) __builtin_amdgcn_s_sleep(1);
  asm volatile("" ::: "memory");
}
DI void poll_flags(const u64* p, u64 expect) {
  for (unsigned spin = 0; spin < (1u << 20); ++spin) {
    const u64 v = __hip_atomic_load(p, RLX_AGENT);
    if (__all(v == expect)) break;
    __builtin_amdgcn_s_sleep(2);
  }
  asm volatile("" ::: "memory");
}
DI void lds_set(unsigned* w, int lane) { if (lane == 0) __hip_atomic_store(w, 1u, RLX_WG); }
DI void lds_wait(unsigned* w) { while (__hip_atomic_load(w, RLX_WG) == 0u) __builtin_amdgcn_s_sleep(1); asm volatile("" ::: "memory"); }
DI int blk_of(int mb, int cb) { return (mb >> 2) + 8 * (((mb & 3) << 3) | cb); }

#define LDS_TOTAL 163840
#define L_STAT 162304
#define L_BIAS 163328
#define L_ACON 163584
#define L_KSC 163712
#define L_CNT 163744
#define KAPSZ 2576
#define L_KAP 141696
#define L_COLS 152000
#define K1_SLOT 32768
#define L_TB 98304
#define L_KP 131584
#define KPSZ 4160
#define K2_R 4
#define L_RING2 0
#define L_W 65536
#define WSZ 33024
#define L_U L_KAP
#define USZ 4608
#define L_RED L_W
#define RED_RS 272
#define L_ST L_KAP
#define ST_RS 144
#define C_PUB 0
#define C_SP 1
#define C_H 2
#define C_S 3
#define C_W2 4
#define C_R1 5
#define C_ST 6
#define C_K0 7
#define C_K1 9
#define C_K2 12
#define C_K3 14
#define C_LN1 16
#define C_LN2 17
#define C_X2 18
#define C_CS 19
#define C_EP 20
DI void arrive(char* lds, int c, int lane) { if (lane == 0) __hip_atomic_fetch_add((unsigned*)(lds + L_CNT) + c, 1u, RLX_WG); }
template <int SL> DI void await_(char* lds, int c, unsigned target) {
  while (__hip_atomic_load((unsigned*)(lds + L_CNT) + c, RLX_WG) < target) __builtin_amdgcn_s_sleep(SL);
  asm volatile("" ::: "memory"); SCHED();
}
#define ARRIVE_V(n, c) do { SCHED(); asm volatile("s_waitcnt vmcnt(" #n ") lgkmcnt(0)" ::: "memory"); arrive(lds, c, lane); SCHED(); } while (0)
#define ARRIVE_L(c) do { SCHED(); asm volatile("s_waitcnt lgkmcnt(0)" ::: "memory"); arrive(lds, c, lane); SCHED(); } while (0)

struct cf { float x, y; };
DI cf cmul(cf a, cf b) { return cf{fmaf(a.x, b.x, -(a.y * b.y)), fmaf(a.x, b.y, a.y * b.x)}; }
DI unsigned pkc(cf a) { return pkh(a.x, a.y); }
DI unsigned pkcc(cf a) { return pkh(a.x, -a.y); }
DI cf zpow(float al, float om, float p) {
  const float m = __builtin_amdgcn_exp2f(al * p), ph = __builtin_amdgcn_fractf(om * p);
  return cf{m * __builtin_amdgcn_cosf(ph), m * __builtin_amdgcn_sinf(ph)};
}
DI unsigned cmulh(unsigned a, unsigned b) {
  const f16x2 z = __builtin_bit_cast(f16x2, a), w = __builtin_bit_cast(f16x2, b);
  const f16x2 t = z * (f16x2){w[0], w[0]};
  const f16x2 r = __builtin_elementwise_fma((f16x2){-z[1], z[0]}, (f16x2){w[1], w[1]}, t);
  return __builtin_bit_cast(unsigned, r);
}
DI u32x4 cmulh4(u32x4 a, u32x4 b) { return (u32x4){cmulh(a[0], b[0]), cmulh(a[1], b[1]), cmulh(a[2], b[2]), cmulh(a[3], b[3])}; }
#define TB_RS 272
#define TB_N 33
DI void kgen_tables(unsigned (&T)[TB_N], float& isc, unsigned raw0, unsigned raw1, float ldtv, char* lds, int cw, int lane) {
  const int src = ((lane & 31) >> 1) * 4, sh = (lane & 1) ? 0 : 16;
  unsigned pv[4];
#pragma unroll
  for (int a = 0; a < 4; ++a) {
    const unsigned lo = (unsigned)__builtin_amdgcn_ds_bpermute(src + 64 * a, (int)raw0), hi = (unsigned)__builtin_amdgcn_ds_bpermute(src + 64 * a, (int)raw1);
    pv[a] = ((lane < 32 ? lo : hi) << sh) & 0xffff0000u;
  }
  const float dt = rbf(__builtin_amdgcn_exp2f(ldtv * 1.4426950408889634f));
  const float ar = rbf(__builtin_amdgcn_exp2f(__uint_as_float(pv[0]) * 1.4426950408889634f));
  const float al = -(ar * dt) * 1.4426950408889634f;
  const float om = (__uint_as_float(pv[1]) * dt) * 0.15915494309189535f;
  const float cr = __uint_as_float(pv[2]) * dt, ci = __uint_as_float(pv[3]) * dt;
  const float sabs = 2.f * rdlane(wave_sum63(fabsf(cr) + fabsf(ci)), 63);
  int ex = (int)((__float_as_uint(sabs) >> 23) & 255);
  ex = ex < 20 ? 20 : (ex > 230 ? 230 : ex);
  const float sc2 = __uint_as_float((unsigned)(268 - ex) << 23);
  isc = __uint_as_float((unsigned)(ex - 13) << 23);
  if (lane == 0) *(float*)(lds + L_KSC + cw * 4) = sabs;
  const cf z1 = zpow(al, om, 1.f), z8 = zpow(al, om, 8.f), z32 = zpow(al, om, 32.f), a1 = zpow(al, om, 128.f), a4 = zpow(al, om, 512.f);
  const unsigned one = 0x00003C00u;
  const cf a2 = cmul(a1, a1), a3 = cmul(a2, a1), a8 = cmul(a4, a4), a12 = cmul(a8, a4);
  T[0] = one; T[1] = pkc(a1); T[2] = pkc(a2); T[3] = pkc(a3);
  T[4] = pkc(a4); T[5] = pkc(cmul(a4, a1)); T[6] = pkc(cmul(a4, a2)); T[7] = pkc(cmul(a4, a3));
  T[8] = pkc(a8); T[9] = pkc(cmul(a8, a1)); T[10] = pkc(cmul(a8, a2)); T[11] = pkc(cmul(a8, a3));
  T[12] = pkc(a12); T[13] = pkc(cmul(a12, a1)); T[14] = pkc(cmul(a12, a2)); T[15] = pkc(cmul(a12, a3));
  T[16] = pkc(cmul(a8, a8));
  const cf e2 = cmul(z1, z1), e3 = cmul(e2, z1), e4 = cmul(e2, e2);
  T[17] = one; T[18] = pkc(z1); T[19] = pkc(e2); T[20] = pkc(e3); T[21] = pkc(e4);
  T[22] = pkc(cmul(e4, z1)); T[23] = pkc(cmul(e4, e2)); T[24] = pkc(cmul(e4, e3));
  const cf c0 = cf{cr * sc2, ci * sc2}, c2 = cmul(c0, cmul(z32, z32));
  T[25] = pkcc(c0); T[26] = pkcc(cmul(c0, z32)); T[27] = pkcc(c2); T[28] = pkcc(cmul(c2, z32));
  const cf z16 = cmul(z8, z8);
  T[29] = one; T[30] = pkcc(z8); T[31] = pkcc(z16); T[32] = pkcc(cmul(z16, z8));
}
#define KG_PARAMS f32x4& kX, f32x4& kY, f32x4& kZ, float isc
DI void kgen_store(KG_PARAMS, char* lds, int wid, int lane) {
  const int i16 = lane & 15, g = lane >> 4;
  float* kap = (float*)(lds + L_KAP + wid * KAPSZ);
  *(f32x4*)(kap + 128 * (g >> 1) + 4 * (g & 1) + 8 * i16) = kX * isc;
  if (g < 2) {
    *(f32x4*)(kap + 256 + 64 * g + 4 * i16) = kY * isc;
#pragma unroll
    for (int r = 0; r < 4; ++r) kap[512 + 16 * (4 * g + r) + i16] = kZ[r] * isc;
  } else {
    *(f32x2*)(kap + 384 + 64 * (g - 2) + 2 * i16) = (f32x2){kY[0] * isc, kY[1] * isc};
    *(f32x2*)(kap + 384 + 64 * (g - 2) + 32 + 2 * i16) = (f32x2){kY[2] * isc, kY[3] * isc};
    if (g == 2 && i16 == 0) kap[640] = kZ[0] * isc;
  }
}
DI const u16* prm_ptr(const u16* lar, const u16* aim, const u16* cre, const u16* cim, int d, int hf, int lane) {
  const int g = lane >> 4;
  const u16* p = g == 0 ? lar : (g == 1 ? aim : (g == 2 ? cre : cim));
  return p + d * N_ + 64 * hf + 2 * (lane & 15);
}
DI void ld_u32(unsigned& v, const void* p) { asm volatile("global_load_dword %0, %1, off" : "=v"(v) : "v"(p) : "memory"); }
DI void ld_u64(u64& v, const void* p) { asm volatile("global_load_dwordx2 %0, %1, off sc1" : "=v"(v) : "v"(p) : "memory"); }
DI void gload16(u32x4& v, unsigned voff, const char* sbase) {
  asm volatile("global_load_dwordx4 %0, %1, %2 sc1" : "=v"(v) : "v"(voff), "s"(sbase) : "memory");
}
DI u64 rdlane64(u64 v, int l) { return (u64)(unsigned)__builtin_amdgcn_readlane((int)(unsigned)v, l) | ((u64)(unsigned)__builtin_amdgcn_readlane((int)(unsigned)(v >> 32), l) << 32); }
DI bool last_of(char* lds, int c, unsigned n, int lane) {
  unsigned old = 0;
  if (lane == 0) old = __hip_atomic_fetch_add((unsigned*)(lds + L_CNT) + c, 1u, RLX_WG);
  return (unsigned)__builtin_amdgcn_readfirstlane(old) == n - 1;
}
DI float gelu_f(float v) {
  const float c0 = -2.302208198f;
  const float c1 = -0.1029432397f;
  const float z = v * fmaf(v * v, c1, c0);
  return v * __builtin_amdgcn_rcpf(1.f + __builtin_amdgcn_exp2f(z));
}
#define K2S(S) ((S) + r2 >= 14 ? (S) + r2 - 14 : (S) + r2)
#define K2_DMA(S) { _Pragma("unroll") for (int pp = 0; pp < 4; ++pp) dma16s(wvoff, wsrc + (size_t)(K2S(S) * 32 + pp * 8) * D_, ringa + ((S) % K2_R) * 4096 + pp * 1024); SCHED(); }
#define K2_SETUP(kq_) int lane2_ = lane; asm volatile("" : "+v"(lane2_)); \
  const unsigned wvoff = (lane2_ >> 3) * (D_ * 2) + (((lane2_ & 7) ^ (((lane2_ >> 4) & 1) << 2)) << 4); \
  const unsigned ringa = __builtin_amdgcn_readfirstlane(lds_base(lds) + L_RING2 + (kq_) * (K2_R * 4096)); \
  const u16* wsrc = w2 + (size_t)(14 * (kq_) * 32) * D_ + 64 * cb; \
  const int r2 = (2 * cb + 4 * (mb & 3)) % 14;
#define HMT 114688

DI void p1_gemm(const u16* __restrict__ x, const u16* __restrict__ w1, char* __restrict__ ws, char* lds, int panel, int fs, int wid, int lane TK0_ARG) {
  STAMP_DECL STAMP_INIT(0) STAMP0();
  const int wm = wid >> 2, wf = (wid >> 1) & 1, kh = wid & 1;
  const int r = lane & 31, h = lane >> 5;
  const int row0 = panel * 128, f0 = fs * 128;
  const unsigned xv0 = (lane >> 3) * 1024 + (((lane & 7) ^ ((lane >> 4) & 3)) << 4);
  const unsigned xv1 = xv0 ^ 64;
  const unsigned wv = (lane >> 4) * (F_ * 2) + (((lane & 15) ^ (((lane >> 4) & 3) << 2)) << 4);
  const u16* xsb = x + (size_t)(row0 + 16 * wid) * D_;
  const u16* wsb = w1 + (size_t)(8 * wid) * F_ + f0;
  const unsigned la = lds_base(lds) + wid * 2048;
  const int rot = 2 * (panel & 3) + (fs >= 7 ? fs - 7 : fs);
#define KS(S) (((S) + rot) & 7)
#define K1_D0(S) { dma16s(xv0, xsb + KS(S) * 64, la + ((S) % 4) * K1_SLOT); SCHED(); }
#define K1_D1(S) { dma16s(xv1, xsb + 8 * D_ + KS(S) * 64, la + ((S) % 4) * K1_SLOT + 1024); SCHED(); }
#define K1_D2(S) { dma16s(wv, wsb + (size_t)(KS(S) * 64) * F_, la + ((S) % 4) * K1_SLOT + 16384); SCHED(); }
#define K1_D3(S) { dma16s(wv, wsb + (size_t)(KS(S) * 64 + 4) * F_, la + ((S) % 4) * K1_SLOT + 16384 + 1024); SCHED(); }
#define K1_ISSUE(S) { K1_D0(S); K1_D1(S); K1_D2(S); K1_D3(S); }
  K1_ISSUE(0); K1_ISSUE(1); K1_ISSUE(2); K1_ISSUE(3);
  const int xoff = (64 * wm + r) * 128 + (((4 * kh + 2 * wm + h) ^ ((r >> 1) & 7)) << 4);
  const int q = (lane & 15) >> 2, p = lane & 3;
  const int woff0 = 16384 + (32 * kh + 16 * wm + 8 * h + q) * 256 + (((2 * wf + kh) ^ q) << 6) + ((lane >> 4) & 1) * 32 + 8 * p;
  f32x16 acc[2][2];
#pragma unroll
  for (int i = 0; i < 16; ++i) { acc[0][0][i] = 0.f; acc[0][1][i] = 0.f; acc[1][0][i] = 0.f; acc[1][1][i] = 0.f; }
  STAMP();
  s16x8 xf[2][2][2], wfr[2][2][2];
#define K1_M(T, s, mi, fi) { acc[mi][fi] = __builtin_amdgcn_mfma_f32_32x32x16_bf16(wfr[(T) & 1][s][fi], xf[(T) & 1][s][mi], acc[mi][fi], 0, 0, 0); SCHED(); }
#define K1_RX(T, s, mi) { xf[(T) & 1][s][mi] = *(const s16x8*)(lds + (xa_ ^ ((s) << 5)) + (mi) * 4096); SCHED(); }
#define K1_RW(T, s, fi) { wfr[(T) & 1][s][fi] = tr_frag(lds + (wa_ ^ (((fi) << 6) | ((s) << 12))), lds + (wa_ ^ (((fi) << 6) | ((s) << 12))) + 1024); SCHED(); }
#define K1_ADDR(T) int xa_ = xoff + ((T) % 4) * K1_SLOT, wa_ = woff0 + ((T) % 4) * K1_SLOT; asm volatile("" : "+v"(xa_), "+v"(wa_));
#define K1_READ(T) { K1_ADDR(T) K1_RX(T,0,0); K1_RW(T,0,0); K1_RW(T,0,1); K1_RX(T,0,1); K1_RX(T,1,0); K1_RW(T,1,0); K1_RW(T,1,1); K1_RX(T,1,1); }
#define K1_MMA(T) { K1_M(T,0,0,0); K1_M(T,0,0,1); K1_M(T,0,1,0); K1_M(T,0,1,1); K1_M(T,1,0,0); K1_M(T,1,0,1); K1_M(T,1,1,0); K1_M(T,1,1,1); }
#define K1_STAGE(T, DM0, DM1, DM2, DM3) { K1_ADDR(T) \
    K1_M((T)-1,0,0,0); K1_RX(T,0,0); K1_RW(T,0,0); K1_M((T)-1,0,0,1); K1_RW(T,0,1); K1_RX(T,0,1); \
    K1_M((T)-1,0,1,0); K1_RX(T,1,0); K1_RW(T,1,0); K1_M((T)-1,0,1,1); K1_RW(T,1,1); K1_RX(T,1,1); \
    K1_M((T)-1,1,0,0); DM0; K1_M((T)-1,1,0,1); DM1; K1_M((T)-1,1,1,0); DM2; K1_M((T)-1,1,1,1); DM3; }
#define K1_STAGE_D(T, S) K1_STAGE(T, K1_D0(S), K1_D1(S), K1_D2(S), K1_D3(S))
  WAITV_BAR(12); STAMP();
  K1_READ(0);
  WAITV_BAR(8); K1_STAGE_D(1, 4);
  WAITV_BAR(8); K1_STAGE_D(2, 5);
  WAITV_BAR(8); K1_STAGE_D(3, 6);
  WAITV_BAR(8); STAMP(); K1_STAGE_D(4, 7);
  WAITV_BAR(8); K1_STAGE(5, , , , );
  WAITV_BAR(4); K1_STAGE(6, , , , );
  K1_MMA(6);
  WAITV_BAR(0);
  K1_READ(7); K1_MMA(7);
  STAMP();
  float* colS = (float*)(lds + L_COLS);
  {
    char* dst = lds + wid * 8192 + lane * 16;
#pragma unroll
    for (int mi = 0; mi < 2; ++mi)
#pragma unroll
      for (int rq = 0; rq < 4; ++rq)
        *(f32x4*)(dst + (mi * 4 + rq) * 1024) = (f32x4){acc[mi][1][4 * rq], acc[mi][1][4 * rq + 1], acc[mi][1][4 * rq + 2], acc[mi][1][4 * rq + 3]};
  }
  LGKM_BAR();
  STAMP();
  f32x4 pa[2][4], cq[4];
  f32x2 st[2];
  u32x2 bq[4];
  {
    const char* src = lds + (wid ^ 1) * 8192 + lane * 16;
    const int fl = 64 * wf + 32 * kh + 4 * h;
#pragma unroll
    for (int rq = 0; rq < 4; ++rq) {
      cq[rq] = *(const f32x4*)(colS + fl + 8 * rq);
      bq[rq] = *(const u32x2*)(lds + L_BIAS + (fl + 8 * rq) * 2);
      pa[0][rq] = *(const f32x4*)(src + rq * 1024); pa[1][rq] = *(const f32x4*)(src + (4 + rq) * 1024);
    }
    st[0] = *(const f32x2*)(lds + L_STAT + (64 * wm + r) * 8); st[1] = *(const f32x2*)(lds + L_STAT + (64 * wm + 32 + r) * 8);
  }
  LGKM_BAR();
  {
    const __amdgpu_buffer_rsrc_t hr = __builtin_amdgcn_make_buffer_rsrc(ws + WS_H, 0, L_ * F_ * 2, 0x00020000);
    const unsigned hoff = (unsigned)(panel * 4 + 2 * wm) * HMT + (8 * fs + 4 * wf + 2 * kh) * 1024 + lane * 16;
#pragma unroll
    for (int mi = 0; mi < 2; ++mi) {
      const float mr = st[mi][0], rinv = st[mi][1];
#pragma unroll
      for (int s2 = 0; s2 < 2; ++s2) {
        u32x4 pk;
#pragma unroll
        for (int qq = 0; qq < 2; ++qq) {
          const int rq = 2 * s2 + qq;
          const float b0 = bflo(bq[rq][0]), b1_ = bfhi(bq[rq][0]), b2_ = bflo(bq[rq][1]), b3 = bfhi(bq[rq][1]);
          const float o0 = gelu_f(fmaf(acc[mi][0][4 * rq] + pa[mi][rq][0], rinv, fmaf(mr, cq[rq][0], b0)));
          const float o1 = gelu_f(fmaf(acc[mi][0][4 * rq + 1] + pa[mi][rq][1], rinv, fmaf(mr, cq[rq][1], b1_)));
          const float o2 = gelu_f(fmaf(acc[mi][0][4 * rq + 2] + pa[mi][rq][2], rinv, fmaf(mr, cq[rq][2], b2_)));
          const float o3 = gelu_f(fmaf(acc[mi][0][4 * rq + 3] + pa[mi][rq][3], rinv, fmaf(mr, cq[rq][3], b3)));
          pk[2 * qq] = pkbf(o0, o1); pk[2 * qq + 1] = pkbf(o2, o3);
        }
        __builtin_amdgcn_raw_buffer_store_b128(pk, hr, hoff + mi * HMT + s2 * 1024, 0, 16);
      }
    }
  }
  STAMP();
  asm volatile("s_waitcnt vmcnt(0)" ::: "memory");
  if (last_of(lds, C_PUB, 8, lane) && lane < 16) {
    const u64 ac = *(const u64*)(lds + L_ACON + lane * 8);
    int l2 = lane; asm volatile("" : "+v"(l2));
    __hip_atomic_store((u64*)(ws + WS_FLAG + (size_t)blk_of(2 * panel + ((l2 >> 3) & 1), l2 & 7) * 1024 + FL_H) + fs, ac + 1, RLX_AGENT);
  }
  STAMP(); STAMP_FLUSH();
}

#define LN_RS 1040
DI void p1_aux(const u16* __restrict__ x, char* __restrict__ ws, char* lds, int idx, int wid, int lane TK0_ARG) {
  STAMP_DECL STAMP_INIT(0) STAMP0();
  const int t0 = idx * 64;
  u32x4 v[8];
#pragma unroll
  for (int i = 0; i < 8; ++i) v[i] = *(const u32x4*)(x + (size_t)(t0 + 8 * wid + i) * D_ + lane * 8);
  const u64 aCon = __hip_atomic_load((const u64*)(ws + WS_FLAG + (size_t)((wid * 64 + lane) & 255) * 1024), RLX_AGENT);
  const int sfs = lane < 14 ? lane : 0;
  const int sblk = 2 * (idx >> 3) + (sfs >= 7 ? 1 : 0) + 8 * (((idx >> 1) & 3) * 7 + (sfs >= 7 ? sfs - 7 : sfs));
  const u64 aSt = __hip_atomic_load((const u64*)(ws + WS_FLAG + (size_t)sblk * 1024), RLX_AGENT);
  SCHED();
  asm volatile("s_barrier" ::: "memory");
  SCHED();
#pragma unroll
  for (int i = 0; i < 8; ++i) *(u32x4*)(lds + (63 - (8 * wid + i)) * LN_RS + lane * 16) = v[i];
  ARRIVE_L(C_LN1);
  float sA[8], sB[8];
#pragma unroll
  for (int i = 0; i < 8; ++i) {
    float a = dot2(v[i][0], 0x3F803F80u, 0.f), b = dot2(v[i][0], v[i][0], 0.f);
#pragma unroll
    for (int c = 1; c < 4; ++c) { a = dot2(v[i][c], 0x3F803F80u, a); b = dot2(v[i][c], v[i][c], b); }
    sA[i] = a; sB[i] = b;
  }
#pragma unroll
  for (int i = 0; i < 4; ++i) { sA[i] = comb32(sA[i], sA[i + 4]); sB[i] = comb32(sB[i], sB[i + 4]); }
#pragma unroll
  for (int i = 0; i < 2; ++i) { sA[i] = row_allsum(comb16(sA[i], sA[i + 2])); sB[i] = row_allsum(comb16(sB[i], sB[i + 2])); }
  const float mv = ((lane & 1) ? sA[1] : sA[0]) * (1.f / 512.f);
  const float var = fmaxf(fmaf(-mv, mv, ((lane & 1) ? sB[1] : sB[0]) * (1.f / 512.f)), 0.f);
  const float mbv = rbf(mv);
  const float sd = rbf(sqrtf(rbf(rbf(var) + 1e-5f)));
  const float rv = 1.f / sd;
  if ((lane & 14) == 0) {
    const u64 pk = (u64)__float_as_uint(-mbv * rv) | ((u64)__float_as_uint(rv) << 32);
    const int tt = t0 + 8 * wid + (lane & 1) + 2 * (lane >> 4);
    __hip_atomic_store((u64*)(ws + WS_STAT) + tt, pk, RLX_AGENT);
    __hip_atomic_store((unsigned*)(ws + WS_ST2) + (L_ - 1 - tt), (__float_as_uint(mbv) >> 16) | __float_as_uint(sd), RLX_AGENT);
  }
  STAMP();
  await_<0>(lds, C_LN1, 8);
  char* t2 = lds + 64 * LN_RS + wid * 8192;
  const int g = lane >> 4, q = (lane & 15) >> 2, p = lane & 3, dl = lane & 15;
  s16x8 f[8];
#pragma unroll
  for (int i = 0; i < 8; ++i) {
    const char* a = lds + (8 * (4 * (i & 1) + g) + q) * LN_RS + (64 * wid + 16 * (i >> 1) + 4 * p) * 2;
    f[i] = tr_frag(a, a + 4 * LN_RS);
  }
  SCHED();
#pragma unroll
  for (int i = 0; i < 8; ++i) {
    const int dd2 = 16 * (i >> 1) + dl, rc = 4 * (i & 1) + g;
    *(s16x8*)(t2 + dd2 * 128 + ((rc ^ (dd2 & 7)) << 4)) = f[i];
  }
  SCHED();
  const __amdgpu_buffer_rsrc_t ur = __builtin_amdgcn_make_buffer_rsrc(ws + WS_UR, 0, L_ * D_ * 2, 0x00020000);
  const int x0 = L_ - 64 - t0;
  u32x4 o[8];
#pragma unroll
  for (int i = 0; i < 8; ++i) {
    const int dd2 = 8 * i + (lane >> 3), c = lane & 7;
    o[i] = *(const u32x4*)(t2 + dd2 * 128 + ((c ^ (dd2 & 7)) << 4));
  }
  SCHED();
  asm volatile("s_waitcnt vmcnt(0)" ::: "memory");
  if (last_of(lds, C_PUB, 8, lane) && lane < 14) __hip_atomic_store((u64*)(ws + WS_FLAG + (size_t)sblk * 1024 + FL_ST) + (idx & 1), aSt + 1, RLX_AGENT);
  STAMP();
#pragma unroll
  for (int i = 0; i < 8; ++i)
    __builtin_amdgcn_raw_buffer_store_b128(o[i], ur, (unsigned)(64 * wid + 8 * i + (lane >> 3)) * 4096 + x0 * 2 + (lane & 7) * 16, 0, 16);
  STAMP();
  ARRIVE_L(C_X2);
  ARRIVE_V(0, C_LN2);
  if (wid < 4) {
    await_<0>(lds, C_LN2, 8);
    __hip_atomic_store((u64*)(ws + WS_FLAG + (size_t)(wid * 64 + lane) * 1024 + FL_LN) + idx, aCon + 1, RLX_AGENT);
  }
  STAMP(); STAMP_FLUSH();
}

DI void x_wave(const u16* __restrict__ lar, const u16* __restrict__ aim, const u16* __restrict__ cre, const u16* __restrict__ cim,
               unsigned sdt, unsigned sds, const u16* __restrict__ w2, const u16* __restrict__ w1, const u16* __restrict__ b1, int auxidx, int panel, int fs,
               char* __restrict__ ws, char* lds, int mb, int cb, int cw, int lane TK0_ARG) {
  STAMP_DECL STAMP_INIT(1) STAMP0();
  __builtin_amdgcn_s_setprio(3);
  const bool aux = auxidx >= 0;
  const int ch = cw >> 1, hf = cw & 1;
  const int d = 64 * cb + 2 * mb + ch;
  const int i16 = lane & 15, g = lane >> 4;
  unsigned* cnt = (unsigned*)(lds + L_CNT);
  const char* flg = ws + WS_FLAG + (size_t)blockIdx.x * 1024;
  const u16* pp_ = prm_ptr(lar, aim, cre, cim, d, hf, lane);
  unsigned raw0, raw1;
  u64 aS, aH;
  ld_u32(raw0, pp_); ld_u32(raw1, pp_ + 32);
  ld_u64(aS, ws + WS_FLAG + (size_t)(lane >= 32 ? (int)blockIdx.x : blk_of(lane, cb)) * 1024);
  ld_u64(aH, ws + WS_FLAG + (size_t)blk_of(2 * panel + ((lane >> 3) & 1), lane & 7) * 1024);
  if (!aux && cw == 2) asm volatile("s_mov_b32 m0, %2\n\ts_nop 0\n\tglobal_load_lds_dword %0, %1" :: "v"(lane * 4), "s"(b1 + fs * 128), "s"(lds_base(lds) + L_BIAS) : "memory");
  if (cw == 0 && lane < 24) cnt[lane] = 0u;
  if (aux) {
    asm volatile("s_waitcnt lgkmcnt(0)\n\ts_barrier" ::: "memory");
    __builtin_amdgcn_s_setprio(1);
    if (auxidx < 28) {
      const u16* wp_ = w1 + (size_t)(128 * cw + (lane >> 3)) * F_ + 64 * auxidx + 8 * (lane & 7);
      const int cblk = 2 * ((lane & 15) >> 2) + (auxidx >= 14 ? 1 : 0) + 8 * ((lane & 3) * 7 + ((auxidx >> 1) % 7));
      const u64 aC = __hip_atomic_load((const u64*)(ws + WS_FLAG + (size_t)cblk * 1024), RLX_AGENT);
      float a8[8] = {0.f, 0.f, 0.f, 0.f, 0.f, 0.f, 0.f, 0.f};
      u32x4 wv[16];
#pragma unroll
      for (int i = 0; i < 16; ++i) wv[i] = *(const u32x4*)(wp_ + (size_t)(8 * i) * F_);
      SCHED();
#pragma unroll
      for (int i = 0; i < 16; ++i)
#pragma unroll
        for (int c = 0; c < 4; ++c) { a8[2 * c] += bflo(wv[i][c]); a8[2 * c + 1] += bfhi(wv[i][c]); }
#pragma unroll
      for (int i = 0; i < 4; ++i) a8[i] = comb32(a8[i], a8[i + 4]);
#pragma unroll
      for (int i = 0; i < 2; ++i) a8[i] = dpp_add<0x128, 0xf>(comb16(a8[i], a8[i + 2]));
      if (!(lane & 8)) *(f32x2*)(lds + L_COLS + cw * 256 + (8 * (lane & 7) + 2 * (g & 1) + 4 * (g >> 1)) * 4) = (f32x2){a8[0], a8[1]};
      SCHED(); asm volatile("s_waitcnt lgkmcnt(0)" ::: "memory");
      if (last_of(lds, C_CS, 4, lane)) {
        const float* cp = (const float*)(lds + L_COLS);
        const float tot = (cp[lane] + cp[64 + lane]) + (cp[128 + lane] + cp[192 + lane]);
        __hip_atomic_store((float*)(ws + WS_CS) + 64 * auxidx + lane, tot, RLX_AGENT);
        asm volatile("s_waitcnt vmcnt(0)" ::: "memory");
        if (lane < 16) __hip_atomic_store((u64*)(ws + WS_FLAG + (size_t)cblk * 1024 + FL_ST) + 2 + (auxidx & 1), aC + 1, RLX_AGENT);
      }
    }
  }
  asm volatile("s_waitcnt vmcnt(2)" : "+v"(raw0), "+v"(raw1) :: "memory");
  float isc;
  unsigned T[TB_N];
  kgen_tables(T, isc, raw0, raw1, (d & 1) ? bfhi(sdt) : bflo(sdt), lds, cw, lane);
#pragma unroll
  for (int i = 0; i < TB_N; ++i) asm volatile("" : "+v"(T[i]));
  asm volatile("s_waitcnt vmcnt(0)" : "+v"(aS), "+v"(aH) :: "memory");
  const u64 aOwn = rdlane64(aS, 32);
  if (cw == 1 && lane < 16) *(u64*)(lds + L_ACON + lane * 8) = aH;
  if (cw == 3) { if (lane == 0) *(u64*)(lds + L_KSC + 16) = aOwn; ARRIVE_L(C_EP); }
  __builtin_amdgcn_s_setprio(1);
  STAMP();
  const u64* lnfl = (const u64*)(flg + FL_LN) + (lane & 31);
  const char* up = ws + WS_UR + (size_t)d * 4096 + hf * 2048;
  u64 fv;
  if (aux) {
    await_<2>(lds, C_X2, 8);
  } else {
    const u64* stfl = (const u64*)(flg + FL_ST) + (lane & 3);
    const char* stp = cw == 1 ? ws + WS_CS + fs * 512 : ws + WS_STAT + (size_t)panel * 1024;
    const unsigned svo = cw == 1 ? (lane & 31) * 16 : lane * 16;
    u32x4 stv;
#define XBAR() do { SCHED(); asm volatile("s_waitcnt lgkmcnt(0)\n\ts_barrier" ::: "memory"); SCHED(); } while (0)
    if (cw < 2) {
      bool ok = false, got = false;
      XBAR(); XBAR(); XBAR(); XBAR();
      ld_u64(fv, stfl); gload16(stv, svo, stp);
#define XPOLL() do { XBAR(); asm volatile("s_waitcnt vmcnt(0)" : "+v"(fv), "+v"(stv) :: "memory"); \
        if (ok && !got) { *(u32x4*)(lds + (cw == 1 ? L_COLS : L_STAT) + svo) = stv; got = true; } \
        ok = __all(fv == aOwn + 1); SCHED(); ld_u64(fv, stfl); gload16(stv, svo, stp); } while (0)
      XPOLL(); XPOLL(); XPOLL(); XPOLL();
      asm volatile("s_waitcnt vmcnt(0)" : "+v"(fv), "+v"(stv) :: "memory");
      if (!got) {
        if (!ok) {
          poll_flags(stfl, aOwn + 1);
          asm volatile("global_load_dwordx4 %0, %1, %2 sc1\n\ts_waitcnt vmcnt(0)" : "=&v"(stv) : "v"(svo), "s"(stp) : "memory");
        }
        *(u32x4*)(lds + (cw == 1 ? L_COLS : L_STAT) + svo) = stv;
      }
    } else {
      XBAR(); XBAR(); XBAR(); XBAR(); XBAR(); XBAR(); XBAR(); XBAR();
    }
    XBAR();
    XBAR();
  }
  STAMP();
  __builtin_amdgcn_s_setprio(3);
  K2_SETUP(cw);
  K2_DMA(0); K2_DMA(1);
  char* wl = lds + L_W + ch * WSZ;
  {
    char* tb = wl + hf * 9216;
#pragma unroll
    for (int i = 0; i < TB_N; ++i) *(unsigned*)(tb + i * TB_RS + lane * 4) = T[i];
    const char* tr = tb + g * 16;
    const int rXa = (i16 >> 3) * TB_RS, rXe = (17 + (i16 & 7)) * TB_RS;
    const int rYa = (i16 < 8 ? 2 + (i16 >> 2) : 4 + ((i16 - 8) >> 1)) * TB_RS, rYe = (17 + (i16 < 8 ? 2 * (i16 & 3) : 4 * (i16 & 1))) * TB_RS;
    const int rZa = (i16 < 8 ? 8 + i16 : 16) * TB_RS;
    const int rB1 = (25 + (i16 >> 2)) * TB_RS, rB0 = (29 + (i16 & 3)) * TB_RS;
    f32x4 kX = {0.f, 0.f, 0.f, 0.f}, kY = kX, kZ = kX;
#pragma unroll
    for (int s = 0; s < 4; ++s) {
      const u32x4 fB = cmulh4(*(const u32x4*)(tr + rB1 + s * 64), *(const u32x4*)(tr + rB0 + s * 64));
      const u32x4 fX = cmulh4(*(const u32x4*)(tr + rXa + s * 64), *(const u32x4*)(tr + rXe + s * 64));
      const u32x4 fY = cmulh4(*(const u32x4*)(tr + rYa + s * 64), *(const u32x4*)(tr + rYe + s * 64));
      const u32x4 fZ = *(const u32x4*)(tr + rZa + s * 64);
      const f16x8 hb = __builtin_bit_cast(f16x8, fB);
      kX = __builtin_amdgcn_mfma_f32_16x16x32_f16(__builtin_bit_cast(f16x8, fX), hb, kX, 0, 0, 0);
      kY = __builtin_amdgcn_mfma_f32_16x16x32_f16(__builtin_bit_cast(f16x8, fY), hb, kY, 0, 0, 0);
      kZ = __builtin_amdgcn_mfma_f32_16x16x32_f16(__builtin_bit_cast(f16x8, fZ), hb, kZ, 0, 0, 0);
    }
    kgen_store(kX, kY, kZ, isc, lds, cw, lane);
  }
  ARRIVE_L(C_K0 + ch);
  ld_u64(fv, lnfl);
  await_<0>(lds, C_K0 + ch, 2);
  const f32x2 ks = *(const f32x2*)(lds + L_KSC + ch * 8);
  int ex = (int)((__float_as_uint(ks[0] + ks[1]) >> 23) & 255);
  ex = ex < 20 ? 20 : (ex > 230 ? 230 : ex);
  const float scale = __uint_as_float((unsigned)(267 - ex) << 23);
  const float iscale = __uint_as_float((unsigned)(ex - 13) << 23);
  char* kp = lds + L_KP + ch * KPSZ;
  {
    const float* k0 = (const float*)(lds + L_KAP + (2 * ch) * KAPSZ);
#define KSUM2(T, i) ((*(const T*)(k0 + (i)) + *(const T*)(k0 + 644 + (i))) * scale)
    const int A = 64 * hf + lane, A2 = 128 + A;
    const float e0 = KSUM2(float, 512 + A), e1 = KSUM2(float, 513 + A);
    u32x4 o;
    if (hf == 0) {
      const int i0 = lane < 32 ? 8 * A : 256 + 4 * (A - 32);
      const f32x4 a = KSUM2(f32x4, i0), b = KSUM2(f32x4, i0 + 4);
      const bool lo = lane < 32;
      o = (u32x4){pkh(a[0], lo ? a[1] : a[0]), pkh(lo ? a[2] : a[1], lo ? a[3] : a[2]), pkh(lo ? b[0] : a[2], lo ? b[1] : a[2]), pkh(lo ? b[2] : a[3], lo ? b[3] : b[0])};
      if (lane < 2) *(u32x4*)(kp + lane * 16) = (u32x4){0u, 0u, 0u, 0u};
    } else {
      const int i0 = 384 + 2 * lane;
      const f32x2 a = KSUM2(f32x2, i0), b = KSUM2(f32x2, i0 + 2);
      o = (u32x4){pkh(a[0], a[0]), pkh(a[0], a[1]), pkh(a[1], a[1]), pkh(b[0], b[0])};
    }
    *(u32x4*)(kp + 32 + A * 16) = o;
    const unsigned p00 = pkh(e0, e0);
    *(u32x4*)(kp + 32 + A2 * 16) = (u32x4){p00, p00, pkh((A2 & 1) ? e1 : e0, e1), pkh(e1, e1)};
  }
  ARRIVE_L(C_K1);
  asm volatile("s_waitcnt vmcnt(0)" : "+v"(fv) :: "memory");
  arrive(lds, C_W2, lane);
  const bool urok = __all(fv == aOwn + 1);
  u32x4 ua_, ub_, q0, q1, q2, q3;
  const char* qp = ws + WS_ST2 + hf * 4096;
#define U_LOADS() { gload16(ua_, lane * 16, up); gload16(ub_, lane * 16, up + 1024); \
    gload16(q0, lane * 32, qp); gload16(q1, lane * 32, qp + 16); gload16(q2, lane * 32, qp + 2048); gload16(q3, lane * 32, qp + 2064); }
#define U_WAIT() asm volatile("s_waitcnt vmcnt(0)" : "+v"(ua_), "+v"(ub_), "+v"(q0), "+v"(q1), "+v"(q2), "+v"(q3) :: "memory")
  U_LOADS();
  await_<0>(lds, C_K1, 4);
  STAMP();
#pragma unroll
  for (int rnd = 0; rnd < 3; ++rnd) {
    const int a = rnd < 2 ? 128 * rnd + 64 * hf + lane : 256;
    if (rnd == 2 && !(hf == 0 && lane == 0)) break;
    const u32x4 c0 = *(const u32x4*)(kp + a * 16), c1 = *(const u32x4*)(kp + a * 16 + 16);
    const unsigned e[8] = {c0[0], c0[1], c0[2], c0[3], c1[0], c1[1], c1[2], c1[3]};
    unsigned o[7];
#pragma unroll
    for (int i = 0; i < 7; ++i) o[i] = __builtin_amdgcn_alignbit(e[i + 1], e[i], 16);
    const int m = (a & 1) | (((a >> 2) & 1) << 1) | (((a >> 1) & 1) << 2);
    char* base = wl + a * 128;
#pragma unroll
    for (int c = 0; c < 8; ++c) {
      u32x4 wv;
      if (c & 1) { const int s = (c + 1) >> 1; wv = (u32x4){e[s], e[s + 1], e[s + 2], e[s + 3]}; }
      else { const int s = c >> 1; wv = (u32x4){o[s], o[s + 1], o[s + 2], o[s + 3]}; }
      *(u32x4*)(base + ((c ^ m) << 4)) = wv;
    }
  }
  SCHED();
  U_WAIT();
  if (!urok) { poll_flags(lnfl, aOwn + 1); U_LOADS(); U_WAIT(); }
  char* ul = lds + L_U + ch * USZ;
  {
#define UN2(xw, s0, s1) ({ const unsigned a_ = pkbf(bflo(xw) - bflo(s0), bfhi(xw) - bflo(s1)); \
      const unsigned b_ = pkbf(bflo(a_) * __builtin_amdgcn_rcpf(bfhi(s0)), bfhi(a_) * __builtin_amdgcn_rcpf(bfhi(s1))); pkh(bflo(b_), bfhi(b_)); })
    const u32x4 o0 = {UN2(ua_[0], q0[0], q0[1]), UN2(ua_[1], q0[2], q0[3]), UN2(ua_[2], q1[0], q1[1]), UN2(ua_[3], q1[2], q1[3])};
    const u32x4 o1 = {UN2(ub_[0], q2[0], q2[1]), UN2(ub_[1], q2[2], q2[3]), UN2(ub_[2], q3[0], q3[1]), UN2(ub_[3], q3[2], q3[3])};
    *(u32x4*)(ul + hf * 2048 + lane * 16) = o0;
    *(u32x4*)(ul + hf * 2048 + 1024 + lane * 16) = o1;
  }
  if (hf == 0 && lane < 32) *(u32x4*)(ul + 4096 + lane * 16) = (u32x4){0u, 0u, 0u, 0u};
  ARRIVE_L(C_K2 + ch);
  STAMP();
  await_<2>(lds, C_PUB, 8);
  await_<0>(lds, C_K2 + ch, 2);
  __builtin_amdgcn_s_setprio(3);
  STAMP();
  f32x4 acc[8];
#pragma unroll
  for (int i = 0; i < 8; ++i) acc[i] = (f32x4){0.f, 0.f, 0.f, 0.f};
  {
    const int c = i16 + 8 * g;
    int fo[2];
#pragma unroll
    for (int par = 0; par < 2; ++par) {
      const int w = 32 * par + c;
      const int m = ((w >> 3) & 1) | (((w >> 5) & 1) << 1) | (((w >> 4) & 1) << 2);
      fo[par] = (w ^ m) << 4;
    }
    const char* fbase = wl + hf * (128 * 16);
    const char* gbase = ul + (2032 - 16 * i16 + 8 * g + 128 * hf) * 2 - 7 * 512;
#define CF(s, e) (*(const f16x8*)(fbase + fo[(s) & 1] + ((s) >> 1) * 1024 + (e) * 4096))
#define CG(s, mu) (*(const f16x8*)(gbase + (s) * 64 + (7 - (mu)) * 512))
    f16x8 F[2][8], G[2];
#pragma unroll
    for (int e = 0; e < 8; ++e) F[0][e] = CF(0, e);
    G[0] = CG(0, 0);
#pragma unroll
    for (int s = 0; s < 4; ++s) {
#pragma unroll
      for (int mu = 0; mu < 8; ++mu) {
        const int it = s * 8 + mu;
        SCHED();
        if (it + 1 < 32) G[(it + 1) & 1] = CG((it + 1) >> 3, (it + 1) & 7);
        if (s < 3) F[(s + 1) & 1][mu] = CF(s + 1, mu);
        SCHED();
#pragma unroll
        for (int e = 0; e + mu < 8; ++e) acc[e + mu] = __builtin_amdgcn_mfma_f32_16x16x32_f16(F[s & 1][e], G[it & 1], acc[e + mu], 0, 0, 0);
      }
    }
  }
  STAMP();
  ARRIVE_L(C_K1 + 1 + ch); await_<0>(lds, C_K1 + 1 + ch, 2);
  {
    char* dst = wl + hf * 4096 + lane * 16;
#pragma unroll
    for (int i = 0; i < 4; ++i) *(f32x4*)(dst + i * 1024) = sel(hf, acc[i], acc[4 + i]);
  }
  ARRIVE_L(C_K3 + ch); await_<0>(lds, C_K3 + ch, 2);
  STAMP();
  {
    const float dskip = (d & 1) ? bfhi(sds) : bflo(sds);
    const char* src = wl + (hf ^ 1) * 4096 + lane * 16;
    char* sdst = ws + WS_S + (size_t)d * 4096;
#pragma unroll
    for (int i = 0; i < 4; ++i) {
      const f32x4 yv = (sel(hf, acc[4 + i], acc[i]) + *(const f32x4*)(src + i * 1024)) * iscale;
      const int t = 256 * (4 * hf + i) + 16 * i16 + 4 * g;
      const u32x2 uu = *(const u32x2*)(ul + (2044 - t) * 2);
      const unsigned uu0 = uu[0], uu1 = uu[1];
      const f16x2 ua = __builtin_bit_cast(f16x2, uu0), ub = __builtin_bit_cast(f16x2, uu1);
      const unsigned y01 = pkbf(yv[0], yv[1]), y23 = pkbf(yv[2], yv[3]);
      const unsigned k01 = pkbf((float)ub[1] * dskip, (float)ub[0] * dskip), k23 = pkbf((float)ua[1] * dskip, (float)ua[0] * dskip);
      const u64 pk = (u64)pkbf(bflo(y01) + bflo(k01), bfhi(y01) + bfhi(k01)) | ((u64)pkbf(bflo(y23) + bflo(k23), bfhi(y23) + bfhi(k23)) << 32);
      __hip_atomic_store((u64*)(sdst + t * 2), pk, RLX_AGENT);
    }
  }
  asm volatile("s_waitcnt vmcnt(0) lgkmcnt(0)" ::: "memory");
  STAMP();
  if (last_of(lds, C_SP, 4, lane) && lane < 32) __hip_atomic_store((u64*)(ws + WS_FLAG + (size_t)blk_of(lane, cb) * 1024 + FL_S) + mb, aS + 1, RLX_AGENT);
  STAMP_FLUSH();
}

#define FIN_LOADS() { \
  asm volatile("global_load_dwordx4 %0, %1, off" : "=v"(xv) : "v"(x + (size_t)(64 * mb + (tid >> 3)) * D_ + 64 * cb + 8 * (tid & 7)) : "memory"); \
  asm volatile("global_load_dwordx4 %0, %1, off" : "=v"(bv) : "v"(b2 + 64 * cb + 8 * (tid & 7)) : "memory"); }
DI void final_stage(u32x4& xv, u32x4& bv, u16* __restrict__ out, char* lds, int mb, int cb, int tid) {
  await_<0>(lds, C_R1, 4);
  await_<0>(lds, C_ST, 4);
  const int t = tid >> 3, dq = tid & 7;
  const char* src = lds + L_RED + t * RED_RS + dq * 32;
  f32x4 a = *(const f32x4*)src, b = *(const f32x4*)(src + 16);
#pragma unroll
  for (int k = 1; k < 4; ++k) { a += *(const f32x4*)(src + k * 64 * RED_RS); b += *(const f32x4*)(src + k * 64 * RED_RS + 16); }
  const u32x4 sv = *(const u32x4*)(lds + L_ST + t * ST_RS + dq * 16);
  asm volatile("s_waitcnt vmcnt(0)" : "+v"(xv), "+v"(bv) :: "memory");
  u32x4 o;
#pragma unroll
  for (int c = 0; c < 4; ++c) {
    const float v0 = (c < 2 ? a[2 * c] : b[2 * c - 4]) + bflo(bv[c]), v1 = (c < 2 ? a[2 * c + 1] : b[2 * c - 3]) + bfhi(bv[c]);
    const float g0 = v0 * __builtin_amdgcn_rcpf(1.f + __builtin_amdgcn_exp2f(-1.4426950408889634f * v0));
    const float g1 = v1 * __builtin_amdgcn_rcpf(1.f + __builtin_amdgcn_exp2f(-1.4426950408889634f * v1));
    const unsigned pv = pkbf(bflo(sv[c]) * g0, bfhi(sv[c]) * g1);
    o[c] = pkbf(bflo(xv[c]) + bflo(pv), bfhi(xv[c]) + bfhi(pv));
  }
  *(u32x4*)(out + (size_t)(64 * mb + t) * D_ + 64 * cb + 8 * dq) = o;
}
DI void p2_gemm(const u16* __restrict__ x, const u16* __restrict__ w2, u64 aOwn, const u16* __restrict__ b2, u16* __restrict__ out, char* __restrict__ ws,
                char* lds, int mb, int cb, int kq, int lane TK0_ARG) {
  STAMP_DECL STAMP_INIT(1) STAMP0();
  const int r = lane & 31, h = lane >> 5;
  unsigned* cnt = (unsigned*)(lds + L_CNT);
  const int tid = kq * 64 + lane;
  K2_SETUP(kq);
  char* ring = lds + L_RING2 + kq * (K2_R * 4096);
  const char* flg = ws + WS_FLAG + (size_t)blockIdx.x * 1024;
  const char* hbase = ws + WS_H + (size_t)(2 * mb) * HMT + 28 * kq * 1024;
  const unsigned hoff0 = lane * 16, hoff1 = lane * 16 + HMT;
  const int q = (lane & 15) >> 2;
  const int toff0 = (4 * h + q) * 128 + ((q >> 1) << 6) + ((lane >> 4) & 1) * 32 + 8 * (lane & 3), toff1 = toff0 ^ 64;
  u32x4 hb[3][4];
  s16x8 wf[2][4];
  f32x16 acc[2][2];
#pragma unroll
  for (int i = 0; i < 16; ++i) { acc[0][0][i] = 0.f; acc[0][1][i] = 0.f; acc[1][0][i] = 0.f; acc[1][1][i] = 0.f; }
  await_<1>(lds, C_PUB, 8);
  K2_DMA(2); K2_DMA(3);
  STAMP();
  if (kq == 0) {
    const u64* fp = (const u64*)(flg + FL_H) + (lane < 14 ? lane : 0);
    for (unsigned spin = 0; spin < (1u << 20); ++spin) {
      u64 v;
      asm volatile("global_load_dwordx2 %0, %1, off sc1\n\ts_waitcnt vmcnt(0)" : "=&v"(v) : "v"(fp) : "memory");
      if (__all(v == aOwn + 1)) break;
      __builtin_amdgcn_s_sleep(2);
    }
    lds_set(cnt + C_H, lane);
  } else {
    lds_wait(cnt + C_H);
  }
  STAMP();
#define K2_HL(S, s2) { const char* hs_ = hbase + K2S(S) * 2048 + (s2) * 1024; gload16(hb[(S) % 3][2 * (s2)], hoff0, hs_); gload16(hb[(S) % 3][2 * (s2) + 1], hoff1, hs_); SCHED(); }
#define K2_HLD(S) { K2_HL(S, 0); K2_HL(S, 1); }
#define K2_TR(S, i) { const char* sl_ = ring + ((S) % K2_R) * 4096 + ((i) >> 1) * 2048 + (((i) & 1) ? toff1 : toff0); wf[(S) & 1][i] = tr_frag(sl_, sl_ + 1024); SCHED(); }
#define K2_D1(S, pp) { dma16s(wvoff, wsrc + (size_t)(K2S(S) * 32 + (pp) * 8) * D_, ringa + ((S) % K2_R) * 4096 + (pp) * 1024); SCHED(); }
#define K2_M(S, s2, mi, di) { acc[mi][di] = __builtin_amdgcn_mfma_f32_32x32x16_bf16(wf[(S) & 1][2 * (s2) + (di)], __builtin_bit_cast(s16x8, hb[(S) % 3][2 * (s2) + (mi)]), acc[mi][di], 0, 0, 0); SCHED(); }
#define K2_STEP(S, W, TRN, HN, DN) { \
    asm volatile("s_waitcnt vmcnt(" #W ")" : "+v"(hb[(S) % 3][0]), "+v"(hb[(S) % 3][1]), "+v"(hb[(S) % 3][2]), "+v"(hb[(S) % 3][3]) :: "memory"); SCHED(); \
    K2_M(S,0,0,0); if (TRN) K2_TR((S)+1,0); K2_M(S,0,1,0); if (TRN) K2_TR((S)+1,1); K2_M(S,0,0,1); if (TRN) K2_TR((S)+1,2); K2_M(S,0,1,1); if (TRN) K2_TR((S)+1,3); \
    K2_M(S,1,0,0); if (DN) { K2_D1((S)+4,0); K2_D1((S)+4,1); } K2_M(S,1,1,0); if (DN) { K2_D1((S)+4,2); K2_D1((S)+4,3); } K2_M(S,1,0,1); if (HN) K2_HL((S)+3,0); K2_M(S,1,1,1); if (HN) K2_HL((S)+3,1); }
  K2_HLD(0); K2_HLD(1); K2_HLD(2);
  await_<0>(lds, C_W2, 4);
  K2_TR(0,0); K2_TR(0,1); K2_TR(0,2); K2_TR(0,3);
  K2_STEP(0, 8, 1, 1, 1); STAMP();
  K2_STEP(1, 12, 1, 1, 1);
  K2_STEP(2, 16, 1, 1, 1);
  K2_STEP(3, 16, 1, 1, 1);
  K2_STEP(4, 16, 1, 1, 1);
  K2_STEP(5, 16, 1, 1, 1);
  K2_STEP(6, 16, 1, 1, 1);
  K2_STEP(7, 16, 1, 1, 1); STAMP();
  K2_STEP(8, 16, 1, 1, 1);
  K2_STEP(9, 16, 1, 1, 1);
  K2_STEP(10, 16, 1, 1, 0);
  u32x4 xv, bv;
  FIN_LOADS();
  K2_STEP(11, 14, 1, 0, 0);
  K2_STEP(12, 6, 1, 0, 0);
  K2_STEP(13, 2, 0, 0, 0);
  STAMP();
  await_<0>(lds, C_SP, 4);
  {
    char* dst = lds + L_RED + (kq * 64 + r) * RED_RS + h * 16;
#pragma unroll
    for (int mi = 0; mi < 2; ++mi)
#pragma unroll
      for (int di = 0; di < 2; ++di)
#pragma unroll
        for (int rq = 0; rq < 4; ++rq)
          *(f32x4*)(dst + mi * 32 * RED_RS + di * 128 + rq * 32) = (f32x4){acc[mi][di][4 * rq], acc[mi][di][4 * rq + 1], acc[mi][di][4 * rq + 2], acc[mi][di][4 * rq + 3]};
  }
  ARRIVE_L(C_R1);
  STAMP();
  final_stage(xv, bv, out, lds, mb, cb, tid);
  if (tid == 0) __hip_atomic_store((u64*)(ws + WS_FLAG + (size_t)blockIdx.x * 1024), aOwn + 1, RLX_AGENT);
  STAMP(); STAMP_FLUSH();
}

DI void p2_helper(const u16* __restrict__ x, const u16* __restrict__ w2, u64 aOwn, const u16* __restrict__ b2, u16* __restrict__ out, char* __restrict__ ws,
                  char* lds, int mb, int cb, int gw, int lane) {
  unsigned* cnt = (unsigned*)(lds + L_CNT);
  const int tid = gw * 64 + lane, hw = gw - 4;
  u32x4 xv, bv;
  FIN_LOADS();
  if (hw == 0) {
    const u64* fp = (const u64*)(ws + WS_FLAG + (size_t)blockIdx.x * 1024 + FL_S) + (lane & 31);
    for (unsigned spin = 0; spin < (1u << 18); ++spin) {
      const u64 v = __hip_atomic_load(fp, RLX_AGENT);
      if (__all(v == aOwn + 1)) break;
      __builtin_amdgcn_s_sleep(8);
    }
    asm volatile("" ::: "memory");
    lds_set(cnt + C_S, lane);
  } else {
    while (__hip_atomic_load(cnt + C_S, RLX_WG) == 0u) __builtin_amdgcn_s_sleep(4);
    asm volatile("" ::: "memory");
  }
  u32x4 sa, sb;
  const char* sp = ws + WS_S + (size_t)(64 * cb + 16 * hw) * 4096 + 64 * mb * 2;
  gload16(sa, (lane >> 3) * 4096 + (lane & 7) * 16, sp);
  gload16(sb, (lane >> 3) * 4096 + (lane & 7) * 16, sp + 8 * 4096);
  asm volatile("s_waitcnt vmcnt(0)" : "+v"(sa), "+v"(sb) :: "memory");
  char* dst = lds + L_ST + (8 * (lane & 7)) * ST_RS + (16 * hw + (lane >> 3)) * 2;
#pragma unroll
  for (int c = 0; c < 4; ++c) {
    *(u16*)(dst + (2 * c) * ST_RS) = (u16)sa[c]; *(u16*)(dst + (2 * c + 1) * ST_RS) = (u16)(sa[c] >> 16);
    *(u16*)(dst + (2 * c) * ST_RS + 16) = (u16)sb[c]; *(u16*)(dst + (2 * c + 1) * ST_RS + 16) = (u16)(sb[c] >> 16);
  }
  ARRIVE_L(C_ST);
  while (__hip_atomic_load(cnt + C_R1, RLX_WG) < 4u) __builtin_amdgcn_s_sleep(2);
  final_stage(xv, bv, out, lds, mb, cb, tid);
}

template <int PH>
__global__ __launch_bounds__(768, 3) void fused_kernel(const u16* __restrict__ x, const u16* __restrict__ lar, const u16* __restrict__ aim,
                                                       const u16* __restrict__ cre, const u16* __restrict__ cim, const u16* __restrict__ ldt,
                                                       const u16* __restrict__ dsk, const u16* __restrict__ w1, const u16* __restrict__ b1,
                                                       const u16* __restrict__ w2, const u16* __restrict__ b2, u16* __restrict__ out,
                                                       char* __restrict__ ws) {
  extern __shared__ __attribute__((aligned(16))) char lds[];
  const int lane = threadIdx.x & 63;
  const int wid = __builtin_amdgcn_readfirstlane(threadIdx.x >> 6);
  const int b = blockIdx.x, xcd = b & 7, j = b >> 3;
  const int mb = 4 * xcd + (j >> 3), cb = j & 7;
  const int jj = j < 28 ? j : 0;
  const int panel = 4 * (xcd >> 1) + jj / 7, fs = 7 * (xcd & 1) + jj % 7;
  if (wid < 4) {
    const int de = 64 * cb + 2 * mb;
    const unsigned sdt = *(const __attribute__((address_space(4))) unsigned*)(ldt + de);
    const unsigned sds = *(const __attribute__((address_space(4))) unsigned*)(dsk + de);
    x_wave(lar, aim, cre, cim, sdt, sds, w2, w1, b1, j >= 28 ? xcd * 4 + (j - 28) : -1, panel, fs, ws, lds, mb, cb, wid, lane TK0_PASS);
    return;
  }
  const int gw = wid - 4;
  __builtin_amdgcn_s_setprio(2);
  if (j < 28) p1_gemm(x, w1, ws, lds, panel, fs, gw, lane TK0_PASS);
  else p1_aux(x, ws, lds, xcd * 4 + (j - 28), gw, lane TK0_PASS);
  __builtin_amdgcn_s_setprio(0);
  await_<0>(lds, C_EP, 1);
  const u64 aOwn = *(const u64*)(lds + L_KSC + 16);
  if (gw < 4) p2_gemm(x, w2, aOwn, b2, out, ws, lds, mb, cb, gw, lane TK0_PASS);
  else p2_helper(x, w2, aOwn, b2, out, ws, lds, mb, cb, gw, lane);
}

extern "C" void kernel_launch(void* const* d_in, const int* in_sizes, int n_in, void* d_out, int out_size, void* d_ws, size_t ws_size,
                              hipStream_t stream) {
  const u16* x = (const u16*)d_in[0];
  const u16* lar = (const u16*)d_in[1];
  const u16* aim = (const u16*)d_in[2];
  const u16* cre = (const u16*)d_in[3];
  const u16* cim = (const u16*)d_in[4];
  const u16* ldt = (const u16*)d_in[5];
  const u16* dsk = (const u16*)d_in[6];
  const u16* w1 = (const u16*)d_in[8];
  const u16* b1 = (const u16*)d_in[9];
  const u16* w2 = (const u16*)d_in[10];
  const u16* b2 = (const u16*)d_in[11];
  (void)hipFuncSetAttribute((const void*)fused_kernel<3>, hipFuncAttributeMaxDynamicSharedMemorySize, LDS_TOTAL);
  fused_kernel<3><<<256, 768, LDS_TOTAL, stream>>>(x, lar, aim, cre, cim, ldt, dsk, w1, b1, w2, b2, (u16*)d_out, (char*)d_ws);
}
```

```cpp
#include <hip/hip_runtime.h>
#include <stdint.h>

typedef unsigned short u16;
typedef unsigned long long u64;
typedef __attribute__((ext_vector_type(4))) short s16x4;
typedef __attribute__((ext_vector_type(8))) short s16x8;
typedef __attribute__((ext_vector_type(8))) _Float16 f16x8;
typedef __attribute__((ext_vector_type(2))) _Float16 f16x2;
typedef __attribute__((ext_vector_type(2))) __bf16 bf16x2;
typedef __attribute__((ext_vector_type(2))) float f32x2;
typedef __attribute__((ext_vector_type(4))) float f32x4;
typedef __attribute__((ext_vector_type(16))) float f32x16;
typedef __attribute__((ext_vector_type(4))) unsigned u32x4;
typedef __attribute__((ext_vector_type(2))) unsigned u32x2;
#define LDSP __attribute__((address_space(3)))
#define GLBP __attribute__((address_space(1)))
#define DI __device__ __forceinline__

#define NLAUNCH 1

#define XPRIO 3
#define L_ 2048
#define D_ 512
#define N_ 128
#define F_ 1792

#define WS_H 0
#define WS_UR (8u << 20)
#define WS_S (10u << 20)
#define WS_STAT (12u << 20)
#define WS_CS ((12u << 20) + 65536)
#define WS_ST2 ((12u << 20) + 131072)
#define WS_FLAG (13u << 20)
#define WS_KAP (16u << 20)
#define FL_LN 128
#define FL_H 384
#define FL_S 512
#define FL_ST 768

#define STAMP_INIT(kid)
#define STAMP()
#define STAMP0()
#define STAMP_DECL
#define STAMP_FLUSH()
#define TK0_ARG
#define TK0_PASS

DI float bf2f(unsigned v) { return __uint_as_float(v << 16); }
DI float bflo(unsigned v) { return __uint_as_float(v << 16); }
DI float bfhi(unsigned v) { return __uint_as_float(v & 0xffff0000u); }
DI unsigned pkbf(float a, float b) { bf16x2 q = {(__bf16)a, (__bf16)b}; return __builtin_bit_cast(unsigned, q); }
DI float rbf(float a) { __bf16 q = (__bf16)a; return (float)q; }
DI unsigned pkh(float a, float b) { f32x2 v = {a, b}; f16x2 p = __builtin_convertvector(v, f16x2); return __builtin_bit_cast(unsigned, p); }
DI float dot2(unsigned a, unsigned b, float c) { return __builtin_amdgcn_fdot2_f32_bf16(__builtin_bit_cast(bf16x2, a), __builtin_bit_cast(bf16x2, b), c, false); }
template <class T> DI T sel(int c, T a, T b) { return c ? a : b; }
DI void dma16(const void* g, char* l) { __builtin_amdgcn_global_load_lds((const GLBP unsigned*)g, (LDSP unsigned*)l, 16, 0, 0); }
DI void dma16s(unsigned voff, const void* sbase, unsigned ldsaddr) {
  asm volatile("s_mov_b32 m0, %2\n\ts_nop 0\n\tglobal_load_lds_dwordx4 %0, %1" :: "v"(voff), "s"(sbase), "s"(ldsaddr) : "memory");
}
DI unsigned lds_base(char* lds) { return __builtin_amdgcn_readfirstlane((unsigned)(size_t)(LDSP char*)lds); }
DI s16x8 tr_frag(const char* p0, const char* p1) {
  s16x4 a = __builtin_amdgcn_ds_read_tr16_b64_v4i16((LDSP s16x4*)p0);
  s16x4 b = __builtin_amdgcn_ds_read_tr16_b64_v4i16((LDSP s16x4*)p1);
  return __builtin_shufflevector(a, b, 0, 1, 2, 3, 4, 5, 6, 7);
}
#define SCHED() __builtin_amdgcn_sched_barrier(0)
#define WAITV_BAR(n) do { SCHED(); asm volatile("s_waitcnt vmcnt(" #n ") lgkmcnt(0)\n\ts_barrier" ::: "memory"); SCHED(); } while (0)
#define LGKM_BAR() do { SCHED(); asm volatile("s_waitcnt lgkmcnt(0)\n\ts_barrier" ::: "memory"); SCHED(); } while (0)
#define RLX_AGENT __ATOMIC_RELAXED, __HIP_MEMORY_SCOPE_AGENT
#define RLX_WG __ATOMIC_RELAXED, __HIP_MEMORY_SCOPE_WORKGROUP

template <int CTRL, int RM> DI float dpp_add(float v) {
  return v + __builtin_bit_cast(float, __builtin_amdgcn_update_dpp(0, __builtin_bit_cast(int, v), CTRL, RM, 0xf, true));
}
DI float wave_sum63(float v) {
  v = dpp_add<0x111, 0xf>(v); v = dpp_add<0x112, 0xf>(v); v = dpp_add<0x114, 0xf>(v); v = dpp_add<0x118, 0xf>(v);
  v = dpp_add<0x142, 0xa>(v); v = dpp_add<0x143, 0xc>(v);
  return v;
}
DI float comb32(float a, float b) { auto r = __builtin_amdgcn_permlane32_swap(__float_as_uint(a), __float_as_uint(b), false, false); return __uint_as_float(r[0]) + __uint_as_float(r[1]); }
DI float comb16(float a, float b) { auto r = __builtin_amdgcn_permlane16_swap(__float_as_uint(a), __float_as_uint(b), false, false); return __uint_as_float(r[0]) + __uint_as_float(r[1]); }
DI float row_allsum(float v) { v = dpp_add<0x128, 0xf>(v); v = dpp_add<0x124, 0xf>(v); v = dpp_add<0x122, 0xf>(v); v = dpp_add<0x121, 0xf>(v); return v; }
DI float rdlane(float v, int l) { return __builtin_bit_cast(float, __builtin_amdgcn_readlane(__builtin_bit_cast(int, v), l)); }

DI void group_barrier(unsigned* cnt, unsigned target, int lane) {
  asm volatile("s_waitcnt lgkmcnt(0)" ::: "memory");
  if (lane == 0) __hip_atomic_fetch_add(cnt, 1u, RLX_WG);
  while (__hip_atomic_load(cnt, RLX_WG) < target) __builtin_amdgcn_s_sleep(1);
  asm volatile("" ::: "memory");
}
DI void poll_flags(const u64* p, u64 expect) {
  for (unsigned spin = 0; spin < (1u << 20); ++spin) {
    const u64 v = __hip_atomic_load(p, RLX_AGENT);
    if (__all(v == expect)) break;
    __builtin_amdgcn_s_sleep(2);
  }
  asm volatile("" ::: "memory");
}
DI void lds_set(unsigned* w, int lane) { if (lane == 0) __hip_atomic_store(w, 1u, RLX_WG); }
DI void lds_wait(unsigned* w) { while (__hip_atomic_load(w, RLX_WG) == 0u) __builtin_amdgcn_s_sleep(1); asm volatile("" ::: "memory"); }
DI int blk_of(int mb, int cb) { return (mb >> 2) + 8 * (((mb & 3) << 3) | cb); }

#define LDS_TOTAL 163840
#define L_STAT 162304
#define L_BIAS 163328
#define L_ACON 163584
#define L_KSC 163712
#define L_CNT 163744
#define KAPSZ 2576
#define L_KAP 141696
#define L_COLS 152000
#define K1_SLOT 32768
#define L_TB 98304
#define L_KP 131584
#define KPSZ 4160
#define K2_R 4
#define L_RING2 0
#define L_W 65536
#define WSZ 33024
#define L_U L_KAP
#define USZ 4608
#define L_RED L_W
#define RED_RS 272
#define L_ST L_KAP
#define ST_RS 144
#define C_PUB 0
#define C_SP 1
#define C_H 2
#define C_S 3
#define C_W2 4
#define C_R1 5
#define C_ST 6
#define C_K0 7
#define C_K1 9
#define C_K2 12
#define C_K3 14
#define C_LN1 16
#define C_LN2 17
#define C_X2 18
#define C_CS 19
#define C_EP 20
DI void arrive(char* lds, int c, int lane) { if (lane == 0) __hip_atomic_fetch_add((unsigned*)(lds + L_CNT) + c, 1u, RLX_WG); }
template <int SL> DI void await_(char* lds, int c, unsigned target) {
  while (__hip_atomic_load((unsigned*)(lds + L_CNT) + c, RLX_WG) < target) __builtin_amdgcn_s_sleep(SL);
  asm volatile("" ::: "memory"); SCHED();
}
#define ARRIVE_V(n, c) do { SCHED(); asm volatile("s_waitcnt vmcnt(" #n ") lgkmcnt(0)" ::: "memory"); arrive(lds, c, lane); SCHED(); } while (0)
#define ARRIVE_L(c) do { SCHED(); asm volatile("s_waitcnt lgkmcnt(0)" ::: "memory"); arrive(lds, c, lane); SCHED(); } while (0)

struct cf { float x, y; };
DI cf cmul(cf a, cf b) { return cf{fmaf(a.x, b.x, -(a.y * b.y)), fmaf(a.x, b.y, a.y * b.x)}; }
DI unsigned pkc(cf a) { return pkh(a.x, a.y); }
DI unsigned pkcc(cf a) { return pkh(a.x, -a.y); }
DI cf zpow(float al, float om, float p) {
  const float m = __builtin_amdgcn_exp2f(al * p), ph = __builtin_amdgcn_fractf(om * p);
  return cf{m * __builtin_amdgcn_cosf(ph), m * __builtin_amdgcn_sinf(ph)};
}
DI unsigned cmulh(unsigned a, unsigned b) {
  const f16x2 z = __builtin_bit_cast(f16x2, a), w = __builtin_bit_cast(f16x2, b);
  const f16x2 t = z * (f16x2){w[0], w[0]};
  const f16x2 r = __builtin_elementwise_fma((f16x2){-z[1], z[0]}, (f16x2){w[1], w[1]}, t);
  return __builtin_bit_cast(unsigned, r);
}
DI u32x4 cmulh4(u32x4 a, u32x4 b) { return (u32x4){cmulh(a[0], b[0]), cmulh(a[1], b[1]), cmulh(a[2], b[2]), cmulh(a[3], b[3])}; }
#define TB_RS 272
#define TB_N 33
DI void kgen_tables(unsigned (&T)[TB_N], float& isc, unsigned raw0, unsigned raw1, float ldtv, char* lds, int cw, int lane) {
  const int src = ((lane & 31) >> 1) * 4, sh = (lane & 1) ? 0 : 16;
  unsigned pv[4];
#pragma unroll
  for (int a = 0; a < 4; ++a) {
    const unsigned lo = (unsigned)__builtin_amdgcn_ds_bpermute(src + 64 * a, (int)raw0), hi = (unsigned)__builtin_amdgcn_ds_bpermute(src + 64 * a, (int)raw1);
    pv[a] = ((lane < 32 ? lo : hi) << sh) & 0xffff0000u;
  }
  const float dt = rbf(__builtin_amdgcn_exp2f(ldtv * 1.4426950408889634f));
  const float ar = rbf(__builtin_amdgcn_exp2f(__uint_as_float(pv[0]) * 1.4426950408889634f));
  const float al = -(ar * dt) * 1.4426950408889634f;
  const float om = (__uint_as_float(pv[1]) * dt) * 0.15915494309189535f;
  const float cr = __uint_as_float(pv[2]) * dt, ci = __uint_as_float(pv[3]) * dt;
  const float sabs = 2.f * rdlane(wave_sum63(fabsf(cr) + fabsf(ci)), 63);
  int ex = (int)((__float_as_uint(sabs) >> 23) & 255);
  ex = ex < 20 ? 20 : (ex > 230 ? 230 : ex);
  const float sc2 = __uint_as_float((unsigned)(268 - ex) << 23);
  isc = __uint_as_float((unsigned)(ex - 13) << 23);
  if (lane == 0) *(float*)(lds + L_KSC + cw * 4) = sabs;
  const cf z1 = zpow(al, om, 1.f), z8 = zpow(al, om, 8.f), z32 = zpow(al, om, 32.f), a1 = zpow(al, om, 128.f), a4 = zpow(al, om, 512.f);
  const unsigned one = 0x00003C00u;
  const cf a2 = cmul(a1, a1), a3 = cmul(a2, a1), a8 = cmul(a4, a4), a12 = cmul(a8, a4);
  T[0] = one; T[1] = pkc(a1); T[2] = pkc(a2); T[3] = pkc(a3);
  T[4] = pkc(a4); T[5] = pkc(cmul(a4, a1)); T[6] = pkc(cmul(a4, a2)); T[7] = pkc(cmul(a4, a3));
  T[8] = pkc(a8); T[9] = pkc(cmul(a8, a1)); T[10] = pkc(cmul(a8, a2)); T[11] = pkc(cmul(a8, a3));
  T[12] = pkc(a12); T[13] = pkc(cmul(a12, a1)); T[14] = pkc(cmul(a12, a2)); T[15] = pkc(cmul(a12, a3));
  T[16] = pkc(cmul(a8, a8));
  const cf e2 = cmul(z1, z1), e3 = cmul(e2, z1), e4 = cmul(e2, e2);
  T[17] = one; T[18] = pkc(z1); T[19] = pkc(e2); T[20] = pkc(e3); T[21] = pkc(e4);
  T[22] = pkc(cmul(e4, z1)); T[23] = pkc(cmul(e4, e2)); T[24] = pkc(cmul(e4, e3));
  const cf c0 = cf{cr * sc2, ci * sc2}, c2 = cmul(c0, cmul(z32, z32));
  T[25] = pkcc(c0); T[26] = pkcc(cmul(c0, z32)); T[27] = pkcc(c2); T[28] = pkcc(cmul(c2, z32));
  const cf z16 = cmul(z8, z8);
  T[29] = one; T[30] = pkcc(z8); T[31] = pkcc(z16); T[32] = pkcc(cmul(z16, z8));
}
#define KG_PARAMS f32x4& kX, f32x4& kY, f32x4& kZ, float isc
DI void kgen_store(KG_PARAMS, char* lds, int wid, int lane) {
  const int i16 = lane & 15, g = lane >> 4;
  float* kap = (float*)(lds + L_KAP + wid * KAPSZ);
  *(f32x4*)(kap + 128 * (g >> 1) + 4 * (g & 1) + 8 * i16) = kX * isc;
  if (g < 2) {
    *(f32x4*)(kap + 256 + 64 * g + 4 * i16) = kY * isc;
#pragma unroll
    for (int r = 0; r < 4; ++r) kap[512 + 16 * (4 * g + r) + i16] = kZ[r] * isc;
  } else {
    *(f32x2*)(kap + 384 + 64 * (g - 2) + 2 * i16) = (f32x2){kY[0] * isc, kY[1] * isc};
    *(f32x2*)(kap + 384 + 64 * (g - 2) + 32 + 2 * i16) = (f32x2){kY[2] * isc, kY[3] * isc};
    if (g == 2 && i16 == 0) kap[640] = kZ[0] * isc;
  }
}
DI const u16* prm_ptr(const u16* lar, const u16* aim, const u16* cre, const u16* cim, int d, int hf, int lane) {
  const int g = lane >> 4;
  const u16* p = g == 0 ? lar : (g == 1 ? aim : (g == 2 ? cre : cim));
  return p + d * N_ + 64 * hf + 2 * (lane & 15);
}
DI void ld_u32(unsigned& v, const void* p) { asm volatile("global_load_dword %0, %1, off" : "=v"(v) : "v"(p) : "memory"); }
DI void ld_u64(u64& v, const void* p) { asm volatile("global_load_dwordx2 %0, %1, off sc1" : "=v"(v) : "v"(p) : "memory"); }
DI void gload16(u32x4& v, unsigned voff, const char* sbase) {
  asm volatile("global_load_dwordx4 %0, %1, %2 sc1" : "=v"(v) : "v"(voff), "s"(sbase) : "memory");
}
DI u64 rdlane64(u64 v, int l) { return (u64)(unsigned)__builtin_amdgcn_readlane((int)(unsigned)v, l) | ((u64)(unsigned)__builtin_amdgcn_readlane((int)(unsigned)(v >> 32), l) << 32); }
DI bool last_of(char* lds, int c, unsigned n, int lane) {
  unsigned old = 0;
  if (lane == 0) old = __hip_atomic_fetch_add((unsigned*)(lds + L_CNT) + c, 1u, RLX_WG);
  return (unsigned)__builtin_amdgcn_readfirstlane(old) == n - 1;
}
DI float gelu_f(float v) {
  const float c0 = -2.302208198f;
  const float c1 = -0.1029432397f;
  const float z = v * fmaf(v * v, c1, c0);
  return v * __builtin_amdgcn_rcpf(1.f + __builtin_amdgcn_exp2f(z));
}
#define K2S(S) ((S) + r2 >= 14 ? (S) + r2 - 14 : (S) + r2)
#define K2_DMA(S) { _Pragma("unroll") for (int pp = 0; pp < 4; ++pp) dma16s(wvoff, wsrc + (size_t)(K2S(S) * 32 + pp * 8) * D_, ringa + ((S) % K2_R) * 4096 + pp * 1024); SCHED(); }
#define K2_SETUP(kq_) int lane2_ = lane; asm volatile("" : "+v"(lane2_)); \
  const unsigned wvoff = (lane2_ >> 3) * (D_ * 2) + (((lane2_ & 7) ^ (((lane2_ >> 4) & 1) << 2)) << 4); \
  const unsigned ringa = __builtin_amdgcn_readfirstlane(lds_base(lds) + L_RING2 + (kq_) * (K2_R * 4096)); \
  const u16* wsrc = w2 + (size_t)(14 * (kq_) * 32) * D_ + 64 * cb; \
  const int r2 = (2 * cb + 4 * (mb & 3)) % 14;
#define HMT 114688

DI void p1_gemm(const u16* __restrict__ x, const u16* __restrict__ w1, char* __restrict__ ws, char* lds, int panel, int fs, int wid, int lane TK0_ARG) {
  STAMP_DECL STAMP_INIT(0) STAMP0();
  const int wm = wid >> 2, wf = (wid >> 1) & 1, kh = wid & 1;
  const int r = lane & 31, h = lane >> 5;
  const int row0 = panel * 128, f0 = fs * 128;
  const unsigned xv0 = (lane >> 3) * 1024 + (((lane & 7) ^ ((lane >> 4) & 3)) << 4);
  const unsigned xv1 = xv0 ^ 64;
  const unsigned wv = (lane >> 4) * (F_ * 2) + (((lane & 15) ^ (((lane >> 4) & 3) << 2)) << 4);
  const u16* xsb = x + (size_t)(row0 + 16 * wid) * D_;
  const u16* wsb = w1 + (size_t)(8 * wid) * F_ + f0;
  const unsigned la = lds_base(lds) + wid * 2048;
  const int rot = 2 * (panel & 3) + (fs >= 7 ? fs - 7 : fs);
#define KS(S) (((S) + rot) & 7)
#define K1_D0(S) { dma16s(xv0, xsb + KS(S) * 64, la + ((S) % 4) * K1_SLOT); SCHED(); }
#define K1_D1(S) { dma16s(xv1, xsb + 8 * D_ + KS(S) * 64, la + ((S) % 4) * K1_SLOT + 1024); SCHED(); }
#define K1_D2(S) { dma16s(wv, wsb + (size_t)(KS(S) * 64) * F_, la + ((S) % 4) * K1_SLOT + 16384); SCHED(); }
#define K1_D3(S) { dma16s(wv, wsb + (size_t)(KS(S) * 64 + 4) * F_, la + ((S) % 4) * K1_SLOT + 16384 + 1024); SCHED(); }
#define K1_ISSUE(S) { K1_D0(S); K1_D1(S); K1_D2(S); K1_D3(S); }
  K1_ISSUE(0); K1_ISSUE(1); K1_ISSUE(2); K1_ISSUE(3);
  const int xoff = (64 * wm + r) * 128 + (((4 * kh + 2 * wm + h) ^ ((r >> 1) & 7)) << 4);
  const int q = (lane & 15) >> 2, p = lane & 3;
  const int woff0 = 16384 + (32 * kh + 16 * wm + 8 * h + q) * 256 + (((2 * wf + kh) ^ q) << 6) + ((lane >> 4) & 1) * 32 + 8 * p;
  f32x16 acc[2][2];
#pragma unroll
  for (int i = 0; i < 16; ++i) { acc[0][0][i] = 0.f; acc[0][1][i] = 0.f; acc[1][0][i] = 0.f; acc[1][1][i] = 0.f; }
  STAMP();
  s16x8 xf[2][2][2], wfr[2][2][2];
#define K1_M(T, s, mi, fi) { acc[mi][fi] = __builtin_amdgcn_mfma_f32_32x32x16_bf16(wfr[(T) & 1][s][fi], xf[(T) & 1][s][mi], acc[mi][fi], 0, 0, 0); SCHED(); }
#define K1_RX(T, s, mi) { xf[(T) & 1][s][mi] = *(const s16x8*)(lds + (xa_ ^ ((s) << 5)) + (mi) * 4096); SCHED(); }
#define K1_RW(T, s, fi) { wfr[(T) & 1][s][fi] = tr_frag(lds + (wa_ ^ (((fi) << 6) | ((s) << 12))), lds + (wa_ ^ (((fi) << 6) | ((s) << 12))) + 1024); SCHED(); }
#define K1_ADDR(T) int xa_ = xoff + ((T) % 4) * K1_SLOT, wa_ = woff0 + ((T) % 4) * K1_SLOT; asm volatile("" : "+v"(xa_), "+v"(wa_));
#define K1_READ(T) { K1_ADDR(T) K1_RX(T,0,0); K1_RW(T,0,0); K1_RW(T,0,1); K1_RX(T,0,1); K1_RX(T,1,0); K1_RW(T,1,0); K1_RW(T,1,1); K1_RX(T,1,1); }
#define K1_MMA(T) { K1_M(T,0,0,0); K1_M(T,0,0,1); K1_M(T,0,1,0); K1_M(T,0,1,1); K1_M(T,1,0,0); K1_M(T,1,0,1); K1_M(T,1,1,0); K1_M(T,1,1,1); }
#define K1_STAGE(T, DM0, DM1, DM2, DM3) { K1_ADDR(T) \
    K1_M((T)-1,0,0,0); K1_RX(T,0,0); K1_RW(T,0,0); K1_M((T)-1,0,0,1); K1_RW(T,0,1); K1_RX(T,0,1); \
    K1_M((T)-1,0,1,0); K1_RX(T,1,0); K1_RW(T,1,0); K1_M((T)-1,0,1,1); K1_RW(T,1,1); K1_RX(T,1,1); \
    K1_M((T)-1,1,0,0); DM0; K1_M((T)-1,1,0,1); DM1; K1_M((T)-1,1,1,0); DM2; K1_M((T)-1,1,1,1); DM3; }
#define K1_STAGE_D(T, S) K1_STAGE(T, K1_D0(S), K1_D1(S), K1_D2(S), K1_D3(S))
  WAITV_BAR(12); STAMP();
  K1_READ(0);
  WAITV_BAR(8); K1_STAGE_D(1, 4);
  WAITV_BAR(8); K1_STAGE_D(2, 5);
  WAITV_BAR(8); K1_STAGE_D(3, 6);
  WAITV_BAR(8); STAMP(); K1_STAGE_D(4, 7);
  WAITV_BAR(8); K1_STAGE(5, , , , );
  WAITV_BAR(4); K1_STAGE(6, , , , );
  K1_MMA(6);
  WAITV_BAR(0);
  K1_READ(7); K1_MMA(7);
  STAMP();
  float* colS = (float*)(lds + L_COLS);
  {
    char* dst = lds + wid * 8192 + lane * 16;
#pragma unroll
    for (int mi = 0; mi < 2; ++mi)
#pragma unroll
      for (int rq = 0; rq < 4; ++rq)
        *(f32x4*)(dst + (mi * 4 + rq) * 1024) = (f32x4){acc[mi][1][4 * rq], acc[mi][1][4 * rq + 1], acc[mi][1][4 * rq + 2], acc[mi][1][4 * rq + 3]};
  }
  LGKM_BAR();
  STAMP();
  f32x4 pa[2][4], cq[4];
  f32x2 st[2];
  u32x2 bq[4];
  {
    const char* src = lds + (wid ^ 1) * 8192 + lane * 16;
    const int fl = 64 * wf + 32 * kh + 4 * h;
#pragma unroll
    for (int rq = 0; rq < 4; ++rq) {
      cq[rq] = *(const f32x4*)(colS + fl + 8 * rq);
      bq[rq] = *(const u32x2*)(lds + L_BIAS + (fl + 8 * rq) * 2);
      pa[0][rq] = *(const f32x4*)(src + rq * 1024); pa[1][rq] = *(const f32x4*)(src + (4 + rq) * 1024);
    }
    st[0] = *(const f32x2*)(lds + L_STAT + (64 * wm + r) * 8); st[1] = *(const f32x2*)(lds + L_STAT + (64 * wm + 32 + r) * 8);
  }
  LGKM_BAR();
  {
    const __amdgpu_buffer_rsrc_t hr = __builtin_amdgcn_make_buffer_rsrc(ws + WS_H, 0, L_ * F_ * 2, 0x00020000);
    const unsigned hoff = (unsigned)(panel * 4 + 2 * wm) * HMT + (8 * fs + 4 * wf + 2 * kh) * 1024 + lane * 16;
#pragma unroll
    for (int mi = 0; mi < 2; ++mi) {
      const float mr = st[mi][0], rinv = st[mi][1];
#pragma unroll
      for (int s2 = 0; s2 < 2; ++s2) {
        u32x4 pk;
#pragma unroll
        for (int qq = 0; qq < 2; ++qq) {
          const int rq = 2 * s2 + qq;
          const float b0 = bflo(bq[rq][0]), b1_ = bfhi(bq[rq][0]), b2_ = bflo(bq[rq][1]), b3 = bfhi(bq[rq][1]);
          const float o0 = gelu_f(fmaf(acc[mi][0][4 * rq] + pa[mi][rq][0], rinv, fmaf(mr, cq[rq][0], b0)));
          const float o1 = gelu_f(fmaf(acc[mi][0][4 * rq + 1] + pa[mi][rq][1], rinv, fmaf(mr, cq[rq][1], b1_)));
          const float o2 = gelu_f(fmaf(acc[mi][0][4 * rq + 2] + pa[mi][rq][2], rinv, fmaf(mr, cq[rq][2], b2_)));
          const float o3 = gelu_f(fmaf(acc[mi][0][4 * rq + 3] + pa[mi][rq][3], rinv, fmaf(mr, cq[rq][3], b3)));
          pk[2 * qq] = pkbf(o0, o1); pk[2 * qq + 1] = pkbf(o2, o3);
        }
        __builtin_amdgcn_raw_buffer_store_b128(pk, hr, hoff + mi * HMT + s2 * 1024, 0, 16);
        SCHED();
      }
    }
  }
  STAMP();
  asm volatile("s_waitcnt vmcnt(0)" ::: "memory");
  if (last_of(lds, C_PUB, 8, lane) && lane < 16) {
    const u64 ac = *(const u64*)(lds + L_ACON + lane * 8);
    int l2 = lane; asm volatile("" : "+v"(l2));
    __hip_atomic_store((u64*)(ws + WS_FLAG + (size_t)blk_of(2 * panel + ((l2 >> 3) & 1), l2 & 7) * 1024 + FL_H) + fs, ac + 1, RLX_AGENT);
  }
  STAMP(); STAMP_FLUSH();
}

#define LN_RS 1040
DI void p1_aux(const u16* __restrict__ x, char* __restrict__ ws, char* lds, int idx, int wid, int lane TK0_ARG) {
  STAMP_DECL STAMP_INIT(0) STAMP0();
  const int t0 = idx * 64;
  u32x4 v[8];
#pragma unroll
  for (int i = 0; i < 8; ++i) v[i] = *(const u32x4*)(x + (size_t)(t0 + 8 * wid + i) * D_ + lane * 8);
  const u64 aCon = __hip_atomic_load((const u64*)(ws + WS_FLAG + (size_t)((wid * 64 + lane) & 255) * 1024), RLX_AGENT);
  const int sfs = lane < 14 ? lane : 0;
  const int sblk = 2 * (idx >> 3) + (sfs >= 7 ? 1 : 0) + 8 * (((idx >> 1) & 3) * 7 + (sfs >= 7 ? sfs - 7 : sfs));
  const u64 aSt = __hip_atomic_load((const u64*)(ws + WS_FLAG + (size_t)sblk * 1024), RLX_AGENT);
  SCHED();
  asm volatile("s_barrier" ::: "memory");
  SCHED();
#pragma unroll
  for (int i = 0; i < 8; ++i) *(u32x4*)(lds + (63 - (8 * wid + i)) * LN_RS + lane * 16) = v[i];
  ARRIVE_L(C_LN1);
  float sA[8], sB[8];
#pragma unroll
  for (int i = 0; i < 8; ++i) {
    float a = dot2(v[i][0], 0x3F803F80u, 0.f), b = dot2(v[i][0], v[i][0], 0.f);
#pragma unroll
    for (int c = 1; c < 4; ++c) { a = dot2(v[i][c], 0x3F803F80u, a); b = dot2(v[i][c], v[i][c], b); }
    sA[i] = a; sB[i] = b;
  }
#pragma unroll
  for (int i = 0; i < 4; ++i) { sA[i] = comb32(sA[i], sA[i + 4]); sB[i] = comb32(sB[i], sB[i + 4]); }
#pragma unroll
  for (int i = 0; i < 2; ++i) { sA[i] = row_allsum(comb16(sA[i], sA[i + 2])); sB[i] = row_allsum(comb16(sB[i], sB[i + 2])); }
  const float mv = ((lane & 1) ? sA[1] : sA[0]) * (1.f / 512.f);
  const float var = fmaxf(fmaf(-mv, mv, ((lane & 1) ? sB[1] : sB[0]) * (1.f / 512.f)), 0.f);
  const float mbv = rbf(mv);
  const float sd = rbf(sqrtf(rbf(rbf(var) + 1e-5f)));
  const float rv = 1.f / sd;
  if ((lane & 14) == 0) {
    const u64 pk = (u64)__float_as_uint(-mbv * rv) | ((u64)__float_as_uint(rv) << 32);
    const int tt = t0 + 8 * wid + (lane & 1) + 2 * (lane >> 4);
    __hip_atomic_store((u64*)(ws + WS_STAT) + tt, pk, RLX_AGENT);
    __hip_atomic_store((unsigned*)(ws + WS_ST2) + (L_ - 1 - tt), (__float_as_uint(mbv) >> 16) | __float_as_uint(sd), RLX_AGENT);
  }
  STAMP();
  await_<0>(lds, C_LN1, 8);
  char* t2 = lds + 64 * LN_RS + wid * 8192;
  const int g = lane >> 4, q = (lane & 15) >> 2, p = lane & 3, dl = lane & 15;
  s16x8 f[8];
#pragma unroll
  for (int i = 0; i < 8; ++i) {
    const char* a = lds + (8 * (4 * (i & 1) + g) + q) * LN_RS + (64 * wid + 16 * (i >> 1) + 4 * p) * 2;
    f[i] = tr_frag(a, a + 4 * LN_RS);
  }
  SCHED();
#pragma unroll
  for (int i = 0; i < 8; ++i) {
    const int dd2 = 16 * (i >> 1) + dl, rc = 4 * (i & 1) + g;
    *(s16x8*)(t2 + dd2 * 128 + ((rc ^ (dd2 & 7)) << 4)) = f[i];
  }
  SCHED();
  const __amdgpu_buffer_rsrc_t ur = __builtin_amdgcn_make_buffer_rsrc(ws + WS_UR, 0, L_ * D_ * 2, 0x00020000);
  const int x0 = L_ - 64 - t0;
  u32x4 o[8];
#pragma unroll
  for (int i = 0; i < 8; ++i) {
    const int dd2 = 8 * i + (lane >> 3), c = lane & 7;
    o[i] = *(const u32x4*)(t2 + dd2 * 128 + ((c ^ (dd2 & 7)) << 4));
  }
  SCHED();
  asm volatile("s_waitcnt vmcnt(0)" ::: "memory");
  if (last_of(lds, C_PUB, 8, lane) && lane < 14) __hip_atomic_store((u64*)(ws + WS_FLAG + (size_t)sblk * 1024 + FL_ST) + (idx & 1), aSt + 1, RLX_AGENT);
  STAMP();
#pragma unroll
  for (int i = 0; i < 8; ++i)
    __builtin_amdgcn_raw_buffer_store_b128(o[i], ur, (unsigned)(64 * wid + 8 * i + (lane >> 3)) * 4096 + x0 * 2 + (lane & 7) * 16, 0, 16);
  STAMP();
  ARRIVE_L(C_X2);
  ARRIVE_V(0, C_LN2);
  if (wid < 4) {
    await_<0>(lds, C_LN2, 8);
    __hip_atomic_store((u64*)(ws + WS_FLAG + (size_t)(wid * 64 + lane) * 1024 + FL_LN) + idx, aCon + 1, RLX_AGENT);
  }
  STAMP(); STAMP_FLUSH();
}

DI void x_wave(const u16* __restrict__ lar, const u16* __restrict__ aim, const u16* __restrict__ cre, const u16* __restrict__ cim,
               unsigned sdt, unsigned sds, const u16* __restrict__ w2, const u16* __restrict__ w1, const u16* __restrict__ b1, int auxidx, int panel, int fs,
               char* __restrict__ ws, char* lds, int mb, int cb, int cw, int lane TK0_ARG) {
  STAMP_DECL STAMP_INIT(1) STAMP0();
  __builtin_amdgcn_s_setprio(3);
  const bool aux = auxidx >= 0;
  const int ch = cw >> 1, hf = cw & 1;
  const int d = 64 * cb + 2 * mb + ch;
  const int i16 = lane & 15, g = lane >> 4;
  unsigned* cnt = (unsigned*)(lds + L_CNT);
  const char* flg = ws + WS_FLAG + (size_t)blockIdx.x * 1024;
  const u16* pp_ = prm_ptr(lar, aim, cre, cim, d, hf, lane);
  unsigned raw0, raw1;
  u64 aS, aH;
  ld_u32(raw0, pp_); ld_u32(raw1, pp_ + 32);
  ld_u64(aS, ws + WS_FLAG + (size_t)(lane >= 32 ? (int)blockIdx.x : blk_of(lane, cb)) * 1024);
  ld_u64(aH, ws + WS_FLAG + (size_t)blk_of(2 * panel + ((lane >> 3) & 1), lane & 7) * 1024);
  if (!aux && cw == 2) asm volatile("s_mov_b32 m0, %2\n\ts_nop 0\n\tglobal_load_lds_dword %0, %1" :: "v"(lane * 4), "s"(b1 + fs * 128), "s"(lds_base(lds) + L_BIAS) : "memory");
  if (cw == 0 && lane < 24) cnt[lane] = 0u;
  if (aux) {
    asm volatile("s_waitcnt lgkmcnt(0)\n\ts_barrier" ::: "memory");
    __builtin_amdgcn_s_setprio(1);
    if (auxidx < 28) {
      const u16* wp_ = w1 + (size_t)(128 * cw + (lane >> 3)) * F_ + 64 * auxidx + 8 * (lane & 7);
      const int cblk = 2 * ((lane & 15) >> 2) + (auxidx >= 14 ? 1 : 0) + 8 * ((lane & 3) * 7 + ((auxidx >> 1) % 7));
      const u64 aC = __hip_atomic_load((const u64*)(ws + WS_FLAG + (size_t)cblk * 1024), RLX_AGENT);
      float a8[8] = {0.f, 0.f, 0.f, 0.f, 0.f, 0.f, 0.f, 0.f};
      u32x4 wv[16];
#pragma unroll
      for (int i = 0; i < 16; ++i) wv[i] = *(const u32x4*)(wp_ + (size_t)(8 * i) * F_);
      SCHED();
#pragma unroll
      for (int i = 0; i < 16; ++i)
#pragma unroll
        for (int c = 0; c < 4; ++c) { a8[2 * c] += bflo(wv[i][c]); a8[2 * c + 1] += bfhi(wv[i][c]); }
#pragma unroll
      for (int i = 0; i < 4; ++i) a8[i] = comb32(a8[i], a8[i + 4]);
#pragma unroll
      for (int i = 0; i < 2; ++i) a8[i] = dpp_add<0x128, 0xf>(comb16(a8[i], a8[i + 2]));
      if (!(lane & 8)) *(f32x2*)(lds + L_COLS + cw * 256 + (8 * (lane & 7) + 2 * (g & 1) + 4 * (g >> 1)) * 4) = (f32x2){a8[0], a8[1]};
      SCHED(); asm volatile("s_waitcnt lgkmcnt(0)" ::: "memory");
      if (last_of(lds, C_CS, 4, lane)) {
        const float* cp = (const float*)(lds + L_COLS);
        const float tot = (cp[lane] + cp[64 + lane]) + (cp[128 + lane] + cp[192 + lane]);
        __hip_atomic_store((float*)(ws + WS_CS) + 64 * auxidx + lane, tot, RLX_AGENT);
        asm volatile("s_waitcnt vmcnt(0)" ::: "memory");
        if (lane < 16) __hip_atomic_store((u64*)(ws + WS_FLAG + (size_t)cblk * 1024 + FL_ST) + 2 + (auxidx & 1), aC + 1, RLX_AGENT);
      }
    }
  }
  asm volatile("s_waitcnt vmcnt(2)" : "+v"(raw0), "+v"(raw1) :: "memory");
  float isc;
  unsigned T[TB_N];
  kgen_tables(T, isc, raw0, raw1, (d & 1) ? bfhi(sdt) : bflo(sdt), lds, cw, lane);
#pragma unroll
  for (int i = 0; i < TB_N; ++i) asm volatile("" : "+v"(T[i]));
  asm volatile("s_waitcnt vmcnt(0)" : "+v"(aS), "+v"(aH) :: "memory");
  const u64 aOwn = rdlane64(aS, 32);
  if (cw == 1 && lane < 16) *(u64*)(lds + L_ACON + lane * 8) = aH;
  if (cw == 3) { if (lane == 0) *(u64*)(lds + L_KSC + 16) = aOwn; ARRIVE_L(C_EP); }
  __builtin_amdgcn_s_setprio(1);
  STAMP();
  const u64* lnfl = (const u64*)(flg + FL_LN) + (lane & 31);
  const char* up = ws + WS_UR + (size_t)d * 4096 + hf * 2048;
  u64 fv;
  if (aux) {
    await_<2>(lds, C_X2, 8);
  } else {
    const u64* stfl = (const u64*)(flg + FL_ST) + (lane & 3);
    const char* stp = cw == 1 ? ws + WS_CS + fs * 512 : ws + WS_STAT + (size_t)panel * 1024;
    const unsigned svo = cw == 1 ? (lane & 31) * 16 : lane * 16;
    u32x4 stv;
#define XBAR() do { SCHED(); asm volatile("s_waitcnt lgkmcnt(0)\n\ts_barrier" ::: "memory"); SCHED(); } while (0)
    if (cw < 2) {
      bool ok = false, got = false;
      XBAR(); XBAR(); XBAR(); XBAR();
      ld_u64(fv, stfl); gload16(stv, svo, stp);
#define XPOLL() do { XBAR(); asm volatile("s_waitcnt vmcnt(0)" : "+v"(fv), "+v"(stv) :: "memory"); \
        if (ok && !got) { *(u32x4*)(lds + (cw == 1 ? L_COLS : L_STAT) + svo) = stv; got = true; } \
        ok = __all(fv == aOwn + 1); SCHED(); ld_u64(fv, stfl); gload16(stv, svo, stp); } while (0)
      XPOLL(); XPOLL(); XPOLL(); XPOLL();
      asm volatile("s_waitcnt vmcnt(0)" : "+v"(fv), "+v"(stv) :: "memory");
      if (!got) {
        if (!ok) {
          poll_flags(stfl, aOwn + 1);
          asm volatile("global_load_dwordx4 %0, %1, %2 sc1\n\ts_waitcnt vmcnt(0)" : "=&v"(stv) : "v"(svo), "s"(stp) : "memory");
        }
        *(u32x4*)(lds + (cw == 1 ? L_COLS : L_STAT) + svo) = stv;
      }
    } else {
      XBAR(); XBAR(); XBAR(); XBAR(); XBAR(); XBAR(); XBAR(); XBAR();
    }
    XBAR();
    XBAR();
  }
  STAMP();
  __builtin_amdgcn_s_setprio(3);
  K2_SETUP(cw);
  K2_DMA(0); K2_DMA(1);
  char* wl = lds + L_W + ch * WSZ;
  {
    char* tb = wl + hf * 9216;
#pragma unroll
    for (int i = 0; i < TB_N; ++i) *(unsigned*)(tb + i * TB_RS + lane * 4) = T[i];
    const char* tr = tb + g * 16;
    const int rXa = (i16 >> 3) * TB_RS, rXe = (17 + (i16 & 7)) * TB_RS;
    const int rYa = (i16 < 8 ? 2 + (i16 >> 2) : 4 + ((i16 - 8) >> 1)) * TB_RS, rYe = (17 + (i16 < 8 ? 2 * (i16 & 3) : 4 * (i16 & 1))) * TB_RS;
    const int rZa = (i16 < 8 ? 8 + i16 : 16) * TB_RS;
    const int rB1 = (25 + (i16 >> 2)) * TB_RS, rB0 = (29 + (i16 & 3)) * TB_RS;
    f32x4 kX = {0.f, 0.f, 0.f, 0.f}, kY = kX, kZ = kX;
#pragma unroll
    for (int sb = 0; sb < 4; sb += 2) {
      u32x4 t_[2][7];
#pragma unroll
      for (int s = 0; s < 2; ++s) {
        const char* tq = tr + (sb + s) * 64;
        t_[s][0] = *(const u32x4*)(tq + rB1); t_[s][1] = *(const u32x4*)(tq + rB0); t_[s][2] = *(const u32x4*)(tq + rXa); t_[s][3] = *(const u32x4*)(tq + rXe);
        t_[s][4] = *(const u32x4*)(tq + rYa); t_[s][5] = *(const u32x4*)(tq + rYe); t_[s][6] = *(const u32x4*)(tq + rZa);
      }
      SCHED();
#pragma unroll
      for (int s = 0; s < 2; ++s) {
        const f16x8 hb = __builtin_bit_cast(f16x8, cmulh4(t_[s][0], t_[s][1]));
        kX = __builtin_amdgcn_mfma_f32_16x16x32_f16(__builtin_bit_cast(f16x8, cmulh4(t_[s][2], t_[s][3])), hb, kX, 0, 0, 0);
        kY = __builtin_amdgcn_mfma_f32_16x16x32_f16(__builtin_bit_cast(f16x8, cmulh4(t_[s][4], t_[s][5])), hb, kY, 0, 0, 0);
        kZ = __builtin_amdgcn_mfma_f32_16x16x32_f16(__builtin_bit_cast(f16x8, t_[s][6]), hb, kZ, 0, 0, 0);
      }
      SCHED();
    }
    kgen_store(kX, kY, kZ, isc, lds, cw, lane);
  }
  ARRIVE_L(C_K0 + ch);
  ld_u64(fv, lnfl);
  await_<0>(lds, C_K0 + ch, 2);
  const f32x2 ks = *(const f32x2*)(lds + L_KSC + ch * 8);
  int ex = (int)((__float_as_uint(ks[0] + ks[1]) >> 23) & 255);
  ex = ex < 20 ? 20 : (ex > 230 ? 230 : ex);
  const float scale = __uint_as_float((unsigned)(267 - ex) << 23);
  const float iscale = __uint_as_float((unsigned)(ex - 13) << 23);
  char* kp = lds + L_KP + ch * KPSZ;
  {
    const float* k0 = (const float*)(lds + L_KAP + (2 * ch) * KAPSZ);
#define KSUM2(T, i) ((*(const T*)(k0 + (i)) + *(const T*)(k0 + 644 + (i))) * scale)
    const int A = 64 * hf + lane, A2 = 128 + A;
    const float e0 = KSUM2(float, 512 + A), e1 = KSUM2(float, 513 + A);
    u32x4 o;
    if (hf == 0) {
      const int i0 = lane < 32 ? 8 * A : 256 + 4 * (A - 32);
      const f32x4 a = KSUM2(f32x4, i0), b = KSUM2(f32x4, i0 + 4);
      const bool lo = lane < 32;
      o = (u32x4){pkh(a[0], lo ? a[1] : a[0]), pkh(lo ? a[2] : a[1], lo ? a[3] : a[2]), pkh(lo ? b[0] : a[2], lo ? b[1] : a[2]), pkh(lo ? b[2] : a[3], lo ? b[3] : b[0])};
      if (lane < 2) *(u32x4*)(kp + lane * 16) = (u32x4){0u, 0u, 0u, 0u};
    } else {
      const int i0 = 384 + 2 * lane;
      const f32x2 a = KSUM2(f32x2, i0), b = KSUM2(f32x2, i0 + 2);
      o = (u32x4){pkh(a[0], a[0]), pkh(a[0], a[1]), pkh(a[1], a[1]), pkh(b[0], b[0])};
    }
    *(u32x4*)(kp + 32 + A * 16) = o;
    const unsigned p00 = pkh(e0, e0);
    *(u32x4*)(kp + 32 + A2 * 16) = (u32x4){p00, p00, pkh((A2 & 1) ? e1 : e0, e1), pkh(e1, e1)};
  }
  ARRIVE_L(C_K1);
  asm volatile("s_waitcnt vmcnt(0)" : "+v"(fv) :: "memory");
  arrive(lds, C_W2, lane);
  const bool urok = __all(fv == aOwn + 1);
  u32x4 ua_, ub_, q0, q1, q2, q3;
  const char* qp = ws + WS_ST2 + hf * 4096;
#define U_LOADS() { gload16(ua_, lane * 16, up); gload16(ub_, lane * 16, up + 1024); \
    gload16(q0, lane * 32, qp); gload16(q1, lane * 32, qp + 16); gload16(q2, lane * 32, qp + 2048); gload16(q3, lane * 32, qp + 2064); }
#define U_WAIT() asm volatile("s_waitcnt vmcnt(0)" : "+v"(ua_), "+v"(ub_), "+v"(q0), "+v"(q1), "+v"(q2), "+v"(q3) :: "memory")
  U_LOADS();
  await_<0>(lds, C_K1, 4);
  STAMP();
#pragma unroll
  for (int rnd = 0; rnd < 3; ++rnd) {
    const int a = rnd < 2 ? 128 * rnd + 64 * hf + lane : 256;
    if (rnd == 2 && !(hf == 0 && lane == 0)) break;
    const u32x4 c0 = *(const u32x4*)(kp + a * 16), c1 = *(const u32x4*)(kp + a * 16 + 16);
    const unsigned e[8] = {c0[0], c0[1], c0[2], c0[3], c1[0], c1[1], c1[2], c1[3]};
    unsigned o[7];
#pragma unroll
    for (int i = 0; i < 7; ++i) o[i] = __builtin_amdgcn_alignbit(e[i + 1], e[i], 16);
    const int m = (a & 1) | (((a >> 2) & 1) << 1) | (((a >> 1) & 1) << 2);
    char* base = wl + a * 128;
#pragma unroll
    for (int c = 0; c < 8; ++c) {
      u32x4 wv;
      if (c & 1) { const int s = (c + 1) >> 1; wv = (u32x4){e[s], e[s + 1], e[s + 2], e[s + 3]}; }
      else { const int s = c >> 1; wv = (u32x4){o[s], o[s + 1], o[s + 2], o[s + 3]}; }
      *(u32x4*)(base + ((c ^ m) << 4)) = wv;
    }
  }
  SCHED();
  U_WAIT();
  if (!urok) { poll_flags(lnfl, aOwn + 1); U_LOADS(); U_WAIT(); }
  char* ul = lds + L_U + ch * USZ;
  {
#define UN2(xw, s0, s1) ({ const unsigned a_ = pkbf(bflo(xw) - bflo(s0), bfhi(xw) - bflo(s1)); \
      const unsigned b_ = pkbf(bflo(a_) * __builtin_amdgcn_rcpf(bfhi(s0)), bfhi(a_) * __builtin_amdgcn_rcpf(bfhi(s1))); pkh(bflo(b_), bfhi(b_)); })
    const u32x4 o0 = {UN2(ua_[0], q0[0], q0[1]), UN2(ua_[1], q0[2], q0[3]), UN2(ua_[2], q1[0], q1[1]), UN2(ua_[3], q1[2], q1[3])};
    const u32x4 o1 = {UN2(ub_[0], q2[0], q2[1]), UN2(ub_[1], q2[2], q2[3]), UN2(ub_[2], q3[0], q3[1]), UN2(ub_[3], q3[2], q3[3])};
    *(u32x4*)(ul + hf * 2048 + lane * 16) = o0;
    *(u32x4*)(ul + hf * 2048 + 1024 + lane * 16) = o1;
  }
  if (hf == 0 && lane < 32) *(u32x4*)(ul + 4096 + lane * 16) = (u32x4){0u, 0u, 0u, 0u};
  ARRIVE_L(C_K2 + ch);
  STAMP();
  await_<2>(lds, C_PUB, 8);
  await_<0>(lds, C_K2 + ch, 2);
  __builtin_amdgcn_s_setprio(3);
  STAMP();
  f32x4 acc[8];
#pragma unroll
  for (int i = 0; i < 8; ++i) acc[i] = (f32x4){0.f, 0.f, 0.f, 0.f};
  {
    const int c = i16 + 8 * g;
    int fo[2];
#pragma unroll
    for (int par = 0; par < 2; ++par) {
      const int w = 32 * par + c;
      const int m = ((w >> 3) & 1) | (((w >> 5) & 1) << 1) | (((w >> 4) & 1) << 2);
      fo[par] = (w ^ m) << 4;
    }
    const char* fbase = wl + hf * (128 * 16);
    const char* gbase = ul + (2032 - 16 * i16 + 8 * g + 128 * hf) * 2 - 7 * 512;
#define CF(s, e) (*(const f16x8*)(fbase + fo[(s) & 1] + ((s) >> 1) * 1024 + (e) * 4096))
#define CG(s, mu) (*(const f16x8*)(gbase + (s) * 64 + (7 - (mu)) * 512))
    f16x8 F[2][8], G[2];
#pragma unroll
    for (int e = 0; e < 8; ++e) F[0][e] = CF(0, e);
    G[0] = CG(0, 0);
#pragma unroll
    for (int s = 0; s < 4; ++s) {
#pragma unroll
      for (int mu = 0; mu < 8; ++mu) {
        const int it = s * 8 + mu;
        SCHED();
        if (it + 1 < 32) G[(it + 1) & 1] = CG((it + 1) >> 3, (it + 1) & 7);
        if (s < 3) F[(s + 1) & 1][mu] = CF(s + 1, mu);
        SCHED();
#pragma unroll
        for (int e = 0; e + mu < 8; ++e) acc[e + mu] = __builtin_amdgcn_mfma_f32_16x16x32_f16(F[s & 1][e], G[it & 1], acc[e + mu], 0, 0, 0);
      }
    }
  }
  STAMP();
  ARRIVE_L(C_K1 + 1 + ch); await_<0>(lds, C_K1 + 1 + ch, 2);
  {
    char* dst = wl + hf * 4096 + lane * 16;
#pragma unroll
    for (int i = 0; i < 4; ++i) *(f32x4*)(dst + i * 1024) = sel(hf, acc[i], acc[4 + i]);
  }
  ARRIVE_L(C_K3 + ch); await_<0>(lds, C_K3 + ch, 2);
  STAMP();
  {
    const float dskip = (d & 1) ? bfhi(sds) : bflo(sds);
    const char* src = wl + (hf ^ 1) * 4096 + lane * 16;
    char* sdst = ws + WS_S + (size_t)d * 4096;
#pragma unroll
    for (int i = 0; i < 4; ++i) {
      const f32x4 yv = (sel(hf, acc[4 + i], acc[i]) + *(const f32x4*)(src + i * 1024)) * iscale;
      const int t = 256 * (4 * hf + i) + 16 * i16 + 4 * g;
      const u32x2 uu = *(const u32x2*)(ul + (2044 - t) * 2);
      const unsigned uu0 = uu[0], uu1 = uu[1];
      const f16x2 ua = __builtin_bit_cast(f16x2, uu0), ub = __builtin_bit_cast(f16x2, uu1);
      const unsigned y01 = pkbf(yv[0], yv[1]), y23 = pkbf(yv[2], yv[3]);
      const unsigned k01 = pkbf((float)ub[1] * dskip, (float)ub[0] * dskip), k23 = pkbf((float)ua[1] * dskip, (float)ua[0] * dskip);
      const u64 pk = (u64)pkbf(bflo(y01) + bflo(k01), bfhi(y01) + bfhi(k01)) | ((u64)pkbf(bflo(y23) + bflo(k23), bfhi(y23) + bfhi(k23)) << 32);
      __hip_atomic_store((u64*)(sdst + t * 2), pk, RLX_AGENT);
    }
  }
  asm volatile("s_waitcnt vmcnt(0) lgkmcnt(0)" ::: "memory");
  STAMP();
  if (last_of(lds, C_SP, 4, lane) && lane < 32) __hip_atomic_store((u64*)(ws + WS_FLAG + (size_t)blk_of(lane, cb) * 1024 + FL_S) + mb, aS + 1, RLX_AGENT);
  STAMP_FLUSH();
}

#define FIN_LOADS() { \
  asm volatile("global_load_dwordx4 %0, %1, off" : "=v"(xv) : "v"(x + (size_t)(64 * mb + (tid >> 3)) * D_ + 64 * cb + 8 * (tid & 7)) : "memory"); \
  asm volatile("global_load_dwordx4 %0, %1, off" : "=v"(bv) : "v"(b2 + 64 * cb + 8 * (tid & 7)) : "memory"); }
DI void final_stage(u32x4& xv, u32x4& bv, u16* __restrict__ out, char* lds, int mb, int cb, int tid) {
  await_<0>(lds, C_R1, 4);
  await_<0>(lds, C_ST, 4);
  const int t = tid >> 3, dq = tid & 7;
  const char* src = lds + L_RED + t * RED_RS + dq * 32;
  f32x4 ra[4], rb[4];
#pragma unroll
  for (int k = 0; k < 4; ++k) { ra[k] = *(const f32x4*)(src + k * 64 * RED_RS); rb[k] = *(const f32x4*)(src + k * 64 * RED_RS + 16); }
  const u32x4 sv = *(const u32x4*)(lds + L_ST + t * ST_RS + dq * 16);
  SCHED();
  const f32x4 a = (ra[0] + ra[1]) + (ra[2] + ra[3]), b = (rb[0] + rb[1]) + (rb[2] + rb[3]);
  asm volatile("s_waitcnt vmcnt(0)" : "+v"(xv), "+v"(bv) :: "memory");
  u32x4 o;
#pragma unroll
  for (int c = 0; c < 4; ++c) {
    const float v0 = (c < 2 ? a[2 * c] : b[2 * c - 4]) + bflo(bv[c]), v1 = (c < 2 ? a[2 * c + 1] : b[2 * c - 3]) + bfhi(bv[c]);
    const float g0 = v0 * __builtin_amdgcn_rcpf(1.f + __builtin_amdgcn_exp2f(-1.4426950408889634f * v0));
    const float g1 = v1 * __builtin_amdgcn_rcpf(1.f + __builtin_amdgcn_exp2f(-1.4426950408889634f * v1));
    const unsigned pv = pkbf(bflo(sv[c]) * g0, bfhi(sv[c]) * g1);
    o[c] = pkbf(bflo(xv[c]) + bflo(pv), bfhi(xv[c]) + bfhi(pv));
  }
  *(u32x4*)(out + (size_t)(64 * mb + t) * D_ + 64 * cb + 8 * dq) = o;
}
DI void p2_gemm(const u16* __restrict__ x, const u16* __restrict__ w2, u64 aOwn, const u16* __restrict__ b2, u16* __restrict__ out, char* __restrict__ ws,
                char* lds, int mb, int cb, int kq, int lane TK0_ARG) {
  STAMP_DECL STAMP_INIT(1) STAMP0();
  const int r = lane & 31, h = lane >> 5;
  unsigned* cnt = (unsigned*)(lds + L_CNT);
  const int tid = kq * 64 + lane;
  K2_SETUP(kq);
  char* ring = lds + L_RING2 + kq * (K2_R * 4096);
  const char* flg = ws + WS_FLAG + (size_t)blockIdx.x * 1024;
  const char* hbase = ws + WS_H + (size_t)(2 * mb) * HMT + 28 * kq * 1024;
  const unsigned hoff0 = lane * 16, hoff1 = lane * 16 + HMT;
  const int q = (lane & 15) >> 2;
  const int toff0 = (4 * h + q) * 128 + ((q >> 1) << 6) + ((lane >> 4) & 1) * 32 + 8 * (lane & 3), toff1 = toff0 ^ 64;
  u32x4 hb[3][4];
  s16x8 wf[2][4];
  f32x16 acc[2][2];
#pragma unroll
  for (int i = 0; i < 16; ++i) { acc[0][0][i] = 0.f; acc[0][1][i] = 0.f; acc[1][0][i] = 0.f; acc[1][1][i] = 0.f; }
  await_<1>(lds, C_PUB, 8);
  K2_DMA(2); K2_DMA(3);
  STAMP();
  if (kq == 0) {
    const u64* fp = (const u64*)(flg + FL_H) + (lane < 14 ? lane : 0);
    for (unsigned spin = 0; spin < (1u << 20); ++spin) {
      u64 v;
      asm volatile("global_load_dwordx2 %0, %1, off sc1\n\ts_waitcnt vmcnt(0)" : "=&v"(v) : "v"(fp) : "memory");
      if (__all(v == aOwn + 1)) break;
      __builtin_amdgcn_s_sleep(2);
    }
    lds_set(cnt + C_H, lane);
  } else {
    lds_wait(cnt + C_H);
  }
  STAMP();
#define K2_HL(S, s2) { const char* hs_ = hbase + K2S(S) * 2048 + (s2) * 1024; gload16(hb[(S) % 3][2 * (s2)], hoff0, hs_); gload16(hb[(S) % 3][2 * (s2) + 1], hoff1, hs_); SCHED(); }
#define K2_HLD(S) { K2_HL(S, 0); K2_HL(S, 1); }
#define K2_TR(S, i) { const char* sl_ = ring + ((S) % K2_R) * 4096 + ((i) >> 1) * 2048 + (((i) & 1) ? toff1 : toff0); wf[(S) & 1][i] = tr_frag(sl_, sl_ + 1024); SCHED(); }
#define K2_D1(S, pp) { dma16s(wvoff, wsrc + (size_t)(K2S(S) * 32 + (pp) * 8) * D_, ringa + ((S) % K2_R) * 4096 + (pp) * 1024); SCHED(); }
#define K2_M(S, s2, mi, di) { acc[mi][di] = __builtin_amdgcn_mfma_f32_32x32x16_bf16(wf[(S) & 1][2 * (s2) + (di)], __builtin_bit_cast(s16x8, hb[(S) % 3][2 * (s2) + (mi)]), acc[mi][di], 0, 0, 0); SCHED(); }
#define K2_STEP(S, W, TRN, HN, DN) { \
    asm volatile("s_waitcnt vmcnt(" #W ")" : "+v"(hb[(S) % 3][0]), "+v"(hb[(S) % 3][1]), "+v"(hb[(S) % 3][2]), "+v"(hb[(S) % 3][3]) :: "memory"); SCHED(); \
    K2_M(S,0,0,0); if (TRN) K2_TR((S)+1,0); K2_M(S,0,1,0); if (TRN) K2_TR((S)+1,1); K2_M(S,0,0,1); if (TRN) K2_TR((S)+1,2); K2_M(S,0,1,1); if (TRN) K2_TR((S)+1,3); \
    K2_M(S,1,0,0); if (DN) { K2_D1((S)+4,0); K2_D1((S)+4,1); } K2_M(S,1,1,0); if (DN) { K2_D1((S)+4,2); K2_D1((S)+4,3); } K2_M(S,1,0,1); if (HN) K2_HL((S)+3,0); K2_M(S,1,1,1); if (HN) K2_HL((S)+3,1); }
  K2_HLD(0); K2_HLD(1); K2_HLD(2);
  await_<0>(lds, C_W2, 4);
  K2_TR(0,0); K2_TR(0,1); K2_TR(0,2); K2_TR(0,3);
  K2_STEP(0, 8, 1, 1, 1); STAMP();
  K2_STEP(1, 12, 1, 1, 1);
  K2_STEP(2, 16, 1, 1, 1);
  K2_STEP(3, 16, 1, 1, 1);
  K2_STEP(4, 16, 1, 1, 1);
  K2_STEP(5, 16, 1, 1, 1);
  K2_STEP(6, 16, 1, 1, 1);
  K2_STEP(7, 16, 1, 1, 1); STAMP();
  K2_STEP(8, 16, 1, 1, 1);
  K2_STEP(9, 16, 1, 1, 1);
  K2_STEP(10, 16, 1, 1, 0);
  u32x4 xv, bv;
  FIN_LOADS();
  K2_STEP(11, 14, 1, 0, 0);
  K2_STEP(12, 6, 1, 0, 0);
  K2_STEP(13, 2, 0, 0, 0);
  STAMP();
  await_<0>(lds, C_SP, 4);
  {
    char* dst = lds + L_RED + (kq * 64 + r) * RED_RS + h * 16;
#pragma unroll
    for (int mi = 0; mi < 2; ++mi)
#pragma unroll
      for (int di = 0; di < 2; ++di)
#pragma unroll
        for (int rq = 0; rq < 4; ++rq)
          *(f32x4*)(dst + mi * 32 * RED_RS + di * 128 + rq * 32) = (f32x4){acc[mi][di][4 * rq], acc[mi][di][4 * rq + 1], acc[mi][di][4 * rq + 2], acc[mi][di][4 * rq + 3]};
  }
  ARRIVE_L(C_R1);
  STAMP();
  final_stage(xv, bv, out, lds, mb, cb, tid);
  if (tid == 0) __hip_atomic_store((u64*)(ws + WS_FLAG + (size_t)blockIdx.x * 1024), aOwn + 1, RLX_AGENT);
  STAMP(); STAMP_FLUSH();
}

DI void p2_helper(const u16* __restrict__ x, const u16* __restrict__ w2, u64 aOwn, const u16* __restrict__ b2, u16* __restrict__ out, char* __restrict__ ws,
                  char* lds, int mb, int cb, int gw, int lane) {
  unsigned* cnt = (unsigned*)(lds + L_CNT);
  const int tid = gw * 64 + lane, hw = gw - 4;
  u32x4 xv, bv;
  FIN_LOADS();
  if (hw == 0) {
    const u64* fp = (const u64*)(ws + WS_FLAG + (size_t)blockIdx.x * 1024 + FL_S) + (lane & 31);
    for (unsigned spin = 0; spin < (1u << 18); ++spin) {
      const u64 v = __hip_atomic_load(fp, RLX_AGENT);
      if (__all(v == aOwn + 1)) break;
      __builtin_amdgcn_s_sleep(8);
    }
    asm volatile("" ::: "memory");
    lds_set(cnt + C_S, lane);
  } else {
    while (__hip_atomic_load(cnt + C_S, RLX_WG) == 0u) __builtin_amdgcn_s_sleep(4);
    asm volatile("" ::: "memory");
  }
  u32x4 sa, sb;
  const char* sp = ws + WS_S + (size_t)(64 * cb + 16 * hw) * 4096 + 64 * mb * 2;
  gload16(sa, (lane >> 3) * 4096 + (lane & 7) * 16, sp);
  gload16(sb, (lane >> 3) * 4096 + (lane & 7) * 16, sp + 8 * 4096);
  asm volatile("s_waitcnt vmcnt(0)" : "+v"(sa), "+v"(sb) :: "memory");
  char* dst = lds + L_ST + (8 * (lane & 7)) * ST_RS + (16 * hw + (lane >> 3)) * 2;
#pragma unroll
  for (int c = 0; c < 4; ++c) {
    *(u16*)(dst + (2 * c) * ST_RS) = (u16)sa[c]; *(u16*)(dst + (2 * c + 1) * ST_RS) = (u16)(sa[c] >> 16);
    *(u16*)(dst + (2 * c) * ST_RS + 16) = (u16)sb[c]; *(u16*)(dst + (2 * c + 1) * ST_RS + 16) = (u16)(sb[c] >> 16);
  }
  ARRIVE_L(C_ST);
  while (__hip_atomic_load(cnt + C_R1, RLX_WG) < 4u) __builtin_amdgcn_s_sleep(2);
  final_stage(xv, bv, out, lds, mb, cb, tid);
}

template <int PH>
__global__ __launch_bounds__(768, 3) void fused_kernel(const u16* __restrict__ x, const u16* __restrict__ lar, const u16* __restrict__ aim,
                                                       const u16* __restrict__ cre, const u16* __restrict__ cim, const u16* __restrict__ ldt,
                                                       const u16* __restrict__ dsk, const u16* __restrict__ w1, const u16* __restrict__ b1,
                                                       const u16* __restrict__ w2, const u16* __restrict__ b2, u16* __restrict__ out,
                                                       char* __restrict__ ws) {
  extern __shared__ __attribute__((aligned(16))) char lds[];
  const int lane = threadIdx.x & 63;
  const int wid = __builtin_amdgcn_readfirstlane(threadIdx.x >> 6);
  const int b = blockIdx.x, xcd = b & 7, j = b >> 3;
  const int mb = 4 * xcd + (j >> 3), cb = j & 7;
  const int jj = j < 28 ? j : 0;
  const int panel = 4 * (xcd >> 1) + jj / 7, fs = 7 * (xcd & 1) + jj % 7;
  if (wid < 4) {
    const int de = 64 * cb + 2 * mb;
    const unsigned sdt = *(const __attribute__((address_space(4))) unsigned*)(ldt + de);
    const unsigned sds = *(const __attribute__((address_space(4))) unsigned*)(dsk + de);
    x_wave(lar, aim, cre, cim, sdt, sds, w2, w1, b1, j >= 28 ? xcd * 4 + (j - 28) : -1, panel, fs, ws, lds, mb, cb, wid, lane TK0_PASS);
    return;
  }
  const int gw = wid - 4;
  __builtin_amdgcn_s_setprio(2);
  if (j < 28) p1_gemm(x, w1, ws, lds, panel, fs, gw, lane TK0_PASS);
  else p1_aux(x, ws, lds, xcd * 4 + (j - 28), gw, lane TK0_PASS);
  __builtin_amdgcn_s_setprio(0);
  await_<0>(lds, C_EP, 1);
  const u64 aOwn = *(const u64*)(lds + L_KSC + 16);
  if (gw < 4) p2_gemm(x, w2, aOwn, b2, out, ws, lds, mb, cb, gw, lane TK0_PASS);
  else p2_helper(x, w2, aOwn, b2, out, ws, lds, mb, cb, gw, lane);
}

extern "C" void kernel_launch(void* const* d_in, const int* in_sizes, int n_in, void* d_out, int out_size, void* d_ws, size_t ws_size,
                              hipStream_t stream) {
  const u16* x = (const u16*)d_in[0];
  const u16* lar = (const u16*)d_in[1];
  const u16* aim = (const u16*)d_in[2];
  const u16* cre = (const u16*)d_in[3];
  const u16* cim = (const u16*)d_in[4];
  const u16* ldt = (const u16*)d_in[5];
  const u16* dsk = (const u16*)d_in[6];
  const u16* w1 = (const u16*)d_in[8];
  const u16* b1 = (const u16*)d_in[9];
  const u16* w2 = (const u16*)d_in[10];
  const u16* b2 = (const u16*)d_in[11];
  (void)hipFuncSetAttribute((const void*)fused_kernel<3>, hipFuncAttributeMaxDynamicSharedMemorySize, LDS_TOTAL);
  fused_kernel<3><<<256, 768, LDS_TOTAL, stream>>>(x, lar, aim, cre, cim, ldt, dsk, w1, b1, w2, b2, (u16*)d_out, (char*)d_ws);
}
```

```cpp
#include <hip/hip_runtime.h>
#include <stdint.h>

typedef unsigned short u16;
typedef unsigned long long u64;
typedef __attribute__((ext_vector_type(4))) short s16x4;
typedef __attribute__((ext_vector_type(8))) short s16x8;
typedef __attribute__((ext_vector_type(8))) _Float16 f16x8;
typedef __attribute__((ext_vector_type(2))) _Float16 f16x2;
typedef __attribute__((ext_vector_type(2))) __bf16 bf16x2;
typedef __attribute__((ext_vector_type(2))) float f32x2;
typedef __attribute__((ext_vector_type(4))) float f32x4;
typedef __attribute__((ext_vector_type(16))) float f32x16;
typedef __attribute__((ext_vector_type(4))) unsigned u32x4;
typedef __attribute__((ext_vector_type(2))) unsigned u32x2;
#define LDSP __attribute__((address_space(3)))
#define GLBP __attribute__((address_space(1)))
#define DI __device__ __forceinline__

#define NLAUNCH 1

#define XPRIO 3
#define L_ 2048
#define D_ 512
#define N_ 128
#define F_ 1792

#define WS_H 0
#define WS_UR (8u << 20)
#define WS_S (10u << 20)
#define WS_STAT (12u << 20)
#define WS_CS ((12u << 20) + 65536)
#define WS_ST2 ((12u << 20) + 131072)
#define WS_FLAG (13u << 20)
#define WS_KAP (16u << 20)
#define FL_LN 128
#define FL_H 384
#define FL_S 512
#define FL_ST 768

#define STAMP_INIT(kid)
#define STAMP()
#define STAMP0()
#define STAMP_DECL
#define STAMP_FLUSH()
#define TK0_ARG
#define TK0_PASS

DI float bf2f(unsigned v) { return __uint_as_float(v << 16); }
DI float bflo(unsigned v) { return __uint_as_float(v << 16); }
DI float bfhi(unsigned v) { return __uint_as_float(v & 0xffff0000u); }
DI unsigned pkbf(float a, float b) { bf16x2 q = {(__bf16)a, (__bf16)b}; return __builtin_bit_cast(unsigned, q); }
DI float rbf(float a) { __bf16 q = (__bf16)a; return (float)q; }
DI unsigned pkh(float a, float b) { f32x2 v = {a, b}; f16x2 p = __builtin_convertvector(v, f16x2); return __builtin_bit_cast(unsigned, p); }
DI float dot2(unsigned a, unsigned b, float c) { return __builtin_amdgcn_fdot2_f32_bf16(__builtin_bit_cast(bf16x2, a), __builtin_bit_cast(bf16x2, b), c, false); }
template <class T> DI T sel(int c, T a, T b) { return c ? a : b; }
DI void dma16(const void* g, char* l) { __builtin_amdgcn_global_load_lds((const GLBP unsigned*)g, (LDSP unsigned*)l, 16, 0, 0); }
DI void dma16s(unsigned voff, const void* sbase, unsigned ldsaddr) {
  asm volatile("s_mov_b32 m0, %2\n\ts_nop 0\n\tglobal_load_lds_dwordx4 %0, %1" :: "v"(voff), "s"(sbase), "s"(ldsaddr) : "memory");
}
DI unsigned lds_base(char* lds) { return __builtin_amdgcn_readfirstlane((unsigned)(size_t)(LDSP char*)lds); }
DI s16x8 tr_frag(const char* p0, const char* p1) {
  s16x4 a = __builtin_amdgcn_ds_read_tr16_b64_v4i16((LDSP s16x4*)p0);
  s16x4 b = __builtin_amdgcn_ds_read_tr16_b64_v4i16((LDSP s16x4*)p1);
  return __builtin_shufflevector(a, b, 0, 1, 2, 3, 4, 5, 6, 7);
}
#define SCHED() __builtin_amdgcn_sched_barrier(0)
#define WAITV_BAR(n) do { SCHED(); asm volatile("s_waitcnt vmcnt(" #n ") lgkmcnt(0)\n\ts_barrier" ::: "memory"); SCHED(); } while (0)
#define LGKM_BAR() do { SCHED(); asm volatile("s_waitcnt lgkmcnt(0)\n\ts_barrier" ::: "memory"); SCHED(); } while (0)
#define RLX_AGENT __ATOMIC_RELAXED, __HIP_MEMORY_SCOPE_AGENT
#define RLX_WG __ATOMIC_RELAXED, __HIP_MEMORY_SCOPE_WORKGROUP

template <int CTRL, int RM> DI float dpp_add(float v) {
  return v + __builtin_bit_cast(float, __builtin_amdgcn_update_dpp(0, __builtin_bit_cast(int, v), CTRL, RM, 0xf, true));
}
DI float wave_sum63(float v) {
  v = dpp_add<0x111, 0xf>(v); v = dpp_add<0x112, 0xf>(v); v = dpp_add<0x114, 0xf>(v); v = dpp_add<0x118, 0xf>(v);
  v = dpp_add<0x142, 0xa>(v); v = dpp_add<0x143, 0xc>(v);
  return v;
}
DI float comb32(float a, float b) { auto r = __builtin_amdgcn_permlane32_swap(__float_as_uint(a), __float_as_uint(b), false, false); return __uint_as_float(r[0]) + __uint_as_float(r[1]); }
DI float comb16(float a, float b) { auto r = __builtin_amdgcn_permlane16_swap(__float_as_uint(a), __float_as_uint(b), false, false); return __uint_as_float(r[0]) + __uint_as_float(r[1]); }
DI float row_allsum(float v) { v = dpp_add<0x128, 0xf>(v); v = dpp_add<0x124, 0xf>(v); v = dpp_add<0x122, 0xf>(v); v = dpp_add<0x121, 0xf>(v); return v; }
DI float rdlane(float v, int l) { return __builtin_bit_cast(float, __builtin_amdgcn_readlane(__builtin_bit_cast(int, v), l)); }

DI void group_barrier(unsigned* cnt, unsigned target, int lane) {
  asm volatile("s_waitcnt lgkmcnt(0)" ::: "memory");
  if (lane == 0) __hip_atomic_fetch_add(cnt, 1u, RLX_WG);
  while (__hip_atomic_load(cnt, RLX_WG) < target) __builtin_amdgcn_s_sleep(1);
  asm volatile("" ::: "memory");
}
DI void poll_flags(const u64* p, u64 expect) {
  for (unsigned spin = 0; spin < (1u << 20); ++spin) {
    const u64 v = __hip_atomic_load(p, RLX_AGENT);
    if (__all(v == expect)) break;
    __builtin_amdgcn_s_sleep(2);
  }
  asm volatile("" ::: "memory");
}
DI void lds_set(unsigned* w, int lane) { if (lane == 0) __hip_atomic_store(w, 1u, RLX_WG); }
DI void lds_wait(unsigned* w) { while (__hip_atomic_load(w, RLX_WG) == 0u) __builtin_amdgcn_s_sleep(1); asm volatile("" ::: "memory"); }
DI int blk_of(int mb, int cb) { return (mb >> 2) + 8 * (((mb & 3) << 3) | cb); }

#define LDS_TOTAL 163840
#define L_STAT 162304
#define L_BIAS 163328
#define L_ACON 163584
#define L_KSC 163712
#define L_CNT 163744
#define KAPSZ 2576
#define L_KAP 141696
#define L_COLS 152000
#define K1_SLOT 32768
#define L_TB 98304
#define L_KP 131584
#define KPSZ 4160
#define K2_R 4
#define L_RING2 0
#define L_W 65536
#define WSZ 33024
#define L_U L_KAP
#define USZ 4608
#define L_RED L_W
#define RED_RS 272
#define L_ST L_KAP
#define ST_RS 144
#define C_PUB 0
#define C_SP 1
#define C_H 2
#define C_S 3
#define C_W2 4
#define C_R1 5
#define C_ST 6
#define C_K0 7
#define C_K1 9
#define C_K2 12
#define C_K3 14
#define C_LN1 16
#define C_LN2 17
#define C_X2 18
#define C_CS 19
#define C_EP 20
DI void arrive(char* lds, int c, int lane) { if (lane == 0) __hip_atomic_fetch_add((unsigned*)(lds + L_CNT) + c, 1u, RLX_WG); }
template <int SL> DI void await_(char* lds, int c, unsigned target) {
  while (__hip_atomic_load((unsigned*)(lds + L_CNT) + c, RLX_WG) < target) __builtin_amdgcn_s_sleep(SL);
  asm volatile("" ::: "memory"); SCHED();
}
#define ARRIVE_V(n, c) do { SCHED(); asm volatile("s_waitcnt vmcnt(" #n ") lgkmcnt(0)" ::: "memory"); arrive(lds, c, lane); SCHED(); } while (0)
#define ARRIVE_L(c) do { SCHED(); asm volatile("s_waitcnt lgkmcnt(0)" ::: "memory"); arrive(lds, c, lane); SCHED(); } while (0)

struct cf { float x, y; };
DI cf cmul(cf a, cf b) { return cf{fmaf(a.x, b.x, -(a.y * b.y)), fmaf(a.x, b.y, a.y * b.x)}; }
DI unsigned pkc(cf a) { return pkh(a.x, a.y); }
DI unsigned pkcc(cf a) { return pkh(a.x, -a.y); }
DI cf zpow(float al, float om, float p) {
  const float m = __builtin_amdgcn_exp2f(al * p), ph = __builtin_amdgcn_fractf(om * p);
  return cf{m * __builtin_amdgcn_cosf(ph), m * __builtin_amdgcn_sinf(ph)};
}
DI unsigned cmulh(unsigned a, unsigned b) {
  const f16x2 z = __builtin_bit_cast(f16x2, a), w = __builtin_bit_cast(f16x2, b);
  const f16x2 t = z * (f16x2){w[0], w[0]};
  const f16x2 r = __builtin_elementwise_fma((f16x2){-z[1], z[0]}, (f16x2){w[1], w[1]}, t);
  return __builtin_bit_cast(unsigned, r);
}
DI u32x4 cmulh4(u32x4 a, u32x4 b) { return (u32x4){cmulh(a[0], b[0]), cmulh(a[1], b[1]), cmulh(a[2], b[2]), cmulh(a[3], b[3])}; }
#define TB_RS 272
#define TB_N 33
DI void kgen_tables(unsigned (&T)[TB_N], float& isc, unsigned raw0, unsigned raw1, float ldtv, char* lds, int cw, int lane) {
  const int src = ((lane & 31) >> 1) * 4, sh = (lane & 1) ? 0 : 16;
  unsigned pv[4];
#pragma unroll
  for (int a = 0; a < 4; ++a) {
    const unsigned lo = (unsigned)__builtin_amdgcn_ds_bpermute(src + 64 * a, (int)raw0), hi = (unsigned)__builtin_amdgcn_ds_bpermute(src + 64 * a, (int)raw1);
    pv[a] = ((lane < 32 ? lo : hi) << sh) & 0xffff0000u;
  }
  const float dt = rbf(__builtin_amdgcn_exp2f(ldtv * 1.4426950408889634f));
  const float ar = rbf(__builtin_amdgcn_exp2f(__uint_as_float(pv[0]) * 1.4426950408889634f));
  const float al = -(ar * dt) * 1.4426950408889634f;
  const float om = (__uint_as_float(pv[1]) * dt) * 0.15915494309189535f;
  const float cr = __uint_as_float(pv[2]) * dt, ci = __uint_as_float(pv[3]) * dt;
  const float sabs = 2.f * rdlane(wave_sum63(fabsf(cr) + fabsf(ci)), 63);
  int ex = (int)((__float_as_uint(sabs) >> 23) & 255);
  ex = ex < 20 ? 20 : (ex > 230 ? 230 : ex);
  const float sc2 = __uint_as_float((unsigned)(268 - ex) << 23);
  isc = __uint_as_float((unsigned)(ex - 13) << 23);
  if (lane == 0) *(float*)(lds + L_KSC + cw * 4) = sabs;
  const cf z1 = zpow(al, om, 1.f), z8 = zpow(al, om, 8.f), z32 = zpow(al, om, 32.f), a1 = zpow(al, om, 128.f), a4 = zpow(al, om, 512.f);
  const unsigned one = 0x00003C00u;
  const cf a2 = cmul(a1, a1), a3 = cmul(a2, a1), a8 = cmul(a4, a4), a12 = cmul(a8, a4);
  T[0] = one; T[1] = pkc(a1); T[2] = pkc(a2); T[3] = pkc(a3);
  T[4] = pkc(a4); T[5] = pkc(cmul(a4, a1)); T[6] = pkc(cmul(a4, a2)); T[7] = pkc(cmul(a4, a3));
  T[8] = pkc(a8); T[9] = pkc(cmul(a8, a1)); T[10] = pkc(cmul(a8, a2)); T[11] = pkc(cmul(a8, a3));
  T[12] = pkc(a12); T[13] = pkc(cmul(a12, a1)); T[14] = pkc(cmul(a12, a2)); T[15] = pkc(cmul(a12, a3));
  T[16] = pkc(cmul(a8, a8));
  const cf e2 = cmul(z1, z1), e3 = cmul(e2, z1), e4 = cmul(e2, e2);
  T[17] = one; T[18] = pkc(z1); T[19] = pkc(e2); T[20] = pkc(e3); T[21] = pkc(e4);
  T[22] = pkc(cmul(e4, z1)); T[23] = pkc(cmul(e4, e2)); T[24] = pkc(cmul(e4, e3));
  const cf c0 = cf{cr * sc2, ci * sc2}, c2 = cmul(c0, cmul(z32, z32));
  T[25] = pkcc(c0); T[26] = pkcc(cmul(c0, z32)); T[27] = pkcc(c2); T[28] = pkcc(cmul(c2, z32));
  const cf z16 = cmul(z8, z8);
  T[29] = one; T[30] = pkcc(z8); T[31] = pkcc(z16); T[32] = pkcc(cmul(z16, z8));
}
#define KG_PARAMS f32x4& kX, f32x4& kY, f32x4& kZ, float isc
DI void kgen_store(KG_PARAMS, char* lds, int wid, int lane) {
  const int i16 = lane & 15, g = lane >> 4;
  float* kap = (float*)(lds + L_KAP + wid * KAPSZ);
  *(f32x4*)(kap + 128 * (g >> 1) + 4 * (g & 1) + 8 * i16) = kX * isc;
  if (g < 2) {
    *(f32x4*)(kap + 256 + 64 * g + 4 * i16) = kY * isc;
#pragma unroll
    for (int r = 0; r < 4; ++r) kap[512 + 16 * (4 * g + r) + i16] = kZ[r] * isc;
  } else {
    *(f32x2*)(kap + 384 + 64 * (g - 2) + 2 * i16) = (f32x2){kY[0] * isc, kY[1] * isc};
    *(f32x2*)(kap + 384 + 64 * (g - 2) + 32 + 2 * i16) = (f32x2){kY[2] * isc, kY[3] * isc};
    if (g == 2 && i16 == 0) kap[640] = kZ[0] * isc;
  }
}
DI const u16* prm_ptr(const u16* lar, const u16* aim, const u16* cre, const u16* cim, int d, int hf, int lane) {
  const int g = lane >> 4;
  const u16* p = g == 0 ? lar : (g == 1 ? aim : (g == 2 ? cre : cim));
  return p + d * N_ + 64 * hf + 2 * (lane & 15);
}
DI void ld_u32(unsigned& v, const void* p) { asm volatile("global_load_dword %0, %1, off" : "=v"(v) : "v"(p) : "memory"); }
DI void ld_u64(u64& v, const void* p) { asm volatile("global_load_dwordx2 %0, %1, off sc1" : "=v"(v) : "v"(p) : "memory"); }
DI void gload16(u32x4& v, unsigned voff, const char* sbase) {
  asm volatile("global_load_dwordx4 %0, %1, %2 sc1" : "=v"(v) : "v"(voff), "s"(sbase) : "memory");
}
DI u64 rdlane64(u64 v, int l) { return (u64)(unsigned)__builtin_amdgcn_readlane((int)(unsigned)v, l) | ((u64)(unsigned)__builtin_amdgcn_readlane((int)(unsigned)(v >> 32), l) << 32); }
DI bool last_of(char* lds, int c, unsigned n, int lane) {
  unsigned old = 0;
  if (lane == 0) old = __hip_atomic_fetch_add((unsigned*)(lds + L_CNT) + c, 1u, RLX_WG);
  return (unsigned)__builtin_amdgcn_readfirstlane(old) == n - 1;
}
DI float gelu_f(float v) {
  const float c0 = -2.302208198f;
  const float c1 = -0.1029432397f;
  const float z = v * fmaf(v * v, c1, c0);
  return v * __builtin_amdgcn_rcpf(1.f + __builtin_amdgcn_exp2f(z));
}
#define K2S(S) ((S) + r2 >= 14 ? (S) + r2 - 14 : (S) + r2)
#define K2_DMA(S) { _Pragma("unroll") for (int pp = 0; pp < 4; ++pp) dma16s(wvoff, wsrc + (size_t)(K2S(S) * 32 + pp * 8) * D_, ringa + ((S) % K2_R) * 4096 + pp * 1024); SCHED(); }
#define K2_SETUP(kq_) int lane2_ = lane; asm volatile("" : "+v"(lane2_)); \
  const unsigned wvoff = (lane2_ >> 3) * (D_ * 2) + (((lane2_ & 7) ^ (((lane2_ >> 4) & 1) << 2)) << 4); \
  const unsigned ringa = __builtin_amdgcn_readfirstlane(lds_base(lds) + L_RING2 + (kq_) * (K2_R * 4096)); \
  const u16* wsrc = w2 + (size_t)(14 * (kq_) * 32) * D_ + 64 * cb; \
  const int r2 = (2 * cb + 4 * (mb & 3)) % 14;
#define HMT 114688

DI void p1_gemm(const u16* __restrict__ x, const u16* __restrict__ w1, char* __restrict__ ws, char* lds, int panel, int fs, int wid, int lane TK0_ARG) {
  STAMP_DECL STAMP_INIT(0) STAMP0();
  const int wm = wid >> 2, wf = (wid >> 1) & 1, kh = wid & 1;
  const int r = lane & 31, h = lane >> 5;
  const int row0 = panel * 128, f0 = fs * 128;
  const unsigned xv0 = (lane >> 3) * 1024 + (((lane & 7) ^ ((lane >> 4) & 3)) << 4);
  const unsigned xv1 = xv0 ^ 64;
  const unsigned wv = (lane >> 4) * (F_ * 2) + (((lane & 15) ^ (((lane >> 4) & 3) << 2)) << 4);
  const u16* xsb = x + (size_t)(row0 + 16 * wid) * D_;
  const u16* wsb = w1 + (size_t)(8 * wid) * F_ + f0;
  const unsigned la = lds_base(lds) + wid * 2048;
  const int rot = 2 * (panel & 3) + (fs >= 7 ? fs - 7 : fs);
#define KS(S) (((S) + rot) & 7)
#define K1_D0(S) { dma16s(xv0, xsb + KS(S) * 64, la + ((S) % 4) * K1_SLOT); SCHED(); }
#define K1_D1(S) { dma16s(xv1, xsb + 8 * D_ + KS(S) * 64, la + ((S) % 4) * K1_SLOT + 1024); SCHED(); }
#define K1_D2(S) { dma16s(wv, wsb + (size_t)(KS(S) * 64) * F_, la + ((S) % 4) * K1_SLOT + 16384); SCHED(); }
#define K1_D3(S) { dma16s(wv, wsb + (size_t)(KS(S) * 64 + 4) * F_, la + ((S) % 4) * K1_SLOT + 16384 + 1024); SCHED(); }
#define K1_ISSUE(S) { K1_D0(S); K1_D1(S); K1_D2(S); K1_D3(S); }
  K1_ISSUE(0); K1_ISSUE(1); K1_ISSUE(2); K1_ISSUE(3);
  const int xoff = (64 * wm + r) * 128 + (((4 * kh + 2 * wm + h) ^ ((r >> 1) & 7)) << 4);
  const int q = (lane & 15) >> 2, p = lane & 3;
  const int woff0 = 16384 + (32 * kh + 16 * wm + 8 * h + q) * 256 + (((2 * wf + kh) ^ q) << 6) + ((lane >> 4) & 1) * 32 + 8 * p;
  f32x16 acc[2][2];
#pragma unroll
  for (int i = 0; i < 16; ++i) { acc[0][0][i] = 0.f; acc[0][1][i] = 0.f; acc[1][0][i] = 0.f; acc[1][1][i] = 0.f; }
  STAMP();
  s16x8 xf[2][2][2], wfr[2][2][2];
#define K1_M(T, s, mi, fi) { acc[mi][fi] = __builtin_amdgcn_mfma_f32_32x32x16_bf16(wfr[(T) & 1][s][fi], xf[(T) & 1][s][mi], acc[mi][fi], 0, 0, 0); SCHED(); }
#define K1_RX(T, s, mi) { xf[(T) & 1][s][mi] = *(const s16x8*)(lds + (xa_ ^ ((s) << 5)) + (mi) * 4096); SCHED(); }
#define K1_RW(T, s, fi) { wfr[(T) & 1][s][fi] = tr_frag(lds + (wa_ ^ (((fi) << 6) | ((s) << 12))), lds + (wa_ ^ (((fi) << 6) | ((s) << 12))) + 1024); SCHED(); }
#define K1_ADDR(T) int xa_ = xoff + ((T) % 4) * K1_SLOT, wa_ = woff0 + ((T) % 4) * K1_SLOT; asm volatile("" : "+v"(xa_), "+v"(wa_));
#define K1_READ(T) { K1_ADDR(T) K1_RX(T,0,0); K1_RW(T,0,0); K1_RW(T,0,1); K1_RX(T,0,1); K1_RX(T,1,0); K1_RW(T,1,0); K1_RW(T,1,1); K1_RX(T,1,1); }
#define K1_MMA(T) { K1_M(T,0,0,0); K1_M(T,0,0,1); K1_M(T,0,1,0); K1_M(T,0,1,1); K1_M(T,1,0,0); K1_M(T,1,0,1); K1_M(T,1,1,0); K1_M(T,1,1,1); }
#define K1_STAGE(T, DM0, DM1, DM2, DM3) { K1_ADDR(T) \
    K1_M((T)-1,0,0,0); K1_RX(T,0,0); K1_RW(T,0,0); K1_M((T)-1,0,0,1); K1_RW(T,0,1); K1_RX(T,0,1); \
    K1_M((T)-1,0,1,0); K1_RX(T,1,0); K1_RW(T,1,0); K1_M((T)-1,0,1,1); K1_RW(T,1,1); K1_RX(T,1,1); \
    K1_M((T)-1,1,0,0); DM0; K1_M((T)-1,1,0,1); DM1; K1_M((T)-1,1,1,0); DM2; K1_M((T)-1,1,1,1); DM3; }
#define K1_STAGE_D(T, S) K1_STAGE(T, K1_D0(S), K1_D1(S), K1_D2(S), K1_D3(S))
  WAITV_BAR(12); STAMP();
  K1_READ(0);
  WAITV_BAR(8); K1_STAGE_D(1, 4);
  WAITV_BAR(8); K1_STAGE_D(2, 5);
  WAITV_BAR(8); K1_STAGE_D(3, 6);
  WAITV_BAR(8); STAMP(); K1_STAGE_D(4, 7);
  WAITV_BAR(8); K1_STAGE(5, , , , );
  WAITV_BAR(4); K1_STAGE(6, , , , );
  K1_MMA(6);
  WAITV_BAR(0);
  K1_READ(7); K1_MMA(7);
  STAMP();
  float* colS = (float*)(lds + L_COLS);
  {
    char* dst = lds + wid * 8192 + lane * 16;
#pragma unroll
    for (int mi = 0; mi < 2; ++mi)
#pragma unroll
      for (int rq = 0; rq < 4; ++rq)
        *(f32x4*)(dst + (mi * 4 + rq) * 1024) = (f32x4){acc[mi][1][4 * rq], acc[mi][1][4 * rq + 1], acc[mi][1][4 * rq + 2], acc[mi][1][4 * rq + 3]};
  }
  LGKM_BAR();
  STAMP();
  f32x4 pa[2][4], cq[4];
  f32x2 st[2];
  u32x2 bq[4];
  {
    const char* src = lds + (wid ^ 1) * 8192 + lane * 16;
    const int fl = 64 * wf + 32 * kh + 4 * h;
#pragma unroll
    for (int rq = 0; rq < 4; ++rq) {
      cq[rq] = *(const f32x4*)(colS + fl + 8 * rq);
      bq[rq] = *(const u32x2*)(lds + L_BIAS + (fl + 8 * rq) * 2);
      pa[0][rq] = *(const f32x4*)(src + rq * 1024); pa[1][rq] = *(const f32x4*)(src + (4 + rq) * 1024);
    }
    st[0] = *(const f32x2*)(lds + L_STAT + (64 * wm + r) * 8); st[1] = *(const f32x2*)(lds + L_STAT + (64 * wm + 32 + r) * 8);
  }
  LGKM_BAR();
  {
    const __amdgpu_buffer_rsrc_t hr = __builtin_amdgcn_make_buffer_rsrc(ws + WS_H, 0, L_ * F_ * 2, 0x00020000);
    const unsigned hoff = (unsigned)(panel * 4 + 2 * wm) * HMT + (8 * fs + 4 * wf + 2 * kh) * 1024 + lane * 16;
#pragma unroll
    for (int mi = 0; mi < 2; ++mi) {
      const float mr = st[mi][0], rinv = st[mi][1];
#pragma unroll
      for (int s2 = 0; s2 < 2; ++s2) {
        u32x4 pk;
#pragma unroll
        for (int qq = 0; qq < 2; ++qq) {
          const int rq = 2 * s2 + qq;
          const float b0 = bflo(bq[rq][0]), b1_ = bfhi(bq[rq][0]), b2_ = bflo(bq[rq][1]), b3 = bfhi(bq[rq][1]);
          const float o0 = gelu_f(fmaf(acc[mi][0][4 * rq] + pa[mi][rq][0], rinv, fmaf(mr, cq[rq][0], b0)));
          const float o1 = gelu_f(fmaf(acc[mi][0][4 * rq + 1] + pa[mi][rq][1], rinv, fmaf(mr, cq[rq][1], b1_)));
          const float o2 = gelu_f(fmaf(acc[mi][0][4 * rq + 2] + pa[mi][rq][2], rinv, fmaf(mr, cq[rq][2], b2_)));
          const float o3 = gelu_f(fmaf(acc[mi][0][4 * rq + 3] + pa[mi][rq][3], rinv, fmaf(mr, cq[rq][3], b3)));
          pk[2 * qq] = pkbf(o0, o1); pk[2 * qq + 1] = pkbf(o2, o3);
        }
        __builtin_amdgcn_raw_buffer_store_b128(pk, hr, hoff + mi * HMT + s2 * 1024, 0, 16);
        SCHED();
      }
    }
  }
  STAMP();
  asm volatile("s_waitcnt vmcnt(0)" ::: "memory");
  if (last_of(lds, C_PUB, 8, lane) && lane < 16) {
    const u64 ac = *(const u64*)(lds + L_ACON + lane * 8);
    int l2 = lane; asm volatile("" : "+v"(l2));
    __hip_atomic_store((u64*)(ws + WS_FLAG + (size_t)blk_of(2 * panel + ((l2 >> 3) & 1), l2 & 7) * 1024 + FL_H) + fs, ac + 1, RLX_AGENT);
  }
  STAMP(); STAMP_FLUSH();
}

#define LN_RS 1040
DI void p1_aux(const u16* __restrict__ x, char* __restrict__ ws, char* lds, int idx, int wid, int lane TK0_ARG) {
  STAMP_DECL STAMP_INIT(0) STAMP0();
  const int t0 = idx * 64;
  u32x4 v[8];
#pragma unroll
  for (int i = 0; i < 8; ++i) v[i] = *(const u32x4*)(x + (size_t)(t0 + 8 * wid + i) * D_ + lane * 8);
  const u64 aCon = __hip_atomic_load((const u64*)(ws + WS_FLAG + (size_t)((wid * 64 + lane) & 255) * 1024), RLX_AGENT);
  const int sfs = lane < 14 ? lane : 0;
  const int sblk = 2 * (idx >> 3) + (sfs >= 7 ? 1 : 0) + 8 * (((idx >> 1) & 3) * 7 + (sfs >= 7 ? sfs - 7 : sfs));
  const u64 aSt = __hip_atomic_load((const u64*)(ws + WS_FLAG + (size_t)sblk * 1024), RLX_AGENT);
  SCHED();
  asm volatile("s_barrier" ::: "memory");
  SCHED();
#pragma unroll
  for (int i = 0; i < 8; ++i) *(u32x4*)(lds + (63 - (8 * wid + i)) * LN_RS + lane * 16) = v[i];
  ARRIVE_L(C_LN1);
  float sA[8], sB[8];
#pragma unroll
  for (int i = 0; i < 8; ++i) {
    float a = dot2(v[i][0], 0x3F803F80u, 0.f), b = dot2(v[i][0], v[i][0], 0.f);
#pragma unroll
    for (int c = 1; c < 4; ++c) { a = dot2(v[i][c], 0x3F803F80u, a); b = dot2(v[i][c], v[i][c], b); }
    sA[i] = a; sB[i] = b;
  }
#pragma unroll
  for (int i = 0; i < 4; ++i) { sA[i] = comb32(sA[i], sA[i + 4]); sB[i] = comb32(sB[i], sB[i + 4]); }
#pragma unroll
  for (int i = 0; i < 2; ++i) { sA[i] = row_allsum(comb16(sA[i], sA[i + 2])); sB[i] = row_allsum(comb16(sB[i], sB[i + 2])); }
  const float mv = ((lane & 1) ? sA[1] : sA[0]) * (1.f / 512.f);
  const float var = fmaxf(fmaf(-mv, mv, ((lane & 1) ? sB[1] : sB[0]) * (1.f / 512.f)), 0.f);
  const float mbv = rbf(mv);
  const float sd = rbf(sqrtf(rbf(rbf(var) + 1e-5f)));
  const float rv = 1.f / sd;
  if ((lane & 14) == 0) {
    const u64 pk = (u64)__float_as_uint(-mbv * rv) | ((u64)__float_as_uint(rv) << 32);
    const int tt = t0 + 8 * wid + (lane & 1) + 2 * (lane >> 4);
    __hip_atomic_store((u64*)(ws + WS_STAT) + tt, pk, RLX_AGENT);
    __hip_atomic_store((unsigned*)(ws + WS_ST2) + (L_ - 1 - tt), (__float_as_uint(mbv) >> 16) | __float_as_uint(sd), RLX_AGENT);
  }
  STAMP();
  await_<0>(lds, C_LN1, 8);
  char* t2 = lds + 64 * LN_RS + wid * 8192;
  const int g = lane >> 4, q = (lane & 15) >> 2, p = lane & 3, dl = lane & 15;
  s16x8 f[8];
#pragma unroll
  for (int i = 0; i < 8; ++i) {
    const char* a = lds + (8 * (4 * (i & 1) + g) + q) * LN_RS + (64 * wid + 16 * (i >> 1) + 4 * p) * 2;
    f[i] = tr_frag(a, a + 4 * LN_RS);
  }
  SCHED();
#pragma unroll
  for (int i = 0; i < 8; ++i) {
    const int dd2 = 16 * (i >> 1) + dl, rc = 4 * (i & 1) + g;
    *(s16x8*)(t2 + dd2 * 128 + ((rc ^ (dd2 & 7)) << 4)) = f[i];
  }
  SCHED();
  const __amdgpu_buffer_rsrc_t ur = __builtin_amdgcn_make_buffer_rsrc(ws + WS_UR, 0, L_ * D_ * 2, 0x00020000);
  const int x0 = L_ - 64 - t0;
  u32x4 o[8];
#pragma unroll
  for (int i = 0; i < 8; ++i) {
    const int dd2 = 8 * i + (lane >> 3), c = lane & 7;
    o[i] = *(const u32x4*)(t2 + dd2 * 128 + ((c ^ (dd2 & 7)) << 4));
  }
  SCHED();
  asm volatile("s_waitcnt vmcnt(0)" ::: "memory");
  if (last_of(lds, C_PUB, 8, lane) && lane < 14) __hip_atomic_store((u64*)(ws + WS_FLAG + (size_t)sblk * 1024 + FL_ST) + (idx & 1), aSt + 1, RLX_AGENT);
  STAMP();
#pragma unroll
  for (int i = 0; i < 8; ++i)
    __builtin_amdgcn_raw_buffer_store_b128(o[i], ur, (unsigned)(64 * wid + 8 * i + (lane >> 3)) * 4096 + x0 * 2 + (lane & 7) * 16, 0, 16);
  STAMP();
  ARRIVE_L(C_X2);
  ARRIVE_V(0, C_LN2);
  if (wid < 4) {
    await_<0>(lds, C_LN2, 8);
    __hip_atomic_store((u64*)(ws + WS_FLAG + (size_t)(wid * 64 + lane) * 1024 + FL_LN) + idx, aCon + 1, RLX_AGENT);
  }
  STAMP(); STAMP_FLUSH();
}

DI void x_wave(const u16* __restrict__ lar, const u16* __restrict__ aim, const u16* __restrict__ cre, const u16* __restrict__ cim,
               unsigned sdt, unsigned sds, const u16* __restrict__ w2, const u16* __restrict__ w1, const u16* __restrict__ b1, int auxidx, int panel, int fs,
               char* __restrict__ ws, char* lds, int mb, int cb, int cw, int lane TK0_ARG) {
  STAMP_DECL STAMP_INIT(1) STAMP0();
  __builtin_amdgcn_s_setprio(3);
  const bool aux = auxidx >= 0;
  const int ch = cw >> 1, hf = cw & 1;
  const int d = 64 * cb + 2 * mb + ch;
  const int i16 = lane & 15, g = lane >> 4;
  unsigned* cnt = (unsigned*)(lds + L_CNT);
  const char* flg = ws + WS_FLAG + (size_t)blockIdx.x * 1024;
  const u16* pp_ = prm_ptr(lar, aim, cre, cim, d, hf, lane);
  unsigned raw0, raw1;
  u64 aS, aH;
  ld_u32(raw0, pp_); ld_u32(raw1, pp_ + 32);
  ld_u64(aS, ws + WS_FLAG + (size_t)(lane >= 32 ? (int)blockIdx.x : blk_of(lane, cb)) * 1024);
  ld_u64(aH, ws + WS_FLAG + (size_t)blk_of(2 * panel + ((lane >> 3) & 1), lane & 7) * 1024);
  if (!aux && cw == 2) asm volatile("s_mov_b32 m0, %2\n\ts_nop 0\n\tglobal_load_lds_dword %0, %1" :: "v"(lane * 4), "s"(b1 + fs * 128), "s"(lds_base(lds) + L_BIAS) : "memory");
  if (cw == 0 && lane < 24) cnt[lane] = 0u;
  if (aux) {
    asm volatile("s_waitcnt lgkmcnt(0)\n\ts_barrier" ::: "memory");
    __builtin_amdgcn_s_setprio(1);
    if (auxidx < 28) {
      const u16* wp_ = w1 + (size_t)(128 * cw + (lane >> 3)) * F_ + 64 * auxidx + 8 * (lane & 7);
      const int cblk = 2 * ((lane & 15) >> 2) + (auxidx >= 14 ? 1 : 0) + 8 * ((lane & 3) * 7 + ((auxidx >> 1) % 7));
      const u64 aC = __hip_atomic_load((const u64*)(ws + WS_FLAG + (size_t)cblk * 1024), RLX_AGENT);
      float a8[8] = {0.f, 0.f, 0.f, 0.f, 0.f, 0.f, 0.f, 0.f};
      u32x4 wv[16];
#pragma unroll
      for (int i = 0; i < 16; ++i) wv[i] = *(const u32x4*)(wp_ + (size_t)(8 * i) * F_);
      SCHED();
#pragma unroll
      for (int i = 0; i < 16; ++i)
#pragma unroll
        for (int c = 0; c < 4; ++c) { a8[2 * c] += bflo(wv[i][c]); a8[2 * c + 1] += bfhi(wv[i][c]); }
#pragma unroll
      for (int i = 0; i < 4; ++i) a8[i] = comb32(a8[i], a8[i + 4]);
#pragma unroll
      for (int i = 0; i < 2; ++i) a8[i] = dpp_add<0x128, 0xf>(comb16(a8[i], a8[i + 2]));
      if (!(lane & 8)) *(f32x2*)(lds + L_COLS + cw * 256 + (8 * (lane & 7) + 2 * (g & 1) + 4 * (g >> 1)) * 4) = (f32x2){a8[0], a8[1]};
      SCHED(); asm volatile("s_waitcnt lgkmcnt(0)" ::: "memory");
      if (last_of(lds, C_CS, 4, lane)) {
        const float* cp = (const float*)(lds + L_COLS);
        const float tot = (cp[lane] + cp[64 + lane]) + (cp[128 + lane] + cp[192 + lane]);
        __hip_atomic_store((float*)(ws + WS_CS) + 64 * auxidx + lane, tot, RLX_AGENT);
        asm volatile("s_waitcnt vmcnt(0)" ::: "memory");
        if (lane < 16) __hip_atomic_store((u64*)(ws + WS_FLAG + (size_t)cblk * 1024 + FL_ST) + 2 + (auxidx & 1), aC + 1, RLX_AGENT);
      }
    }
  }
  asm volatile("s_waitcnt vmcnt(2)" : "+v"(raw0), "+v"(raw1) :: "memory");
  float isc;
  unsigned T[TB_N];
  kgen_tables(T, isc, raw0, raw1, (d & 1) ? bfhi(sdt) : bflo(sdt), lds, cw, lane);
#pragma unroll
  for (int i = 0; i < TB_N; ++i) asm volatile("" : "+v"(T[i]));
  asm volatile("s_waitcnt vmcnt(0)" : "+v"(aS), "+v"(aH) :: "memory");
  const u64 aOwn = rdlane64(aS, 32);
  if (cw == 1 && lane < 16) *(u64*)(lds + L_ACON + lane * 8) = aH;
  if (cw == 3) { if (lane == 0) *(u64*)(lds + L_KSC + 16) = aOwn; ARRIVE_L(C_EP); }
  __builtin_amdgcn_s_setprio(1);
  STAMP();
  const u64* lnfl = (const u64*)(flg + FL_LN) + (lane & 31);
  const char* up = ws + WS_UR + (size_t)d * 4096 + hf * 2048;
  u64 fv;
  if (aux) {
    await_<2>(lds, C_X2, 8);
  } else {
    const u64* stfl = (const u64*)(flg + FL_ST) + (lane & 3);
    const char* stp = cw == 1 ? ws + WS_CS + fs * 512 : ws + WS_STAT + (size_t)panel * 1024;
    const unsigned svo = cw == 1 ? (lane & 31) * 16 : lane * 16;
    u32x4 stv;
#define XBAR() do { SCHED(); asm volatile("s_waitcnt lgkmcnt(0)\n\ts_barrier" ::: "memory"); SCHED(); } while (0)
    if (cw < 2) {
      bool ok = false, got = false;
      XBAR(); XBAR(); XBAR(); XBAR();
      ld_u64(fv, stfl); gload16(stv, svo, stp);
#define XPOLL() do { XBAR(); asm volatile("s_waitcnt vmcnt(0)" : "+v"(fv), "+v"(stv) :: "memory"); \
        if (ok && !got) { *(u32x4*)(lds + (cw == 1 ? L_COLS : L_STAT) + svo) = stv; got = true; } \
        ok = __all(fv == aOwn + 1); SCHED(); ld_u64(fv, stfl); gload16(stv, svo, stp); } while (0)
      XPOLL(); XPOLL(); XPOLL(); XPOLL();
      asm volatile("s_waitcnt vmcnt(0)" : "+v"(fv), "+v"(stv) :: "memory");
      if (!got) {
        if (!ok) {
          poll_flags(stfl, aOwn + 1);
          asm volatile("global_load_dwordx4 %0, %1, %2 sc1\n\ts_waitcnt vmcnt(0)" : "=&v"(stv) : "v"(svo), "s"(stp) : "memory");
        }
        *(u32x4*)(lds + (cw == 1 ? L_COLS : L_STAT) + svo) = stv;
      }
    } else {
      XBAR(); XBAR(); XBAR(); XBAR(); XBAR(); XBAR(); XBAR(); XBAR();
    }
    XBAR();
    XBAR();
  }
  STAMP();
  __builtin_amdgcn_s_setprio(3);
  K2_SETUP(cw);
  K2_DMA(0); K2_DMA(1);
  char* wl = lds + L_W + ch * WSZ;
  {
    char* tb = wl + hf * 9216;
#pragma unroll
    for (int i = 0; i < TB_N; ++i) *(unsigned*)(tb + i * TB_RS + lane * 4) = T[i];
    const char* tr = tb + g * 16;
    const int rXa = (i16 >> 3) * TB_RS, rXe = (17 + (i16 & 7)) * TB_RS;
    const int rYa = (i16 < 8 ? 2 + (i16 >> 2) : 4 + ((i16 - 8) >> 1)) * TB_RS, rYe = (17 + (i16 < 8 ? 2 * (i16 & 3) : 4 * (i16 & 1))) * TB_RS;
    const int rZa = (i16 < 8 ? 8 + i16 : 16) * TB_RS;
    const int rB1 = (25 + (i16 >> 2)) * TB_RS, rB0 = (29 + (i16 & 3)) * TB_RS;
    f32x4 kX = {0.f, 0.f, 0.f, 0.f}, kY = kX, kZ = kX;
#pragma unroll
    for (int sb = 0; sb < 4; sb += 2) {
      u32x4 t_[2][7];
#pragma unroll
      for (int s = 0; s < 2; ++s) {
        const char* tq = tr + (sb + s) * 64;
        t_[s][0] = *(const u32x4*)(tq + rB1); t_[s][1] = *(const u32x4*)(tq + rB0); t_[s][2] = *(const u32x4*)(tq + rXa); t_[s][3] = *(const u32x4*)(tq + rXe);
        t_[s][4] = *(const u32x4*)(tq + rYa); t_[s][5] = *(const u32x4*)(tq + rYe); t_[s][6] = *(const u32x4*)(tq + rZa);
      }
      SCHED();
#pragma unroll
      for (int s = 0; s < 2; ++s) {
        const f16x8 hb = __builtin_bit_cast(f16x8, cmulh4(t_[s][0], t_[s][1]));
        kX = __builtin_amdgcn_mfma_f32_16x16x32_f16(__builtin_bit_cast(f16x8, cmulh4(t_[s][2], t_[s][3])), hb, kX, 0, 0, 0);
        kY = __builtin_amdgcn_mfma_f32_16x16x32_f16(__builtin_bit_cast(f16x8, cmulh4(t_[s][4], t_[s][5])), hb, kY, 0, 0, 0);
        kZ = __builtin_amdgcn_mfma_f32_16x16x32_f16(__builtin_bit_cast(f16x8, t_[s][6]), hb, kZ, 0, 0, 0);
      }
      SCHED();
    }
    kgen_store(kX, kY, kZ, isc, lds, cw, lane);
  }
  ARRIVE_L(C_K0 + ch);
  ld_u64(fv, lnfl);
  await_<0>(lds, C_K0 + ch, 2);
  const f32x2 ks = *(const f32x2*)(lds + L_KSC + ch * 8);
  int ex = (int)((__float_as_uint(ks[0] + ks[1]) >> 23) & 255);
  ex = ex < 20 ? 20 : (ex > 230 ? 230 : ex);
  const float scale = __uint_as_float((unsigned)(267 - ex) << 23);
  const float iscale = __uint_as_float((unsigned)(ex - 13) << 23);
  char* kp = lds + L_KP + ch * KPSZ;
  {
    const float* k0 = (const float*)(lds + L_KAP + (2 * ch) * KAPSZ);
#define KSUM2(T, i) ((*(const T*)(k0 + (i)) + *(const T*)(k0 + 644 + (i))) * scale)
    const int A = 64 * hf + lane, A2 = 128 + A;
    const float e0 = KSUM2(float, 512 + A), e1 = KSUM2(float, 513 + A);
    u32x4 o;
    if (hf == 0) {
      const int i0 = lane < 32 ? 8 * A : 256 + 4 * (A - 32);
      const f32x4 a = KSUM2(f32x4, i0), b = KSUM2(f32x4, i0 + 4);
      const bool lo = lane < 32;
      o = (u32x4){pkh(a[0], lo ? a[1] : a[0]), pkh(lo ? a[2] : a[1], lo ? a[3] : a[2]), pkh(lo ? b[0] : a[2], lo ? b[1] : a[2]), pkh(lo ? b[2] : a[3], lo ? b[3] : b[0])};
      if (lane < 2) *(u32x4*)(kp + lane * 16) = (u32x4){0u, 0u, 0u, 0u};
    } else {
      const int i0 = 384 + 2 * lane;
      const f32x2 a = KSUM2(f32x2, i0), b = KSUM2(f32x2, i0 + 2);
      o = (u32x4){pkh(a[0], a[0]), pkh(a[0], a[1]), pkh(a[1], a[1]), pkh(b[0], b[0])};
    }
    *(u32x4*)(kp + 32 + A * 16) = o;
    const unsigned p00 = pkh(e0, e0);
    *(u32x4*)(kp + 32 + A2 * 16) = (u32x4){p00, p00, pkh((A2 & 1) ? e1 : e0, e1), pkh(e1, e1)};
  }
  ARRIVE_L(C_K1);
  asm volatile("s_waitcnt vmcnt(0)" : "+v"(fv) :: "memory");
  arrive(lds, C_W2, lane);
  const bool urok = __all(fv == aOwn + 1);
  u32x4 ua_, ub_, q0, q1, q2, q3;
  const char* qp = ws + WS_ST2 + hf * 4096;
#define U_LOADS() { gload16(ua_, lane * 16, up); gload16(ub_, lane * 16, up + 1024); \
    gload16(q0, lane * 32, qp); gload16(q1, lane * 32, qp + 16); gload16(q2, lane * 32, qp + 2048); gload16(q3, lane * 32, qp + 2064); }
#define U_WAIT() asm volatile("s_waitcnt vmcnt(0)" : "+v"(ua_), "+v"(ub_), "+v"(q0), "+v"(q1), "+v"(q2), "+v"(q3) :: "memory")
  U_LOADS();
  await_<0>(lds, C_K1, 4);
  STAMP();
#pragma unroll
  for (int rnd = 0; rnd < 3; ++rnd) {
    const int a = rnd < 2 ? 128 * rnd + 64 * hf + lane : 256;
    if (rnd == 2 && !(hf == 0 && lane == 0)) break;
    const u32x4 c0 = *(const u32x4*)(kp + a * 16), c1 = *(const u32x4*)(kp + a * 16 + 16);
    const unsigned e[8] = {c0[0], c0[1], c0[2], c0[3], c1[0], c1[1], c1[2], c1[3]};
    unsigned o[7];
#pragma unroll
    for (int i = 0; i < 7; ++i) o[i] = __builtin_amdgcn_alignbit(e[i + 1], e[i], 16);
    const int m = (a & 1) | (((a >> 2) & 1) << 1) | (((a >> 1) & 1) << 2);
    char* base = wl + a * 128;
#pragma unroll
    for (int c = 0; c < 8; ++c) {
      u32x4 wv;
      if (c & 1) { const int s = (c + 1) >> 1; wv = (u32x4){e[s], e[s + 1], e[s + 2], e[s + 3]}; }
      else { const int s = c >> 1; wv = (u32x4){o[s], o[s + 1], o[s + 2], o[s + 3]}; }
      *(u32x4*)(base + ((c ^ m) << 4)) = wv;
    }
  }
  SCHED();
  U_WAIT();
  if (!urok) { poll_flags(lnfl, aOwn + 1); U_LOADS(); U_WAIT(); }
  char* ul = lds + L_U + ch * USZ;
  {
#define UN2(xw, s0, s1) ({ const unsigned a_ = pkbf(bflo(xw) - bflo(s0), bfhi(xw) - bflo(s1)); \
      const unsigned b_ = pkbf(bflo(a_) * __builtin_amdgcn_rcpf(bfhi(s0)), bfhi(a_) * __builtin_amdgcn_rcpf(bfhi(s1))); pkh(bflo(b_), bfhi(b_)); })
    const u32x4 o0 = {UN2(ua_[0], q0[0], q0[1]), UN2(ua_[1], q0[2], q0[3]), UN2(ua_[2], q1[0], q1[1]), UN2(ua_[3], q1[2], q1[3])};
    const u32x4 o1 = {UN2(ub_[0], q2[0], q2[1]), UN2(ub_[1], q2[2], q2[3]), UN2(ub_[2], q3[0], q3[1]), UN2(ub_[3], q3[2], q3[3])};
    *(u32x4*)(ul + hf * 2048 + lane * 16) = o0;
    *(u32x4*)(ul + hf * 2048 + 1024 + lane * 16) = o1;
  }
  if (hf == 0 && lane < 32) *(u32x4*)(ul + 4096 + lane * 16) = (u32x4){0u, 0u, 0u, 0u};
  ARRIVE_L(C_K2 + ch);
  STAMP();
  await_<2>(lds, C_PUB, 8);
  await_<0>(lds, C_K2 + ch, 2);
  __builtin_amdgcn_s_setprio(3);
  STAMP();
  f32x4 acc[8];
#pragma unroll
  for (int i = 0; i < 8; ++i) acc[i] = (f32x4){0.f, 0.f, 0.f, 0.f};
  {
    const int c = i16 + 8 * g;
    int fo[2];
#pragma unroll
    for (int par = 0; par < 2; ++par) {
      const int w = 32 * par + c;
      const int m = ((w >> 3) & 1) | (((w >> 5) & 1) << 1) | (((w >> 4) & 1) << 2);
      fo[par] = (w ^ m) << 4;
    }
    const char* fbase = wl + hf * (128 * 16);
    const char* gbase = ul + (2032 - 16 * i16 + 8 * g + 128 * hf) * 2 - 7 * 512;
#define CF(s, e) (*(const f16x8*)(fbase + fo[(s) & 1] + ((s) >> 1) * 1024 + (e) * 4096))
#define CG(s, mu) (*(const f16x8*)(gbase + (s) * 64 + (7 - (mu)) * 512))
    f16x8 F[2][8], G[2];
#pragma unroll
    for (int e = 0; e < 8; ++e) F[0][e] = CF(0, e);
    G[0] = CG(0, 0);
#pragma unroll
    for (int s = 0; s < 4; ++s) {
#pragma unroll
      for (int mu = 0; mu < 8; ++mu) {
        const int it = s * 8 + mu;
        SCHED();
        if (it + 1 < 32) G[(it + 1) & 1] = CG((it + 1) >> 3, (it + 1) & 7);
        if (s < 3) F[(s + 1) & 1][mu] = CF(s + 1, mu);
        SCHED();
#pragma unroll
        for (int e = 0; e + mu < 8; ++e) acc[e + mu] = __builtin_amdgcn_mfma_f32_16x16x32_f16(F[s & 1][e], G[it & 1], acc[e + mu], 0, 0, 0);
      }
    }
  }
  STAMP();
  ARRIVE_L(C_K1 + 1 + ch); await_<0>(lds, C_K1 + 1 + ch, 2);
  {
    char* dst = wl + hf * 4096 + lane * 16;
#pragma unroll
    for (int i = 0; i < 4; ++i) *(f32x4*)(dst + i * 1024) = sel(hf, acc[i], acc[4 + i]);
  }
  ARRIVE_L(C_K3 + ch); await_<0>(lds, C_K3 + ch, 2);
  STAMP();
  {
    const float dskip = (d & 1) ? bfhi(sds) : bflo(sds);
    const char* src = wl + (hf ^ 1) * 4096 + lane * 16;
    char* sdst = ws + WS_S + (size_t)d * 4096;
#pragma unroll
    for (int i = 0; i < 4; ++i) {
      const f32x4 yv = (sel(hf, acc[4 + i], acc[i]) + *(const f32x4*)(src + i * 1024)) * iscale;
      const int t = 256 * (4 * hf + i) + 16 * i16 + 4 * g;
      const u32x2 uu = *(const u32x2*)(ul + (2044 - t) * 2);
      const unsigned uu0 = uu[0], uu1 = uu[1];
      const f16x2 ua = __builtin_bit_cast(f16x2, uu0), ub = __builtin_bit_cast(f16x2, uu1);
      const unsigned y01 = pkbf(yv[0], yv[1]), y23 = pkbf(yv[2], yv[3]);
      const unsigned k01 = pkbf((float)ub[1] * dskip, (float)ub[0] * dskip), k23 = pkbf((float)ua[1] * dskip, (float)ua[0] * dskip);
      const u64 pk = (u64)pkbf(bflo(y01) + bflo(k01), bfhi(y01) + bfhi(k01)) | ((u64)pkbf(bflo(y23) + bflo(k23), bfhi(y23) + bfhi(k23)) << 32);
      __hip_atomic_store((u64*)(sdst + t * 2), pk, RLX_AGENT);
    }
  }
  asm volatile("s_waitcnt vmcnt(0) lgkmcnt(0)" ::: "memory");
  STAMP();
  if (last_of(lds, C_SP, 4, lane) && lane < 32) __hip_atomic_store((u64*)(ws + WS_FLAG + (size_t)blk_of(lane, cb) * 1024 + FL_S) + mb, aS + 1, RLX_AGENT);
  STAMP_FLUSH();
}

#define FIN_LOADS() { \
  asm volatile("global_load_dwordx4 %0, %1, off" : "=v"(xv) : "v"(x + (size_t)(64 * mb + (tid >> 3)) * D_ + 64 * cb + 8 * (tid & 7)) : "memory"); \
  asm volatile("global_load_dwordx4 %0, %1, off" : "=v"(bv) : "v"(b2 + 64 * cb + 8 * (tid & 7)) : "memory"); }
DI void final_stage(u32x4& xv, u32x4& bv, u16* __restrict__ out, char* lds, int mb, int cb, int tid) {
  await_<0>(lds, C_R1, 4);
  await_<0>(lds, C_ST, 4);
  const int t = tid >> 3, dq = tid & 7;
  const char* src = lds + L_RED + t * RED_RS + dq * 32;
  f32x4 ra[4], rb[4];
#pragma unroll
  for (int k = 0; k < 4; ++k) { ra[k] = *(const f32x4*)(src + k * 64 * RED_RS); rb[k] = *(const f32x4*)(src + k * 64 * RED_RS + 16); }
  const u32x4 sv = *(const u32x4*)(lds + L_ST + t * ST_RS + dq * 16);
  SCHED();
  const f32x4 a = (ra[0] + ra[1]) + (ra[2] + ra[3]), b = (rb[0] + rb[1]) + (rb[2] + rb[3]);
  asm volatile("s_waitcnt vmcnt(0)" : "+v"(xv), "+v"(bv) :: "memory");
  u32x4 o;
#pragma unroll
  for (int c = 0; c < 4; ++c) {
    const float v0 = (c < 2 ? a[2 * c] : b[2 * c - 4]) + bflo(bv[c]), v1 = (c < 2 ? a[2 * c + 1] : b[2 * c - 3]) + bfhi(bv[c]);
    const float g0 = v0 * __builtin_amdgcn_rcpf(1.f + __builtin_amdgcn_exp2f(-1.4426950408889634f * v0));
    const float g1 = v1 * __builtin_amdgcn_rcpf(1.f + __builtin_amdgcn_exp2f(-1.4426950408889634f * v1));
    const unsigned pv = pkbf(bflo(sv[c]) * g0, bfhi(sv[c]) * g1);
    o[c] = pkbf(bflo(xv[c]) + bflo(pv), bfhi(xv[c]) + bfhi(pv));
  }
  *(u32x4*)(out + (size_t)(64 * mb + t) * D_ + 64 * cb + 8 * dq) = o;
}
DI void p2_gemm(const u16* __restrict__ x, const u16* __restrict__ w2, u64 aOwn, const u16* __restrict__ b2, u16* __restrict__ out, char* __restrict__ ws,
                char* lds, int mb, int cb, int kq, int lane TK0_ARG) {
  STAMP_DECL STAMP_INIT(1) STAMP0();
  const int r = lane & 31, h = lane >> 5;
  unsigned* cnt = (unsigned*)(lds + L_CNT);
  const int tid = kq * 64 + lane;
  K2_SETUP(kq);
  char* ring = lds + L_RING2 + kq * (K2_R * 4096);
  const char* flg = ws + WS_FLAG + (size_t)blockIdx.x * 1024;
  const char* hbase = ws + WS_H + (size_t)(2 * mb) * HMT + 28 * kq * 1024;
  const unsigned hoff0 = lane * 16, hoff1 = lane * 16 + HMT;
  const int q = (lane & 15) >> 2;
  const int toff0 = (4 * h + q) * 128 + ((q >> 1) << 6) + ((lane >> 4) & 1) * 32 + 8 * (lane & 3), toff1 = toff0 ^ 64;
  u32x4 hb[3][4];
  s16x8 wf[2][4];
  f32x16 acc[2][2];
#pragma unroll
  for (int i = 0; i < 16; ++i) { acc[0][0][i] = 0.f; acc[0][1][i] = 0.f; acc[1][0][i] = 0.f; acc[1][1][i] = 0.f; }
  await_<1>(lds, C_PUB, 8);
  K2_DMA(2); K2_DMA(3);
  STAMP();
  if (kq == 0) {
    const u64* fp = (const u64*)(flg + FL_H) + (lane < 14 ? lane : 0);
    for (unsigned spin = 0; spin < (1u << 20); ++spin) {
      u64 v;
      asm volatile("global_load_dwordx2 %0, %1, off sc1\n\ts_waitcnt vmcnt(0)" : "=&v"(v) : "v"(fp) : "memory");
      if (__all(v == aOwn + 1)) break;
      __builtin_amdgcn_s_sleep(2);
    }
    lds_set(cnt + C_H, lane);
  } else {
    lds_wait(cnt + C_H);
  }
  STAMP();
#define K2_HL(S, s2) { const char* hs_ = hbase + K2S(S) * 2048 + (s2) * 1024; gload16(hb[(S) % 3][2 * (s2)], hoff0, hs_); gload16(hb[(S) % 3][2 * (s2) + 1], hoff1, hs_); SCHED(); }
#define K2_HLD(S) { K2_HL(S, 0); K2_HL(S, 1); }
#define K2_TR(S, i) { const char* sl_ = ring + ((S) % K2_R) * 4096 + ((i) >> 1) * 2048 + (((i) & 1) ? toff1 : toff0); wf[(S) & 1][i] = tr_frag(sl_, sl_ + 1024); SCHED(); }
#define K2_D1(S, pp) { dma16s(wvoff, wsrc + (size_t)(K2S(S) * 32 + (pp) * 8) * D_, ringa + ((S) % K2_R) * 4096 + (pp) * 1024); SCHED(); }
#define K2_M(S, s2, mi, di) { acc[mi][di] = __builtin_amdgcn_mfma_f32_32x32x16_bf16(wf[(S) & 1][2 * (s2) + (di)], __builtin_bit_cast(s16x8, hb[(S) % 3][2 * (s2) + (mi)]), acc[mi][di], 0, 0, 0); SCHED(); }
#define K2_STEP(S, W, TRN, HN, DN) { \
    asm volatile("s_waitcnt vmcnt(" #W ")" : "+v"(hb[(S) % 3][0]), "+v"(hb[(S) % 3][1]), "+v"(hb[(S) % 3][2]), "+v"(hb[(S) % 3][3]) :: "memory"); SCHED(); \
    K2_M(S,0,0,0); if (TRN) K2_TR((S)+1,0); K2_M(S,0,1,0); if (TRN) K2_TR((S)+1,1); K2_M(S,0,0,1); if (TRN) K2_TR((S)+1,2); K2_M(S,0,1,1); if (TRN) K2_TR((S)+1,3); \
    K2_M(S,1,0,0); if (DN) { K2_D1((S)+4,0); K2_D1((S)+4,1); } K2_M(S,1,1,0); if (DN) { K2_D1((S)+4,2); K2_D1((S)+4,3); } K2_M(S,1,0,1); if (HN) K2_HL((S)+3,0); K2_M(S,1,1,1); if (HN) K2_HL((S)+3,1); }
  K2_HLD(0); K2_HLD(1); K2_HLD(2);
  await_<0>(lds, C_W2, 4);
  K2_TR(0,0); K2_TR(0,1); K2_TR(0,2); K2_TR(0,3);
  K2_STEP(0, 8, 1, 1, 1); STAMP();
  K2_STEP(1, 12, 1, 1, 1);
  K2_STEP(2, 16, 1, 1, 1);
  K2_STEP(3, 16, 1, 1, 1);
  K2_STEP(4, 16, 1, 1, 1);
  K2_STEP(5, 16, 1, 1, 1);
  K2_STEP(6, 16, 1, 1, 1);
  K2_STEP(7, 16, 1, 1, 1); STAMP();
  K2_STEP(8, 16, 1, 1, 1);
  K2_STEP(9, 16, 1, 1, 1);
  K2_STEP(10, 16, 1, 1, 0);
  u32x4 xv, bv;
  FIN_LOADS();
  K2_STEP(11, 14, 1, 0, 0);
  K2_STEP(12, 6, 1, 0, 0);
  K2_STEP(13, 2, 0, 0, 0);
  STAMP();
  await_<0>(lds, C_SP, 4);
  {
    char* dst = lds + L_RED + (kq * 64 + r) * RED_RS + h * 16;
#pragma unroll
    for (int mi = 0; mi < 2; ++mi)
#pragma unroll
      for (int di = 0; di < 2; ++di)
#pragma unroll
        for (int rq = 0; rq < 4; ++rq)
          *(f32x4*)(dst + mi * 32 * RED_RS + di * 128 + rq * 32) = (f32x4){acc[mi][di][4 * rq], acc[mi][di][4 * rq + 1], acc[mi][di][4 * rq + 2], acc[mi][di][4 * rq + 3]};
  }
  ARRIVE_L(C_R1);
  STAMP();
  final_stage(xv, bv, out, lds, mb, cb, tid);
  if (tid == 0) __hip_atomic_store((u64*)(ws + WS_FLAG + (size_t)blockIdx.x * 1024), aOwn + 1, RLX_AGENT);
  STAMP(); STAMP_FLUSH();
}

DI void p2_helper(const u16* __restrict__ x, const u16* __restrict__ w2, u64 aOwn, const u16* __restrict__ b2, u16* __restrict__ out, char* __restrict__ ws,
                  char* lds, int mb, int cb, int gw, int lane) {
  unsigned* cnt = (unsigned*)(lds + L_CNT);
  const int tid = gw * 64 + lane, hw = gw - 4;
  u32x4 xv, bv;
  FIN_LOADS();
  if (hw == 0) {
    const u64* fp = (const u64*)(ws + WS_FLAG + (size_t)blockIdx.x * 1024 + FL_S) + (lane & 31);
    for (unsigned spin = 0; spin < (1u << 18); ++spin) {
      const u64 v = __hip_atomic_load(fp, RLX_AGENT);
      if (__all(v == aOwn + 1)) break;
      __builtin_amdgcn_s_sleep(8);
    }
    asm volatile("" ::: "memory");
    lds_set(cnt + C_S, lane);
  } else {
    while (__hip_atomic_load(cnt + C_S, RLX_WG) == 0u) __builtin_amdgcn_s_sleep(4);
    asm volatile("" ::: "memory");
  }
  u32x4 sa, sb;
  const char* sp = ws + WS_S + (size_t)(64 * cb + 16 * hw) * 4096 + 64 * mb * 2;
  gload16(sa, (lane >> 3) * 4096 + (lane & 7) * 16, sp);
  gload16(sb, (lane >> 3) * 4096 + (lane & 7) * 16, sp + 8 * 4096);
  asm volatile("s_waitcnt vmcnt(0)" : "+v"(sa), "+v"(sb) :: "memory");
  char* dst = lds + L_ST + (8 * (lane & 7)) * ST_RS + (16 * hw + (lane >> 3)) * 2;
#pragma unroll
  for (int c = 0; c < 4; ++c) {
    *(u16*)(dst + (2 * c) * ST_RS) = (u16)sa[c]; *(u16*)(dst + (2 * c + 1) * ST_RS) = (u16)(sa[c] >> 16);
    *(u16*)(dst + (2 * c) * ST_RS + 16) = (u16)sb[c]; *(u16*)(dst + (2 * c + 1) * ST_RS + 16) = (u16)(sb[c] >> 16);
  }
  ARRIVE_L(C_ST);
  while (__hip_atomic_load(cnt + C_R1, RLX_WG) < 4u) __builtin_amdgcn_s_sleep(2);
  final_stage(xv, bv, out, lds, mb, cb, tid);
}

template <int PH>
__global__ __launch_bounds__(768, 3) void fused_kernel(const u16* __restrict__ x, const u16* __restrict__ lar, const u16* __restrict__ aim,
                                                       const u16* __restrict__ cre, const u16* __restrict__ cim, const u16* __restrict__ ldt,
                                                       const u16* __restrict__ dsk, const u16* __restrict__ w1, const u16* __restrict__ b1,
                                                       const u16* __restrict__ w2, const u16* __restrict__ b2, u16* __restrict__ out,
                                                       char* __restrict__ ws) {
  extern __shared__ __attribute__((aligned(16))) char lds[];
  u64 pc0; asm volatile("s_getpc_b64 %0" : "=s"(pc0));
  const int lane = threadIdx.x & 63;
  const int wid = __builtin_amdgcn_readfirstlane(threadIdx.x >> 6);
  if (wid == 3) {
    const char* cp = (const char*)(pc0 & ~255ull);
#pragma unroll
    for (int i = 0; i < 4; ++i)
      asm volatile("s_mov_b32 m0, %2\n\ts_nop 0\n\tglobal_load_lds_dword %0, %1" :: "v"(lane * 128), "s"(cp + i * 8192), "s"(lds_base(lds) + L_KP) : "memory");
  }
  const int b = blockIdx.x, xcd = b & 7, j = b >> 3;
  const int mb = 4 * xcd + (j >> 3), cb = j & 7;
  const int jj = j < 28 ? j : 0;
  const int panel = 4 * (xcd >> 1) + jj / 7, fs = 7 * (xcd & 1) + jj % 7;
  if (wid < 4) {
    const int de = 64 * cb + 2 * mb;
    const unsigned sdt = *(const __attribute__((address_space(4))) unsigned*)(ldt + de);
    const unsigned sds = *(const __attribute__((address_space(4))) unsigned*)(dsk + de);
    x_wave(lar, aim, cre, cim, sdt, sds, w2, w1, b1, j >= 28 ? xcd * 4 + (j - 28) : -1, panel, fs, ws, lds, mb, cb, wid, lane TK0_PASS);
    return;
  }
  const int gw = wid - 4;
  __builtin_amdgcn_s_setprio(2);
  if (j < 28) p1_gemm(x, w1, ws, lds, panel, fs, gw, lane TK0_PASS);
  else p1_aux(x, ws, lds, xcd * 4 + (j - 28), gw, lane TK0_PASS);
  __builtin_amdgcn_s_setprio(0);
  await_<0>(lds, C_EP, 1);
  const u64 aOwn = *(const u64*)(lds + L_KSC + 16);
  if (gw < 4) p2_gemm(x, w2, aOwn, b2, out, ws, lds, mb, cb, gw, lane TK0_PASS);
  else p2_helper(x, w2, aOwn, b2, out, ws, lds, mb, cb, gw, lane);
}

extern "C" void kernel_launch(void* const* d_in, const int* in_sizes, int n_in, void* d_out, int out_size, void* d_ws, size_t ws_size,
                              hipStream_t stream) {
  const u16* x = (const u16*)d_in[0];
  const u16* lar = (const u16*)d_in[1];
  const u16* aim = (const u16*)d_in[2];
  const u16* cre = (const u16*)d_in[3];
  const u16* cim = (const u16*)d_in[4];
  const u16* ldt = (const u16*)d_in[5];
  const u16* dsk = (const u16*)d_in[6];
  const u16* w1 = (const u16*)d_in[8];
  const u16* b1 = (const u16*)d_in[9];
  const u16* w2 = (const u16*)d_in[10];
  const u16* b2 = (const u16*)d_in[11];
  (void)hipFuncSetAttribute((const void*)fused_kernel<3>, hipFuncAttributeMaxDynamicSharedMemorySize, LDS_TOTAL);
  fused_kernel<3><<<256, 768, LDS_TOTAL, stream>>>(x, lar, aim, cre, cim, ldt, dsk, w1, b1, w2, b2, (u16*)d_out, (char*)d_ws);
}
```

```cpp
#include <hip/hip_runtime.h>
#include <stdint.h>

typedef unsigned short u16;
typedef unsigned long long u64;
typedef __attribute__((ext_vector_type(4))) short s16x4;
typedef __attribute__((ext_vector_type(8))) short s16x8;
typedef __attribute__((ext_vector_type(8))) _Float16 f16x8;
typedef __attribute__((ext_vector_type(2))) _Float16 f16x2;
typedef __attribute__((ext_vector_type(2))) __bf16 bf16x2;
typedef __attribute__((ext_vector_type(2))) float f32x2;
typedef __attribute__((ext_vector_type(4))) float f32x4;
typedef __attribute__((ext_vector_type(16))) float f32x16;
typedef __attribute__((ext_vector_type(4))) unsigned u32x4;
typedef __attribute__((ext_vector_type(2))) unsigned u32x2;
#define LDSP __attribute__((address_space(3)))
#define GLBP __attribute__((address_space(1)))
#define DI __device__ __forceinline__

#define NLAUNCH 1

#define XPRIO 3
#define L_ 2048
#define D_ 512
#define N_ 128
#define F_ 1792

#define WS_H 0
#define WS_UR (8u << 20)
#define WS_S (10u << 20)
#define WS_STAT (12u << 20)
#define WS_CS ((12u << 20) + 65536)
#define WS_ST2 ((12u << 20) + 131072)
#define WS_FLAG (13u << 20)
#define WS_KAP (16u << 20)
#define FL_LN 128
#define FL_H 384
#define FL_S 512
#define FL_ST 768

#define STAMP_INIT(kid)
#define STAMP()
#define STAMP0()
#define STAMP_DECL
#define STAMP_FLUSH()
#define TK0_ARG
#define TK0_PASS

DI float bf2f(unsigned v) { return __uint_as_float(v << 16); }
DI float bflo(unsigned v) { return __uint_as_float(v << 16); }
DI float bfhi(unsigned v) { return __uint_as_float(v & 0xffff0000u); }
DI unsigned pkbf(float a, float b) { bf16x2 q = {(__bf16)a, (__bf16)b}; return __builtin_bit_cast(unsigned, q); }
DI float rbf(float a) { __bf16 q = (__bf16)a; return (float)q; }
DI unsigned pkh(float a, float b) { f32x2 v = {a, b}; f16x2 p = __builtin_convertvector(v, f16x2); return __builtin_bit_cast(unsigned, p); }
DI float dot2(unsigned a, unsigned b, float c) { return __builtin_amdgcn_fdot2_f32_bf16(__builtin_bit_cast(bf16x2, a), __builtin_bit_cast(bf16x2, b), c, false); }
template <class T> DI T sel(int c, T a, T b) { return c ? a : b; }
DI void dma16(const void* g, char* l) { __builtin_amdgcn_global_load_lds((const GLBP unsigned*)g, (LDSP unsigned*)l, 16, 0, 0); }
DI void dma16s(unsigned voff, const void* sbase, unsigned ldsaddr) {
  asm volatile("s_mov_b32 m0, %2\n\ts_nop 0\n\tglobal_load_lds_dwordx4 %0, %1" :: "v"(voff), "s"(sbase), "s"(ldsaddr) : "memory");
}
DI unsigned lds_base(char* lds) { return __builtin_amdgcn_readfirstlane((unsigned)(size_t)(LDSP char*)lds); }
DI s16x8 tr_frag(const char* p0, const char* p1) {
  s16x4 a = __builtin_amdgcn_ds_read_tr16_b64_v4i16((LDSP s16x4*)p0);
  s16x4 b = __builtin_amdgcn_ds_read_tr16_b64_v4i16((LDSP s16x4*)p1);
  return __builtin_shufflevector(a, b, 0, 1, 2, 3, 4, 5, 6, 7);
}
#define SCHED() __builtin_amdgcn_sched_barrier(0)
#define WAITV_BAR(n) do { SCHED(); asm volatile("s_waitcnt vmcnt(" #n ") lgkmcnt(0)\n\ts_barrier" ::: "memory"); SCHED(); } while (0)
#define LGKM_BAR() do { SCHED(); asm volatile("s_waitcnt lgkmcnt(0)\n\ts_barrier" ::: "memory"); SCHED(); } while (0)
#define RLX_AGENT __ATOMIC_RELAXED, __HIP_MEMORY_SCOPE_AGENT
#define RLX_WG __ATOMIC_RELAXED, __HIP_MEMORY_SCOPE_WORKGROUP

template <int CTRL, int RM> DI float dpp_add(float v) {
  return v + __builtin_bit_cast(float, __builtin_amdgcn_update_dpp(0, __builtin_bit_cast(int, v), CTRL, RM, 0xf, true));
}
DI float wave_sum63(float v) {
  v = dpp_add<0x111, 0xf>(v); v = dpp_add<0x112, 0xf>(v); v = dpp_add<0x114, 0xf>(v); v = dpp_add<0x118, 0xf>(v);
  v = dpp_add<0x142, 0xa>(v); v = dpp_add<0x143, 0xc>(v);
  return v;
}
DI float comb32(float a, float b) { auto r = __builtin_amdgcn_permlane32_swap(__float_as_uint(a), __float_as_uint(b), false, false); return __uint_as_float(r[0]) + __uint_as_float(r[1]); }
DI float comb16(float a, float b) { auto r = __builtin_amdgcn_permlane16_swap(__float_as_uint(a), __float_as_uint(b), false, false); return __uint_as_float(r[0]) + __uint_as_float(r[1]); }
DI float row_allsum(float v) { v = dpp_add<0x128, 0xf>(v); v = dpp_add<0x124, 0xf>(v); v = dpp_add<0x122, 0xf>(v); v = dpp_add<0x121, 0xf>(v); return v; }
DI float rdlane(float v, int l) { return __builtin_bit_cast(float, __builtin_amdgcn_readlane(__builtin_bit_cast(int, v), l)); }

DI void group_barrier(unsigned* cnt, unsigned target, int lane) {
  asm volatile("s_waitcnt lgkmcnt(0)" ::: "memory");
  if (lane == 0) __hip_atomic_fetch_add(cnt, 1u, RLX_WG);
  while (__hip_atomic_load(cnt, RLX_WG) < target) __builtin_amdgcn_s_sleep(1);
  asm volatile("" ::: "memory");
}
DI void poll_flags(const u64* p, u64 expect) {
  for (unsigned spin = 0; spin < (1u << 20); ++spin) {
    const u64 v = __hip_atomic_load(p, RLX_AGENT);
    if (__all(v == expect)) break;
    __builtin_amdgcn_s_sleep(2);
  }
  asm volatile("" ::: "memory");
}
DI void lds_set(unsigned* w, int lane) { if (lane == 0) __hip_atomic_store(w, 1u, RLX_WG); }
DI void lds_wait(unsigned* w) { while (__hip_atomic_load(w, RLX_WG) == 0u) __builtin_amdgcn_s_sleep(1); asm volatile("" ::: "memory"); }
DI int blk_of(int mb, int cb) { return (mb >> 2) + 8 * (((mb & 3) << 3) | cb); }

#define LDS_TOTAL 163840
#define L_STAT 162304
#define L_BIAS 163328
#define L_ACON 163584
#define L_KSC 163712
#define L_CNT 163744
#define KAPSZ 2576
#define L_KAP 141696
#define L_COLS 152000
#define K1_SLOT 32768
#define L_TB 98304
#define L_KP 131584
#define KPSZ 4160
#define K2_R 4
#define L_RING2 0
#define L_W 65536
#define WSZ 33024
#define L_U L_KAP
#define USZ 4608
#define L_RED L_W
#define RED_RS 272
#define L_ST L_KAP
#define ST_RS 144
#define C_PUB 0
#define C_SP 1
#define C_H 2
#define C_S 3
#define C_W2 4
#define C_R1 5
#define C_ST 6
#define C_K0 7
#define C_K1 9
#define C_K2 12
#define C_K3 14
#define C_LN1 16
#define C_LN2 17
#define C_X2 18
#define C_CS 19
#define C_EP 20
DI void arrive(char* lds, int c, int lane) { if (lane == 0) __hip_atomic_fetch_add((unsigned*)(lds + L_CNT) + c, 1u, RLX_WG); }
template <int SL> DI void await_(char* lds, int c, unsigned target) {
  while (__hip_atomic_load((unsigned*)(lds + L_CNT) + c, RLX_WG) < target) __builtin_amdgcn_s_sleep(SL);
  asm volatile("" ::: "memory"); SCHED();
}
#define ARRIVE_V(n, c) do { SCHED(); asm volatile("s_waitcnt vmcnt(" #n ") lgkmcnt(0)" ::: "memory"); arrive(lds, c, lane); SCHED(); } while (0)
#define ARRIVE_L(c) do { SCHED(); asm volatile("s_waitcnt lgkmcnt(0)" ::: "memory"); arrive(lds, c, lane); SCHED(); } while (0)

struct cf { float x, y; };
DI cf cmul(cf a, cf b) { return cf{fmaf(a.x, b.x, -(a.y * b.y)), fmaf(a.x, b.y, a.y * b.x)}; }
DI unsigned pkc(cf a) { return pkh(a.x, a.y); }
DI unsigned pkcc(cf a) { return pkh(a.x, -a.y); }
DI cf zpow(float al, float om, float p) {
  const float m = __builtin_amdgcn_exp2f(al * p), ph = __builtin_amdgcn_fractf(om * p);
  return cf{m * __builtin_amdgcn_cosf(ph), m * __builtin_amdgcn_sinf(ph)};
}
DI unsigned cmulh(unsigned a, unsigned b) {
  const f16x2 z = __builtin_bit_cast(f16x2, a), w = __builtin_bit_cast(f16x2, b);
  const f16x2 t = z * (f16x2){w[0], w[0]};
  const f16x2 r = __builtin_elementwise_fma((f16x2){-z[1], z[0]}, (f16x2){w[1], w[1]}, t);
  return __builtin_bit_cast(unsigned, r);
}
DI u32x4 cmulh4(u32x4 a, u32x4 b) { return (u32x4){cmulh(a[0], b[0]), cmulh(a[1], b[1]), cmulh(a[2], b[2]), cmulh(a[3], b[3])}; }
#define TB_RS 272
#define TB_N 33
DI void kgen_tables(unsigned (&T)[TB_N], float& isc, unsigned raw0, unsigned raw1, float ldtv, char* lds, int cw, int lane) {
  const int src = ((lane & 31) >> 1) * 4, sh = (lane & 1) ? 0 : 16;
  unsigned pv[4];
#pragma unroll
  for (int a = 0; a < 4; ++a) {
    const unsigned lo = (unsigned)__builtin_amdgcn_ds_bpermute(src + 64 * a, (int)raw0), hi = (unsigned)__builtin_amdgcn_ds_bpermute(src + 64 * a, (int)raw1);
    pv[a] = ((lane < 32 ? lo : hi) << sh) & 0xffff0000u;
  }
  const float dt = rbf(__builtin_amdgcn_exp2f(ldtv * 1.4426950408889634f));
  const float ar = rbf(__builtin_amdgcn_exp2f(__uint_as_float(pv[0]) * 1.4426950408889634f));
  const float al = -(ar * dt) * 1.4426950408889634f;
  const float om = (__uint_as_float(pv[1]) * dt) * 0.15915494309189535f;
  const float cr = __uint_as_float(pv[2]) * dt, ci = __uint_as_float(pv[3]) * dt;
  const float sabs = 2.f * rdlane(wave_sum63(fabsf(cr) + fabsf(ci)), 63);
  int ex = (int)((__float_as_uint(sabs) >> 23) & 255);
  ex = ex < 20 ? 20 : (ex > 230 ? 230 : ex);
  const float sc2 = __uint_as_float((unsigned)(268 - ex) << 23);
  isc = __uint_as_float((unsigned)(ex - 13) << 23);
  if (lane == 0) *(float*)(lds + L_KSC + cw * 4) = sabs;
  const cf z1 = zpow(al, om, 1.f), z8 = zpow(al, om, 8.f), z32 = zpow(al, om, 32.f), a1 = zpow(al, om, 128.f), a4 = zpow(al, om, 512.f);
  const unsigned one = 0x00003C00u;
  const cf a2 = cmul(a1, a1), a3 = cmul(a2, a1), a8 = cmul(a4, a4), a12 = cmul(a8, a4);
  T[0] = one; T[1] = pkc(a1); T[2] = pkc(a2); T[3] = pkc(a3);
  T[4] = pkc(a4); T[5] = pkc(cmul(a4, a1)); T[6] = pkc(cmul(a4, a2)); T[7] = pkc(cmul(a4, a3));
  T[8] = pkc(a8); T[9] = pkc(cmul(a8, a1)); T[10] = pkc(cmul(a8, a2)); T[11] = pkc(cmul(a8, a3));
  T[12] = pkc(a12); T[13] = pkc(cmul(a12, a1)); T[14] = pkc(cmul(a12, a2)); T[15] = pkc(cmul(a12, a3));
  T[16] = pkc(cmul(a8, a8));
  const cf e2 = cmul(z1, z1), e3 = cmul(e2, z1), e4 = cmul(e2, e2);
  T[17] = one; T[18] = pkc(z1); T[19] = pkc(e2); T[20] = pkc(e3); T[21] = pkc(e4);
  T[22] = pkc(cmul(e4, z1)); T[23] = pkc(cmul(e4, e2)); T[24] = pkc(cmul(e4, e3));
  const cf c0 = cf{cr * sc2, ci * sc2}, c2 = cmul(c0, cmul(z32, z32));
  T[25] = pkcc(c0); T[26] = pkcc(cmul(c0, z32)); T[27] = pkcc(c2); T[28] = pkcc(cmul(c2, z32));
  const cf z16 = cmul(z8, z8);
  T[29] = one; T[30] = pkcc(z8); T[31] = pkcc(z16); T[32] = pkcc(cmul(z16, z8));
}
#define KG_PARAMS f32x4& kX, f32x4& kY, f32x4& kZ, float isc
DI void kgen_store(KG_PARAMS, char* lds, int wid, int lane) {
  const int i16 = lane & 15, g = lane >> 4;
  float* kap = (float*)(lds + L_KAP + wid * KAPSZ);
  *(f32x4*)(kap + 128 * (g >> 1) + 4 * (g & 1) + 8 * i16) = kX * isc;
  if (g < 2) {
    *(f32x4*)(kap + 256 + 64 * g + 4 * i16) = kY * isc;
#pragma unroll
    for (int r = 0; r < 4; ++r) kap[512 + 16 * (4 * g + r) + i16] = kZ[r] * isc;
  } else {
    *(f32x2*)(kap + 384 + 64 * (g - 2) + 2 * i16) = (f32x2){kY[0] * isc, kY[1] * isc};
    *(f32x2*)(kap + 384 + 64 * (g - 2) + 32 + 2 * i16) = (f32x2){kY[2] * isc, kY[3] * isc};
    if (g == 2 && i16 == 0) kap[640] = kZ[0] * isc;
  }
}
DI const u16* prm_ptr(const u16* lar, const u16* aim, const u16* cre, const u16* cim, int d, int hf, int lane) {
  const int g = lane >> 4;
  const u16* p = g == 0 ? lar : (g == 1 ? aim : (g == 2 ? cre : cim));
  return p + d * N_ + 64 * hf + 2 * (lane & 15);
}
DI void ld_u32(unsigned& v, const void* p) { asm volatile("global_load_dword %0, %1, off" : "=v"(v) : "v"(p) : "memory"); }
DI void ld_u64(u64& v, const void* p) { asm volatile("global_load_dwordx2 %0, %1, off sc1" : "=v"(v) : "v"(p) : "memory"); }
DI void gload16(u32x4& v, unsigned voff, const char* sbase) {
  asm volatile("global_load_dwordx4 %0, %1, %2 sc1" : "=v"(v) : "v"(voff), "s"(sbase) : "memory");
}
DI u64 rdlane64(u64 v, int l) { return (u64)(unsigned)__builtin_amdgcn_readlane((int)(unsigned)v, l) | ((u64)(unsigned)__builtin_amdgcn_readlane((int)(unsigned)(v >> 32), l) << 32); }
DI bool last_of(char* lds, int c, unsigned n, int lane) {
  unsigned old = 0;
  if (lane == 0) old = __hip_atomic_fetch_add((unsigned*)(lds + L_CNT) + c, 1u, RLX_WG);
  return (unsigned)__builtin_amdgcn_readfirstlane(old) == n - 1;
}
DI float gelu_f(float v) {
  const float c0 = -2.302208198f;
  const float c1 = -0.1029432397f;
  const float z = v * fmaf(v * v, c1, c0);
  return v * __builtin_amdgcn_rcpf(1.f + __builtin_amdgcn_exp2f(z));
}
#define K2S(S) ((S) + r2 >= 14 ? (S) + r2 - 14 : (S) + r2)
#define K2_DMA(S) { _Pragma("unroll") for (int pp = 0; pp < 4; ++pp) dma16s(wvoff, wsrc + (size_t)(K2S(S) * 32 + pp * 8) * D_, ringa + ((S) % K2_R) * 4096 + pp * 1024); SCHED(); }
#define K2_SETUP(kq_) int lane2_ = lane; asm volatile("" : "+v"(lane2_)); \
  const unsigned wvoff = (lane2_ >> 3) * (D_ * 2) + (((lane2_ & 7) ^ (((lane2_ >> 4) & 1) << 2)) << 4); \
  const unsigned ringa = __builtin_amdgcn_readfirstlane(lds_base(lds) + L_RING2 + (kq_) * (K2_R * 4096)); \
  const u16* wsrc = w2 + (size_t)(14 * (kq_) * 32) * D_ + 64 * cb; \
  const int r2 = (2 * cb + 4 * (mb & 3)) % 14;
#define HMT 114688

DI void p1_gemm(const u16* __restrict__ x, const u16* __restrict__ w1, char* __restrict__ ws, char* lds, int panel, int fs, int wid, int lane TK0_ARG) {
  STAMP_DECL STAMP_INIT(0) STAMP0();
  const int wm = wid >> 2, wf = (wid >> 1) & 1, kh = wid & 1;
  const int r = lane & 31, h = lane >> 5;
  const int row0 = panel * 128, f0 = fs * 128;
  const unsigned xv0 = (lane >> 3) * 1024 + (((lane & 7) ^ ((lane >> 4) & 3)) << 4);
  const unsigned xv1 = xv0 ^ 64;
  const unsigned wv = (lane >> 4) * (F_ * 2) + (((lane & 15) ^ (((lane >> 4) & 3) << 2)) << 4);
  const u16* xsb = x + (size_t)(row0 + 16 * wid) * D_;
  const u16* wsb = w1 + (size_t)(8 * wid) * F_ + f0;
  const unsigned la = lds_base(lds) + wid * 2048;
  const int rot = 2 * (panel & 3) + (fs >= 7 ? fs - 7 : fs);
#define KS(S) (((S) + rot) & 7)
#define K1_D0(S) { dma16s(xv0, xsb + KS(S) * 64, la + ((S) % 4) * K1_SLOT); SCHED(); }
#define K1_D1(S) { dma16s(xv1, xsb + 8 * D_ + KS(S) * 64, la + ((S) % 4) * K1_SLOT + 1024); SCHED(); }
#define K1_D2(S) { dma16s(wv, wsb + (size_t)(KS(S) * 64) * F_, la + ((S) % 4) * K1_SLOT + 16384); SCHED(); }
#define K1_D3(S) { dma16s(wv, wsb + (size_t)(KS(S) * 64 + 4) * F_, la + ((S) % 4) * K1_SLOT + 16384 + 1024); SCHED(); }
#define K1_ISSUE(S) { K1_D0(S); K1_D1(S); K1_D2(S); K1_D3(S); }
  K1_ISSUE(0); K1_ISSUE(1); K1_ISSUE(2); K1_ISSUE(3);
  const int xoff = (64 * wm + r) * 128 + (((4 * kh + 2 * wm + h) ^ ((r >> 1) & 7)) << 4);
  const int q = (lane & 15) >> 2, p = lane & 3;
  const int woff0 = 16384 + (32 * kh + 16 * wm + 8 * h + q) * 256 + (((2 * wf + kh) ^ q) << 6) + ((lane >> 4) & 1) * 32 + 8 * p;
  f32x16 acc[2][2];
#pragma unroll
  for (int i = 0; i < 16; ++i) { acc[0][0][i] = 0.f; acc[0][1][i] = 0.f; acc[1][0][i] = 0.f; acc[1][1][i] = 0.f; }
  STAMP();
  s16x8 xf[2][2][2], wfr[2][2][2];
#define K1_M(T, s, mi, fi) { acc[mi][fi] = __builtin_amdgcn_mfma_f32_32x32x16_bf16(wfr[(T) & 1][s][fi], xf[(T) & 1][s][mi], acc[mi][fi], 0, 0, 0); SCHED(); }
#define K1_RX(T, s, mi) { xf[(T) & 1][s][mi] = *(const s16x8*)(lds + (xa_ ^ ((s) << 5)) + (mi) * 4096); SCHED(); }
#define K1_RW(T, s, fi) { wfr[(T) & 1][s][fi] = tr_frag(lds + (wa_ ^ (((fi) << 6) | ((s) << 12))), lds + (wa_ ^ (((fi) << 6) | ((s) << 12))) + 1024); SCHED(); }
#define K1_ADDR(T) int xa_ = xoff + ((T) % 4) * K1_SLOT, wa_ = woff0 + ((T) % 4) * K1_SLOT; asm volatile("" : "+v"(xa_), "+v"(wa_));
#define K1_READ(T) { K1_ADDR(T) K1_RX(T,0,0); K1_RW(T,0,0); K1_RW(T,0,1); K1_RX(T,0,1); K1_RX(T,1,0); K1_RW(T,1,0); K1_RW(T,1,1); K1_RX(T,1,1); }
#define K1_MMA(T) { K1_M(T,0,0,0); K1_M(T,0,0,1); K1_M(T,0,1,0); K1_M(T,0,1,1); K1_M(T,1,0,0); K1_M(T,1,0,1); K1_M(T,1,1,0); K1_M(T,1,1,1); }
#define K1_STAGE(T, DM0, DM1, DM2, DM3) { K1_ADDR(T) \
    K1_M((T)-1,0,0,0); K1_RX(T,0,0); K1_RW(T,0,0); K1_M((T)-1,0,0,1); K1_RW(T,0,1); K1_RX(T,0,1); \
    K1_M((T)-1,0,1,0); K1_RX(T,1,0); K1_RW(T,1,0); K1_M((T)-1,0,1,1); K1_RW(T,1,1); K1_RX(T,1,1); \
    K1_M((T)-1,1,0,0); DM0; K1_M((T)-1,1,0,1); DM1; K1_M((T)-1,1,1,0); DM2; K1_M((T)-1,1,1,1); DM3; }
#define K1_STAGE_D(T, S) K1_STAGE(T, K1_D0(S), K1_D1(S), K1_D2(S), K1_D3(S))
  WAITV_BAR(12); STAMP();
  K1_READ(0);
  WAITV_BAR(8); K1_STAGE_D(1, 4);
  WAITV_BAR(8); K1_STAGE_D(2, 5);
  WAITV_BAR(8); K1_STAGE_D(3, 6);
  WAITV_BAR(8); STAMP(); K1_STAGE_D(4, 7);
  WAITV_BAR(8); K1_STAGE(5, , , , );
  WAITV_BAR(4); K1_STAGE(6, , , , );
  K1_MMA(6);
  WAITV_BAR(0);
  K1_READ(7); K1_MMA(7);
  STAMP();
  float* colS = (float*)(lds + L_COLS);
  {
    char* dst = lds + wid * 8192 + lane * 16;
#pragma unroll
    for (int mi = 0; mi < 2; ++mi)
#pragma unroll
      for (int rq = 0; rq < 4; ++rq)
        *(f32x4*)(dst + (mi * 4 + rq) * 1024) = (f32x4){acc[mi][1][4 * rq], acc[mi][1][4 * rq + 1], acc[mi][1][4 * rq + 2], acc[mi][1][4 * rq + 3]};
  }
  LGKM_BAR();
  STAMP();
  f32x4 pa[2][4], cq[4];
  f32x2 st[2];
  u32x2 bq[4];
  {
    const char* src = lds + (wid ^ 1) * 8192 + lane * 16;
    const int fl = 64 * wf + 32 * kh + 4 * h;
#pragma unroll
    for (int rq = 0; rq < 4; ++rq) {
      cq[rq] = *(const f32x4*)(colS + fl + 8 * rq);
      bq[rq] = *(const u32x2*)(lds + L_BIAS + (fl + 8 * rq) * 2);
      pa[0][rq] = *(const f32x4*)(src + rq * 1024); pa[1][rq] = *(const f32x4*)(src + (4 + rq) * 1024);
    }
    st[0] = *(const f32x2*)(lds + L_STAT + (64 * wm + r) * 8); st[1] = *(const f32x2*)(lds + L_STAT + (64 * wm + 32 + r) * 8);
  }
  LGKM_BAR();
  {
    const __amdgpu_buffer_rsrc_t hr = __builtin_amdgcn_make_buffer_rsrc(ws + WS_H, 0, L_ * F_ * 2, 0x00020000);
    const unsigned hoff = (unsigned)(panel * 4 + 2 * wm) * HMT + (8 * fs + 4 * wf + 2 * kh) * 1024 + lane * 16;
#pragma unroll
    for (int mi = 0; mi < 2; ++mi) {
      const float mr = st[mi][0], rinv = st[mi][1];
#pragma unroll
      for (int s2 = 0; s2 < 2; ++s2) {
        u32x4 pk;
#pragma unroll
        for (int qq = 0; qq < 2; ++qq) {
          const int rq = 2 * s2 + qq;
          const float b0 = bflo(bq[rq][0]), b1_ = bfhi(bq[rq][0]), b2_ = bflo(bq[rq][1]), b3 = bfhi(bq[rq][1]);
          const float o0 = gelu_f(fmaf(acc[mi][0][4 * rq] + pa[mi][rq][0], rinv, fmaf(mr, cq[rq][0], b0)));
          const float o1 = gelu_f(fmaf(acc[mi][0][4 * rq + 1] + pa[mi][rq][1], rinv, fmaf(mr, cq[rq][1], b1_)));
          const float o2 = gelu_f(fmaf(acc[mi][0][4 * rq + 2] + pa[mi][rq][2], rinv, fmaf(mr, cq[rq][2], b2_)));
          const float o3 = gelu_f(fmaf(acc[mi][0][4 * rq + 3] + pa[mi][rq][3], rinv, fmaf(mr, cq[rq][3], b3)));
          pk[2 * qq] = pkbf(o0, o1); pk[2 * qq + 1] = pkbf(o2, o3);
        }
        __builtin_amdgcn_raw_buffer_store_b128(pk, hr, hoff + mi * HMT + s2 * 1024, 0, 16);
        SCHED();
      }
    }
  }
  STAMP();
  asm volatile("s_waitcnt vmcnt(0)" ::: "memory");
  if (last_of(lds, C_PUB, 8, lane) && lane < 16) {
    const u64 ac = *(const u64*)(lds + L_ACON + lane * 8);
    int l2 = lane; asm volatile("" : "+v"(l2));
    __hip_atomic_store((u64*)(ws + WS_FLAG + (size_t)blk_of(2 * panel + ((l2 >> 3) & 1), l2 & 7) * 1024 + FL_H) + fs, ac + 1, RLX_AGENT);
  }
  STAMP(); STAMP_FLUSH();
}

#define LN_RS 1040
DI void p1_aux(const u16* __restrict__ x, char* __restrict__ ws, char* lds, int idx, int wid, int lane TK0_ARG) {
  STAMP_DECL STAMP_INIT(0) STAMP0();
  const int t0 = idx * 64;
  u32x4 v[8];
#pragma unroll
  for (int i = 0; i < 8; ++i) v[i] = *(const u32x4*)(x + (size_t)(t0 + 8 * wid + i) * D_ + lane * 8);
  const u64 aCon = __hip_atomic_load((const u64*)(ws + WS_FLAG + (size_t)((wid * 64 + lane) & 255) * 1024), RLX_AGENT);
  const int sfs = lane < 14 ? lane : 0;
  const int sblk = 2 * (idx >> 3) + (sfs >= 7 ? 1 : 0) + 8 * (((idx >> 1) & 3) * 7 + (sfs >= 7 ? sfs - 7 : sfs));
  const u64 aSt = __hip_atomic_load((const u64*)(ws + WS_FLAG + (size_t)sblk * 1024), RLX_AGENT);
  SCHED();
  asm volatile("s_barrier" ::: "memory");
  SCHED();
#pragma unroll
  for (int i = 0; i < 8; ++i) *(u32x4*)(lds + (63 - (8 * wid + i)) * LN_RS + lane * 16) = v[i];
  ARRIVE_L(C_LN1);
  float sA[8], sB[8];
#pragma unroll
  for (int i = 0; i < 8; ++i) {
    float a = dot2(v[i][0], 0x3F803F80u, 0.f), b = dot2(v[i][0], v[i][0], 0.f);
#pragma unroll
    for (int c = 1; c < 4; ++c) { a = dot2(v[i][c], 0x3F803F80u, a); b = dot2(v[i][c], v[i][c], b); }
    sA[i] = a; sB[i] = b;
  }
#pragma unroll
  for (int i = 0; i < 4; ++i) { sA[i] = comb32(sA[i], sA[i + 4]); sB[i] = comb32(sB[i], sB[i + 4]); }
#pragma unroll
  for (int i = 0; i < 2; ++i) { sA[i] = row_allsum(comb16(sA[i], sA[i + 2])); sB[i] = row_allsum(comb16(sB[i], sB[i + 2])); }
  const float mv = ((lane & 1) ? sA[1] : sA[0]) * (1.f / 512.f);
  const float var = fmaxf(fmaf(-mv, mv, ((lane & 1) ? sB[1] : sB[0]) * (1.f / 512.f)), 0.f);
  const float mbv = rbf(mv);
  const float sd = rbf(sqrtf(rbf(rbf(var) + 1e-5f)));
  const float rv = 1.f / sd;
  if ((lane & 14) == 0) {
    const u64 pk = (u64)__float_as_uint(-mbv * rv) | ((u64)__float_as_uint(rv) << 32);
    const int tt = t0 + 8 * wid + (lane & 1) + 2 * (lane >> 4);
    __hip_atomic_store((u64*)(ws + WS_STAT) + tt, pk, RLX_AGENT);
    __hip_atomic_store((unsigned*)(ws + WS_ST2) + (L_ - 1 - tt), (__float_as_uint(mbv) >> 16) | __float_as_uint(sd), RLX_AGENT);
  }
  STAMP();
  await_<0>(lds, C_LN1, 8);
  char* t2 = lds + 64 * LN_RS + wid * 8192;
  const int g = lane >> 4, q = (lane & 15) >> 2, p = lane & 3, dl = lane & 15;
  s16x8 f[8];
#pragma unroll
  for (int i = 0; i < 8; ++i) {
    const char* a = lds + (8 * (4 * (i & 1) + g) + q) * LN_RS + (64 * wid + 16 * (i >> 1) + 4 * p) * 2;
    f[i] = tr_frag(a, a + 4 * LN_RS);
  }
  SCHED();
#pragma unroll
  for (int i = 0; i < 8; ++i) {
    const int dd2 = 16 * (i >> 1) + dl, rc = 4 * (i & 1) + g;
    *(s16x8*)(t2 + dd2 * 128 + ((rc ^ (dd2 & 7)) << 4)) = f[i];
  }
  SCHED();
  const __amdgpu_buffer_rsrc_t ur = __builtin_amdgcn_make_buffer_rsrc(ws + WS_UR, 0, L_ * D_ * 2, 0x00020000);
  const int x0 = L_ - 64 - t0;
  u32x4 o[8];
#pragma unroll
  for (int i = 0; i < 8; ++i) {
    const int dd2 = 8 * i + (lane >> 3), c = lane & 7;
    o[i] = *(const u32x4*)(t2 + dd2 * 128 + ((c ^ (dd2 & 7)) << 4));
  }
  SCHED();
  asm volatile("s_waitcnt vmcnt(0)" ::: "memory");
  if (last_of(lds, C_PUB, 8, lane) && lane < 14) __hip_atomic_store((u64*)(ws + WS_FLAG + (size_t)sblk * 1024 + FL_ST) + (idx & 1), aSt + 1, RLX_AGENT);
  STAMP();
#pragma unroll
  for (int i = 0; i < 8; ++i)
    __builtin_amdgcn_raw_buffer_store_b128(o[i], ur, (unsigned)(64 * wid + 8 * i + (lane >> 3)) * 4096 + x0 * 2 + (lane & 7) * 16, 0, 16);
  STAMP();
  ARRIVE_L(C_X2);
  ARRIVE_V(0, C_LN2);
  if (wid < 4) {
    await_<0>(lds, C_LN2, 8);
    __hip_atomic_store((u64*)(ws + WS_FLAG + (size_t)(wid * 64 + lane) * 1024 + FL_LN) + idx, aCon + 1, RLX_AGENT);
  }
  STAMP(); STAMP_FLUSH();
}

DI void x_wave(const u16* __restrict__ lar, const u16* __restrict__ aim, const u16* __restrict__ cre, const u16* __restrict__ cim,
               unsigned sdt, unsigned sds, const u16* __restrict__ w2, const u16* __restrict__ w1, const u16* __restrict__ b1, int auxidx, int panel, int fs,
               char* __restrict__ ws, char* lds, int mb, int cb, int cw, int lane TK0_ARG) {
  STAMP_DECL STAMP_INIT(1) STAMP0();
  __builtin_amdgcn_s_setprio(3);
  const bool aux = auxidx >= 0;
  const int ch = cw >> 1, hf = cw & 1;
  const int d = 64 * cb + 2 * mb + ch;
  const int i16 = lane & 15, g = lane >> 4;
  unsigned* cnt = (unsigned*)(lds + L_CNT);
  const char* flg = ws + WS_FLAG + (size_t)blockIdx.x * 1024;
  const u16* pp_ = prm_ptr(lar, aim, cre, cim, d, hf, lane);
  unsigned raw0, raw1;
  u64 aS, aH;
  ld_u32(raw0, pp_); ld_u32(raw1, pp_ + 32);
  ld_u64(aS, ws + WS_FLAG + (size_t)(lane >= 32 ? (int)blockIdx.x : blk_of(lane, cb)) * 1024);
  ld_u64(aH, ws + WS_FLAG + (size_t)blk_of(2 * panel + ((lane >> 3) & 1), lane & 7) * 1024);
  if (!aux && cw == 2) asm volatile("s_mov_b32 m0, %2\n\ts_nop 0\n\tglobal_load_lds_dword %0, %1" :: "v"(lane * 4), "s"(b1 + fs * 128), "s"(lds_base(lds) + L_BIAS) : "memory");
  if (cw == 0 && lane < 24) cnt[lane] = 0u;
  if (aux) {
    asm volatile("s_waitcnt lgkmcnt(0)\n\ts_barrier" ::: "memory");
    __builtin_amdgcn_s_setprio(1);
    if (auxidx < 28) {
      const u16* wp_ = w1 + (size_t)(128 * cw + (lane >> 3)) * F_ + 64 * auxidx + 8 * (lane & 7);
      const int cblk = 2 * ((lane & 15) >> 2) + (auxidx >= 14 ? 1 : 0) + 8 * ((lane & 3) * 7 + ((auxidx >> 1) % 7));
      const u64 aC = __hip_atomic_load((const u64*)(ws + WS_FLAG + (size_t)cblk * 1024), RLX_AGENT);
      float a8[8] = {0.f, 0.f, 0.f, 0.f, 0.f, 0.f, 0.f, 0.f};
      u32x4 wv[16];
#pragma unroll
      for (int i = 0; i < 16; ++i) wv[i] = *(const u32x4*)(wp_ + (size_t)(8 * i) * F_);
      SCHED();
#pragma unroll
      for (int i = 0; i < 16; ++i)
#pragma unroll
        for (int c = 0; c < 4; ++c) { a8[2 * c] += bflo(wv[i][c]); a8[2 * c + 1] += bfhi(wv[i][c]); }
#pragma unroll
      for (int i = 0; i < 4; ++i) a8[i] = comb32(a8[i], a8[i + 4]);
#pragma unroll
      for (int i = 0; i < 2; ++i) a8[i] = dpp_add<0x128, 0xf>(comb16(a8[i], a8[i + 2]));
      if (!(lane & 8)) *(f32x2*)(lds + L_COLS + cw * 256 + (8 * (lane & 7) + 2 * (g & 1) + 4 * (g >> 1)) * 4) = (f32x2){a8[0], a8[1]};
      SCHED(); asm volatile("s_waitcnt lgkmcnt(0)" ::: "memory");
      if (last_of(lds, C_CS, 4, lane)) {
        const float* cp = (const float*)(lds + L_COLS);
        const float tot = (cp[lane] + cp[64 + lane]) + (cp[128 + lane] + cp[192 + lane]);
        __hip_atomic_store((float*)(ws + WS_CS) + 64 * auxidx + lane, tot, RLX_AGENT);
        asm volatile("s_waitcnt vmcnt(0)" ::: "memory");
        if (lane < 16) __hip_atomic_store((u64*)(ws + WS_FLAG + (size_t)cblk * 1024 + FL_ST) + 2 + (auxidx & 1), aC + 1, RLX_AGENT);
      }
    }
  }
  asm volatile("s_waitcnt vmcnt(2)" : "+v"(raw0), "+v"(raw1) :: "memory");
  float isc;
  unsigned T[TB_N];
  kgen_tables(T, isc, raw0, raw1, (d & 1) ? bfhi(sdt) : bflo(sdt), lds, cw, lane);
#pragma unroll
  for (int i = 0; i < TB_N; ++i) asm volatile("" : "+v"(T[i]));
  asm volatile("s_waitcnt vmcnt(0)" : "+v"(aS), "+v"(aH) :: "memory");
  const u64 aOwn = rdlane64(aS, 32);
  if (cw == 1 && lane < 16) *(u64*)(lds + L_ACON + lane * 8) = aH;
  if (cw == 3) { if (lane == 0) *(u64*)(lds + L_KSC + 16) = aOwn; ARRIVE_L(C_EP); }
  __builtin_amdgcn_s_setprio(1);
  STAMP();
  const u64* lnfl = (const u64*)(flg + FL_LN) + (lane & 31);
  const char* up = ws + WS_UR + (size_t)d * 4096 + hf * 2048;
  u64 fv;
  if (aux) {
    await_<2>(lds, C_X2, 8);
  } else {
    const u64* stfl = (const u64*)(flg + FL_ST) + (lane & 3);
    const char* stp = cw == 1 ? ws + WS_CS + fs * 512 : ws + WS_STAT + (size_t)panel * 1024;
    const unsigned svo = cw == 1 ? (lane & 31) * 16 : lane * 16;
    u32x4 stv;
#define XBAR() do { SCHED(); asm volatile("s_waitcnt lgkmcnt(0)\n\ts_barrier" ::: "memory"); SCHED(); } while (0)
    if (cw < 2) {
      bool ok = false, got = false;
      XBAR(); XBAR(); XBAR(); XBAR();
      ld_u64(fv, stfl); gload16(stv, svo, stp);
#define XPOLL() do { XBAR(); asm volatile("s_waitcnt vmcnt(0)" : "+v"(fv), "+v"(stv) :: "memory"); \
        if (ok && !got) { *(u32x4*)(lds + (cw == 1 ? L_COLS : L_STAT) + svo) = stv; got = true; } \
        ok = __all(fv == aOwn + 1); SCHED(); ld_u64(fv, stfl); gload16(stv, svo, stp); } while (0)
      XPOLL(); XPOLL(); XPOLL(); XPOLL();
      asm volatile("s_waitcnt vmcnt(0)" : "+v"(fv), "+v"(stv) :: "memory");
      if (!got) {
        if (!ok) {
          poll_flags(stfl, aOwn + 1);
          asm volatile("global_load_dwordx4 %0, %1, %2 sc1\n\ts_waitcnt vmcnt(0)" : "=&v"(stv) : "v"(svo), "s"(stp) : "memory");
        }
        *(u32x4*)(lds + (cw == 1 ? L_COLS : L_STAT) + svo) = stv;
      }
    } else {
      XBAR(); XBAR(); XBAR(); XBAR(); XBAR(); XBAR(); XBAR(); XBAR();
    }
    XBAR();
    XBAR();
  }
  STAMP();
  __builtin_amdgcn_s_setprio(3);
  K2_SETUP(cw);
  K2_DMA(0); K2_DMA(1);
  char* wl = lds + L_W + ch * WSZ;
  {
    char* tb = wl + hf * 9216;
#pragma unroll
    for (int i = 0; i < TB_N; ++i) *(unsigned*)(tb + i * TB_RS + lane * 4) = T[i];
    const char* tr = tb + g * 16;
    const int rXa = (i16 >> 3) * TB_RS, rXe = (17 + (i16 & 7)) * TB_RS;
    const int rYa = (i16 < 8 ? 2 + (i16 >> 2) : 4 + ((i16 - 8) >> 1)) * TB_RS, rYe = (17 + (i16 < 8 ? 2 * (i16 & 3) : 4 * (i16 & 1))) * TB_RS;
    const int rZa = (i16 < 8 ? 8 + i16 : 16) * TB_RS;
    const int rB1 = (25 + (i16 >> 2)) * TB_RS, rB0 = (29 + (i16 & 3)) * TB_RS;
    f32x4 kX = {0.f, 0.f, 0.f, 0.f}, kY = kX, kZ = kX;
#pragma unroll
    for (int sb = 0; sb < 4; sb += 2) {
      u32x4 t_[2][7];
#pragma unroll
      for (int s = 0; s < 2; ++s) {
        const char* tq = tr + (sb + s) * 64;
        t_[s][0] = *(const u32x4*)(tq + rB1); t_[s][1] = *(const u32x4*)(tq + rB0); t_[s][2] = *(const u32x4*)(tq + rXa); t_[s][3] = *(const u32x4*)(tq + rXe);
        t_[s][4] = *(const u32x4*)(tq + rYa); t_[s][5] = *(const u32x4*)(tq + rYe); t_[s][6] = *(const u32x4*)(tq + rZa);
      }
      SCHED();
#pragma unroll
      for (int s = 0; s < 2; ++s) {
        const f16x8 hb = __builtin_bit_cast(f16x8, cmulh4(t_[s][0], t_[s][1]));
        kX = __builtin_amdgcn_mfma_f32_16x16x32_f16(__builtin_bit_cast(f16x8, cmulh4(t_[s][2], t_[s][3])), hb, kX, 0, 0, 0);
        kY = __builtin_amdgcn_mfma_f32_16x16x32_f16(__builtin_bit_cast(f16x8, cmulh4(t_[s][4], t_[s][5])), hb, kY, 0, 0, 0);
        kZ = __builtin_amdgcn_mfma_f32_16x16x32_f16(__builtin_bit_cast(f16x8, t_[s][6]), hb, kZ, 0, 0, 0);
      }
      SCHED();
    }
    kgen_store(kX, kY, kZ, isc, lds, cw, lane);
  }
  ARRIVE_L(C_K0 + ch);
  ld_u64(fv, lnfl);
  await_<0>(lds, C_K0 + ch, 2);
  const f32x2 ks = *(const f32x2*)(lds + L_KSC + ch * 8);
  int ex = (int)((__float_as_uint(ks[0] + ks[1]) >> 23) & 255);
  ex = ex < 20 ? 20 : (ex > 230 ? 230 : ex);
  const float scale = __uint_as_float((unsigned)(267 - ex) << 23);
  const float iscale = __uint_as_float((unsigned)(ex - 13) << 23);
  char* kp = lds + L_KP + ch * KPSZ;
  {
    const float* k0 = (const float*)(lds + L_KAP + (2 * ch) * KAPSZ);
#define KSUM2(T, i) ((*(const T*)(k0 + (i)) + *(const T*)(k0 + 644 + (i))) * scale)
    const int A = 64 * hf + lane, A2 = 128 + A;
    const float e0 = KSUM2(float, 512 + A), e1 = KSUM2(float, 513 + A);
    u32x4 o;
    if (hf == 0) {
      const int i0 = lane < 32 ? 8 * A : 256 + 4 * (A - 32);
      const f32x4 a = KSUM2(f32x4, i0), b = KSUM2(f32x4, i0 + 4);
      const bool lo = lane < 32;
      o = (u32x4){pkh(a[0], lo ? a[1] : a[0]), pkh(lo ? a[2] : a[1], lo ? a[3] : a[2]), pkh(lo ? b[0] : a[2], lo ? b[1] : a[2]), pkh(lo ? b[2] : a[3], lo ? b[3] : b[0])};
      if (lane < 2) *(u32x4*)(kp + lane * 16) = (u32x4){0u, 0u, 0u, 0u};
    } else {
      const int i0 = 384 + 2 * lane;
      const f32x2 a = KSUM2(f32x2, i0), b = KSUM2(f32x2, i0 + 2);
      o = (u32x4){pkh(a[0], a[0]), pkh(a[0], a[1]), pkh(a[1], a[1]), pkh(b[0], b[0])};
    }
    *(u32x4*)(kp + 32 + A * 16) = o;
    const unsigned p00 = pkh(e0, e0);
    *(u32x4*)(kp + 32 + A2 * 16) = (u32x4){p00, p00, pkh((A2 & 1) ? e1 : e0, e1), pkh(e1, e1)};
  }
  ARRIVE_L(C_K1);
  asm volatile("s_waitcnt vmcnt(0)" : "+v"(fv) :: "memory");
  arrive(lds, C_W2, lane);
  const bool urok = __all(fv == aOwn + 1);
  u32x4 ua_, ub_, q0, q1, q2, q3;
  const char* qp = ws + WS_ST2 + hf * 4096;
#define U_LOADS() { gload16(ua_, lane * 16, up); gload16(ub_, lane * 16, up + 1024); \
    gload16(q0, lane * 32, qp); gload16(q1, lane * 32, qp + 16); gload16(q2, lane * 32, qp + 2048); gload16(q3, lane * 32, qp + 2064); }
#define U_WAIT() asm volatile("s_waitcnt vmcnt(0)" : "+v"(ua_), "+v"(ub_), "+v"(q0), "+v"(q1), "+v"(q2), "+v"(q3) :: "memory")
  U_LOADS();
  await_<0>(lds, C_K1, 4);
  STAMP();
#pragma unroll
  for (int rnd = 0; rnd < 3; ++rnd) {
    const int a = rnd < 2 ? 128 * rnd + 64 * hf + lane : 256;
    if (rnd == 2 && !(hf == 0 && lane == 0)) break;
    const u32x4 c0 = *(const u32x4*)(kp + a * 16), c1 = *(const u32x4*)(kp + a * 16 + 16);
    const unsigned e[8] = {c0[0], c0[1], c0[2], c0[3], c1[0], c1[1], c1[2], c1[3]};
    unsigned o[7];
#pragma unroll
    for (int i = 0; i < 7; ++i) o[i] = __builtin_amdgcn_alignbit(e[i + 1], e[i], 16);
    const int m = (a & 1) | (((a >> 2) & 1) << 1) | (((a >> 1) & 1) << 2);
    char* base = wl + a * 128;
#pragma unroll
    for (int c = 0; c < 8; ++c) {
      u32x4 wv;
      if (c & 1) { const int s = (c + 1) >> 1; wv = (u32x4){e[s], e[s + 1], e[s + 2], e[s + 3]}; }
      else { const int s = c >> 1; wv = (u32x4){o[s], o[s + 1], o[s + 2], o[s + 3]}; }
      *(u32x4*)(base + ((c ^ m) << 4)) = wv;
    }
  }
  SCHED();
  U_WAIT();
  if (!urok) { poll_flags(lnfl, aOwn + 1); U_LOADS(); U_WAIT(); }
  char* ul = lds + L_U + ch * USZ;
  {
#define UN2(xw, s0, s1) ({ const unsigned a_ = pkbf(bflo(xw) - bflo(s0), bfhi(xw) - bflo(s1)); \
      const unsigned b_ = pkbf(bflo(a_) * __builtin_amdgcn_rcpf(bfhi(s0)), bfhi(a_) * __builtin_amdgcn_rcpf(bfhi(s1))); pkh(bflo(b_), bfhi(b_)); })
    const u32x4 o0 = {UN2(ua_[0], q0[0], q0[1]), UN2(ua_[1], q0[2], q0[3]), UN2(ua_[2], q1[0], q1[1]), UN2(ua_[3], q1[2], q1[3])};
    const u32x4 o1 = {UN2(ub_[0], q2[0], q2[1]), UN2(ub_[1], q2[2], q2[3]), UN2(ub_[2], q3[0], q3[1]), UN2(ub_[3], q3[2], q3[3])};
    *(u32x4*)(ul + hf * 2048 + lane * 16) = o0;
    *(u32x4*)(ul + hf * 2048 + 1024 + lane * 16) = o1;
  }
  if (hf == 0 && lane < 32) *(u32x4*)(ul + 4096 + lane * 16) = (u32x4){0u, 0u, 0u, 0u};
  ARRIVE_L(C_K2 + ch);
  STAMP();
  await_<2>(lds, C_PUB, 8);
  await_<0>(lds, C_K2 + ch, 2);
  __builtin_amdgcn_s_setprio(3);
  STAMP();
  f32x4 acc[8];
#pragma unroll
  for (int i = 0; i < 8; ++i) acc[i] = (f32x4){0.f, 0.f, 0.f, 0.f};
  {
    const int c = i16 + 8 * g;
    int fo[2];
#pragma unroll
    for (int par = 0; par < 2; ++par) {
      const int w = 32 * par + c;
      const int m = ((w >> 3) & 1) | (((w >> 5) & 1) << 1) | (((w >> 4) & 1) << 2);
      fo[par] = (w ^ m) << 4;
    }
    const char* fbase = wl + hf * (128 * 16);
    const char* gbase = ul + (2032 - 16 * i16 + 8 * g + 128 * hf) * 2 - 7 * 512;
#define CF(s, e) (*(const f16x8*)(fbase + fo[(s) & 1] + ((s) >> 1) * 1024 + (e) * 4096))
#define CG(s, mu) (*(const f16x8*)(gbase + (s) * 64 + (7 - (mu)) * 512))
    f16x8 F[2][8], G[2];
#pragma unroll
    for (int e = 0; e < 8; ++e) F[0][e] = CF(0, e);
    G[0] = CG(0, 0);
#pragma unroll
    for (int s = 0; s < 4; ++s) {
#pragma unroll
      for (int mu = 0; mu < 8; ++mu) {
        const int it = s * 8 + mu;
        SCHED();
        if (it + 1 < 32) G[(it + 1) & 1] = CG((it + 1) >> 3, (it + 1) & 7);
        if (s < 3) F[(s + 1) & 1][mu] = CF(s + 1, mu);
        SCHED();
#pragma unroll
        for (int e = 0; e + mu < 8; ++e) acc[e + mu] = __builtin_amdgcn_mfma_f32_16x16x32_f16(F[s & 1][e], G[it & 1], acc[e + mu], 0, 0, 0);
      }
    }
  }
  STAMP();
  ARRIVE_L(C_K1 + 1 + ch); await_<0>(lds, C_K1 + 1 + ch, 2);
  {
    char* dst = wl + hf * 4096 + lane * 16;
#pragma unroll
    for (int i = 0; i < 4; ++i) *(f32x4*)(dst + i * 1024) = sel(hf, acc[i], acc[4 + i]);
  }
  ARRIVE_L(C_K3 + ch); await_<0>(lds, C_K3 + ch, 2);
  STAMP();
  {
    const float dskip = (d & 1) ? bfhi(sds) : bflo(sds);
    const char* src = wl + (hf ^ 1) * 4096 + lane * 16;
    char* sdst = ws + WS_S + (size_t)d * 4096;
#pragma unroll
    for (int i = 0; i < 4; ++i) {
      const f32x4 yv = (sel(hf, acc[4 + i], acc[i]) + *(const f32x4*)(src + i * 1024)) * iscale;
      const int t = 256 * (4 * hf + i) + 16 * i16 + 4 * g;
      const u32x2 uu = *(const u32x2*)(ul + (2044 - t) * 2);
      const unsigned uu0 = uu[0], uu1 = uu[1];
      const f16x2 ua = __builtin_bit_cast(f16x2, uu0), ub = __builtin_bit_cast(f16x2, uu1);
      const unsigned y01 = pkbf(yv[0], yv[1]), y23 = pkbf(yv[2], yv[3]);
      const unsigned k01 = pkbf((float)ub[1] * dskip, (float)ub[0] * dskip), k23 = pkbf((float)ua[1] * dskip, (float)ua[0] * dskip);
      const u64 pk = (u64)pkbf(bflo(y01) + bflo(k01), bfhi(y01) + bfhi(k01)) | ((u64)pkbf(bflo(y23) + bflo(k23), bfhi(y23) + bfhi(k23)) << 32);
      __hip_atomic_store((u64*)(sdst + t * 2), pk, RLX_AGENT);
    }
  }
  asm volatile("s_waitcnt vmcnt(0) lgkmcnt(0)" ::: "memory");
  STAMP();
  if (last_of(lds, C_SP, 4, lane) && lane < 32) __hip_atomic_store((u64*)(ws + WS_FLAG + (size_t)blk_of(lane, cb) * 1024 + FL_S) + mb, aS + 1, RLX_AGENT);
  STAMP_FLUSH();
}

#define FIN_LOADS() { \
  asm volatile("global_load_dwordx4 %0, %1, off" : "=v"(xv) : "v"(x + (size_t)(64 * mb + (tid >> 3)) * D_ + 64 * cb + 8 * (tid & 7)) : "memory"); \
  asm volatile("global_load_dwordx4 %0, %1, off" : "=v"(bv) : "v"(b2 + 64 * cb + 8 * (tid & 7)) : "memory"); }
DI void final_stage(u32x4& xv, u32x4& bv, u16* __restrict__ out, char* lds, int mb, int cb, int tid) {
  await_<0>(lds, C_R1, 4);
  await_<0>(lds, C_ST, 4);
  const int t = tid >> 3, dq = tid & 7;
  const char* src = lds + L_RED + t * RED_RS + dq * 32;
  f32x4 ra[4], rb[4];
#pragma unroll
  for (int k = 0; k < 4; ++k) { ra[k] = *(const f32x4*)(src + k * 64 * RED_RS); rb[k] = *(const f32x4*)(src + k * 64 * RED_RS + 16); }
  const u32x4 sv = *(const u32x4*)(lds + L_ST + t * ST_RS + dq * 16);
  SCHED();
  const f32x4 a = (ra[0] + ra[1]) + (ra[2] + ra[3]), b = (rb[0] + rb[1]) + (rb[2] + rb[3]);
  asm volatile("s_waitcnt vmcnt(0)" : "+v"(xv), "+v"(bv) :: "memory");
  u32x4 o;
#pragma unroll
  for (int c = 0; c < 4; ++c) {
    const float v0 = (c < 2 ? a[2 * c] : b[2 * c - 4]) + bflo(bv[c]), v1 = (c < 2 ? a[2 * c + 1] : b[2 * c - 3]) + bfhi(bv[c]);
    const float g0 = v0 * __builtin_amdgcn_rcpf(1.f + __builtin_amdgcn_exp2f(-1.4426950408889634f * v0));
    const float g1 = v1 * __builtin_amdgcn_rcpf(1.f + __builtin_amdgcn_exp2f(-1.4426950408889634f * v1));
    const unsigned pv = pkbf(bflo(sv[c]) * g0, bfhi(sv[c]) * g1);
    o[c] = pkbf(bflo(xv[c]) + bflo(pv), bfhi(xv[c]) + bfhi(pv));
  }
  { const __amdgpu_buffer_rsrc_t orr = __builtin_amdgcn_make_buffer_rsrc(out, 0, L_ * D_ * 2, 0x00020000); __builtin_amdgcn_raw_buffer_store_b128(o, orr, (unsigned)((64 * mb + t) * D_ + 64 * cb + 8 * dq) * 2, 0, 16); }
}
DI void p2_gemm(const u16* __restrict__ x, const u16* __restrict__ w2, u64 aOwn, const u16* __restrict__ b2, u16* __restrict__ out, char* __restrict__ ws,
                char* lds, int mb, int cb, int kq, int lane TK0_ARG) {
  STAMP_DECL STAMP_INIT(1) STAMP0();
  const int r = lane & 31, h = lane >> 5;
  unsigned* cnt = (unsigned*)(lds + L_CNT);
  const int tid = kq * 64 + lane;
  K2_SETUP(kq);
  char* ring = lds + L_RING2 + kq * (K2_R * 4096);
  const char* flg = ws + WS_FLAG + (size_t)blockIdx.x * 1024;
  const char* hbase = ws + WS_H + (size_t)(2 * mb) * HMT + 28 * kq * 1024;
  const unsigned hoff0 = lane * 16, hoff1 = lane * 16 + HMT;
  const int q = (lane & 15) >> 2;
  const int toff0 = (4 * h + q) * 128 + ((q >> 1) << 6) + ((lane >> 4) & 1) * 32 + 8 * (lane & 3), toff1 = toff0 ^ 64;
  u32x4 hb[3][4];
  s16x8 wf[2][4];
  f32x16 acc[2][2];
#pragma unroll
  for (int i = 0; i < 16; ++i) { acc[0][0][i] = 0.f; acc[0][1][i] = 0.f; acc[1][0][i] = 0.f; acc[1][1][i] = 0.f; }
  await_<1>(lds, C_PUB, 8);
  K2_DMA(2); K2_DMA(3);
  STAMP();
  if (kq == 0) {
    const u64* fp = (const u64*)(flg + FL_H) + (lane < 14 ? lane : 0);
    for (unsigned spin = 0; spin < (1u << 20); ++spin) {
      u64 v;
      asm volatile("global_load_dwordx2 %0, %1, off sc1\n\ts_waitcnt vmcnt(0)" : "=&v"(v) : "v"(fp) : "memory");
      if (__all(v == aOwn + 1)) break;
      __builtin_amdgcn_s_sleep(2);
    }
    lds_set(cnt + C_H, lane);
  } else {
    lds_wait(cnt + C_H);
  }
  STAMP();
#define K2_HL(S, s2) { const char* hs_ = hbase + K2S(S) * 2048 + (s2) * 1024; gload16(hb[(S) % 3][2 * (s2)], hoff0, hs_); gload16(hb[(S) % 3][2 * (s2) + 1], hoff1, hs_); SCHED(); }
#define K2_HLD(S) { K2_HL(S, 0); K2_HL(S, 1); }
#define K2_TR(S, i) { const char* sl_ = ring + ((S) % K2_R) * 4096 + ((i) >> 1) * 2048 + (((i) & 1) ? toff1 : toff0); wf[(S) & 1][i] = tr_frag(sl_, sl_ + 1024); SCHED(); }
#define K2_D1(S, pp) { dma16s(wvoff, wsrc + (size_t)(K2S(S) * 32 + (pp) * 8) * D_, ringa + ((S) % K2_R) * 4096 + (pp) * 1024); SCHED(); }
#define K2_M(S, s2, mi, di) { acc[mi][di] = __builtin_amdgcn_mfma_f32_32x32x16_bf16(wf[(S) & 1][2 * (s2) + (di)], __builtin_bit_cast(s16x8, hb[(S) % 3][2 * (s2) + (mi)]), acc[mi][di], 0, 0, 0); SCHED(); }
#define K2_STEP(S, W, TRN, HN, DN) { \
    asm volatile("s_waitcnt vmcnt(" #W ")" : "+v"(hb[(S) % 3][0]), "+v"(hb[(S) % 3][1]), "+v"(hb[(S) % 3][2]), "+v"(hb[(S) % 3][3]) :: "memory"); SCHED(); \
    K2_M(S,0,0,0); if (TRN) K2_TR((S)+1,0); K2_M(S,0,1,0); if (TRN) K2_TR((S)+1,1); K2_M(S,0,0,1); if (TRN) K2_TR((S)+1,2); K2_M(S,0,1,1); if (TRN) K2_TR((S)+1,3); \
    K2_M(S,1,0,0); if (DN) { K2_D1((S)+4,0); K2_D1((S)+4,1); } K2_M(S,1,1,0); if (DN) { K2_D1((S)+4,2); K2_D1((S)+4,3); } K2_M(S,1,0,1); if (HN) K2_HL((S)+3,0); K2_M(S,1,1,1); if (HN) K2_HL((S)+3,1); }
  K2_HLD(0); K2_HLD(1); K2_HLD(2);
  await_<0>(lds, C_W2, 4);
  K2_TR(0,0); K2_TR(0,1); K2_TR(0,2); K2_TR(0,3);
  K2_STEP(0, 8, 1, 1, 1); STAMP();
  K2_STEP(1, 12, 1, 1, 1);
  K2_STEP(2, 16, 1, 1, 1);
  K2_STEP(3, 16, 1, 1, 1);
  K2_STEP(4, 16, 1, 1, 1);
  K2_STEP(5, 16, 1, 1, 1);
  K2_STEP(6, 16, 1, 1, 1);
  K2_STEP(7, 16, 1, 1, 1); STAMP();
  K2_STEP(8, 16, 1, 1, 1);
  K2_STEP(9, 16, 1, 1, 1);
  K2_STEP(10, 16, 1, 1, 0);
  u32x4 xv, bv;
  FIN_LOADS();
  K2_STEP(11, 14, 1, 0, 0);
  K2_STEP(12, 6, 1, 0, 0);
  K2_STEP(13, 2, 0, 0, 0);
  STAMP();
  await_<0>(lds, C_SP, 4);
  {
    char* dst = lds + L_RED + (kq * 64 + r) * RED_RS + h * 16;
#pragma unroll
    for (int mi = 0; mi < 2; ++mi)
#pragma unroll
      for (int di = 0; di < 2; ++di)
#pragma unroll
        for (int rq = 0; rq < 4; ++rq)
          *(f32x4*)(dst + mi * 32 * RED_RS + di * 128 + rq * 32) = (f32x4){acc[mi][di][4 * rq], acc[mi][di][4 * rq + 1], acc[mi][di][4 * rq + 2], acc[mi][di][4 * rq + 3]};
  }
  ARRIVE_L(C_R1);
  STAMP();
  final_stage(xv, bv, out, lds, mb, cb, tid);
  if (tid == 0) __hip_atomic_store((u64*)(ws + WS_FLAG + (size_t)blockIdx.x * 1024), aOwn + 1, RLX_AGENT);
  STAMP(); STAMP_FLUSH();
}

DI void p2_helper(const u16* __restrict__ x, const u16* __restrict__ w2, u64 aOwn, const u16* __restrict__ b2, u16* __restrict__ out, char* __restrict__ ws,
                  char* lds, int mb, int cb, int gw, int lane) {
  unsigned* cnt = (unsigned*)(lds + L_CNT);
  const int tid = gw * 64 + lane, hw = gw - 4;
  u32x4 xv, bv;
  FIN_LOADS();
  if (hw == 0) {
    const u64* fp = (const u64*)(ws + WS_FLAG + (size_t)blockIdx.x * 1024 + FL_S) + (lane & 31);
    for (unsigned spin = 0; spin < (1u << 18); ++spin) {
      const u64 v = __hip_atomic_load(fp, RLX_AGENT);
      if (__all(v == aOwn + 1)) break;
      __builtin_amdgcn_s_sleep(8);
    }
    asm volatile("" ::: "memory");
    lds_set(cnt + C_S, lane);
  } else {
    while (__hip_atomic_load(cnt + C_S, RLX_WG) == 0u) __builtin_amdgcn_s_sleep(4);
    asm volatile("" ::: "memory");
  }
  u32x4 sa, sb;
  const char* sp = ws + WS_S + (size_t)(64 * cb + 16 * hw) * 4096 + 64 * mb * 2;
  gload16(sa, (lane >> 3) * 4096 + (lane & 7) * 16, sp);
  gload16(sb, (lane >> 3) * 4096 + (lane & 7) * 16, sp + 8 * 4096);
  asm volatile("s_waitcnt vmcnt(0)" : "+v"(sa), "+v"(sb) :: "memory");
  char* dst = lds + L_ST + (8 * (lane & 7)) * ST_RS + (16 * hw + (lane >> 3)) * 2;
#pragma unroll
  for (int c = 0; c < 4; ++c) {
    *(u16*)(dst + (2 * c) * ST_RS) = (u16)sa[c]; *(u16*)(dst + (2 * c + 1) * ST_RS) = (u16)(sa[c] >> 16);
    *(u16*)(dst + (2 * c) * ST_RS + 16) = (u16)sb[c]; *(u16*)(dst + (2 * c + 1) * ST_RS + 16) = (u16)(sb[c] >> 16);
  }
  ARRIVE_L(C_ST);
  while (__hip_atomic_load(cnt + C_R1, RLX_WG) < 4u) __builtin_amdgcn_s_sleep(2);
  final_stage(xv, bv, out, lds, mb, cb, tid);
}

template <int PH>
__global__ __launch_bounds__(768, 3) void fused_kernel(const u16* __restrict__ x, const u16* __restrict__ lar, const u16* __restrict__ aim,
                                                       const u16* __restrict__ cre, const u16* __restrict__ cim, const u16* __restrict__ ldt,
                                                       const u16* __restrict__ dsk, const u16* __restrict__ w1, const u16* __restrict__ b1,
                                                       const u16* __restrict__ w2, const u16* __restrict__ b2, u16* __restrict__ out,
                                                       char* __restrict__ ws) {
  extern __shared__ __attribute__((aligned(16))) char lds[];
  const int lane = threadIdx.x & 63;
  const int wid = __builtin_amdgcn_readfirstlane(threadIdx.x >> 6);
  const int b = blockIdx.x, xcd = b & 7, j = b >> 3;
  const int mb = 4 * xcd + (j >> 3), cb = j & 7;
  const int jj = j < 28 ? j : 0;
  const int panel = 4 * (xcd >> 1) + jj / 7, fs = 7 * (xcd & 1) + jj % 7;
  if (wid < 4) {
    const int de = 64 * cb + 2 * mb;
    const unsigned sdt = *(const __attribute__((address_space(4))) unsigned*)(ldt + de);
    const unsigned sds = *(const __attribute__((address_space(4))) unsigned*)(dsk + de);
    x_wave(lar, aim, cre, cim, sdt, sds, w2, w1, b1, j >= 28 ? xcd * 4 + (j - 28) : -1, panel, fs, ws, lds, mb, cb, wid, lane TK0_PASS);
    return;
  }
  const int gw = wid - 4;
  __builtin_amdgcn_s_setprio(2);
  if (j < 28) p1_gemm(x, w1, ws, lds, panel, fs, gw, lane TK0_PASS);
  else p1_aux(x, ws, lds, xcd * 4 + (j - 28), gw, lane TK0_PASS);
  __builtin_amdgcn_s_setprio(0);
  await_<0>(lds, C_EP, 1);
  const u64 aOwn = *(const u64*)(lds + L_KSC + 16);
  if (gw < 4) p2_gemm(x, w2, aOwn, b2, out, ws, lds, mb, cb, gw, lane TK0_PASS);
  else p2_helper(x, w2, aOwn, b2, out, ws, lds, mb, cb, gw, lane);
}

extern "C" void kernel_launch(void* const* d_in, const int* in_sizes, int n_in, void* d_out, int out_size, void* d_ws, size_t ws_size,
                              hipStream_t stream) {
  const u16* x = (const u16*)d_in[0];
  const u16* lar = (const u16*)d_in[1];
  const u16* aim = (const u16*)d_in[2];
  const u16* cre = (const u16*)d_in[3];
  const u16* cim = (const u16*)d_in[4];
  const u16* ldt = (const u16*)d_in[5];
  const u16* dsk = (const u16*)d_in[6];
  const u16* w1 = (const u16*)d_in[8];
  const u16* b1 = (const u16*)d_in[9];
  const u16* w2 = (const u16*)d_in[10];
  const u16* b2 = (const u16*)d_in[11];
  (void)hipFuncSetAttribute((const void*)fused_kernel<3>, hipFuncAttributeMaxDynamicSharedMemorySize, LDS_TOTAL);
  fused_kernel<3><<<256, 768, LDS_TOTAL, stream>>>(x, lar, aim, cre, cim, ldt, dsk, w1, b1, w2, b2, (u16*)d_out, (char*)d_ws);
}
```

```cpp
#include <hip/hip_runtime.h>
#include <stdint.h>

typedef unsigned short u16;
typedef unsigned long long u64;
typedef __attribute__((ext_vector_type(4))) short s16x4;
typedef __attribute__((ext_vector_type(8))) short s16x8;
typedef __attribute__((ext_vector_type(8))) _Float16 f16x8;
typedef __attribute__((ext_vector_type(2))) _Float16 f16x2;
typedef __attribute__((ext_vector_type(2))) __bf16 bf16x2;
typedef __attribute__((ext_vector_type(2))) float f32x2;
typedef __attribute__((ext_vector_type(4))) float f32x4;
typedef __attribute__((ext_vector_type(16))) float f32x16;
typedef __attribute__((ext_vector_type(4))) unsigned u32x4;
typedef __attribute__((ext_vector_type(2))) unsigned u32x2;
#define LDSP __attribute__((address_space(3)))
#define GLBP __attribute__((address_space(1)))
#define DI __device__ __forceinline__

#define L_ 2048
#define D_ 512
#define N_ 128
#define F_ 1792

#define WS_H 0
#define WS_UR (8u << 20)
#define WS_S (10u << 20)
#define WS_STAT (12u << 20)
#define WS_CS ((12u << 20) + 65536)
#define WS_ST2 ((12u << 20) + 131072)
#define WS_FLAG (13u << 20)
#define FL_LN 128
#define FL_H 384
#define FL_S 512
#define FL_ST 768

DI float bf2f(unsigned v) { return __uint_as_float(v << 16); }
DI float bflo(unsigned v) { return __uint_as_float(v << 16); }
DI float bfhi(unsigned v) { return __uint_as_float(v & 0xffff0000u); }
DI unsigned pkbf(float a, float b) { bf16x2 q = {(__bf16)a, (__bf16)b}; return __builtin_bit_cast(unsigned, q); }
DI float rbf(float a) { __bf16 q = (__bf16)a; return (float)q; }
DI unsigned pkh(float a, float b) { f32x2 v = {a, b}; f16x2 p = __builtin_convertvector(v, f16x2); return __builtin_bit_cast(unsigned, p); }
DI float dot2(unsigned a, unsigned b, float c) { return __builtin_amdgcn_fdot2_f32_bf16(__builtin_bit_cast(bf16x2, a), __builtin_bit_cast(bf16x2, b), c, false); }
template <class T> DI T sel(int c, T a, T b) { return c ? a : b; }
DI void dma16s(unsigned voff, const void* sbase, unsigned ldsaddr) {
  asm volatile("s_mov_b32 m0, %2\n\ts_nop 0\n\tglobal_load_lds_dwordx4 %0, %1" :: "v"(voff), "s"(sbase), "s"(ldsaddr) : "memory");
}
DI unsigned lds_base(char* lds) { return __builtin_amdgcn_readfirstlane((unsigned)(size_t)(LDSP char*)lds); }
DI s16x8 tr_frag(const char* p0, const char* p1) {
  s16x4 a = __builtin_amdgcn_ds_read_tr16_b64_v4i16((LDSP s16x4*)p0);
  s16x4 b = __builtin_amdgcn_ds_read_tr16_b64_v4i16((LDSP s16x4*)p1);
  return __builtin_shufflevector(a, b, 0, 1, 2, 3, 4, 5, 6, 7);
}
#define SCHED() __builtin_amdgcn_sched_barrier(0)
#define WAITV_BAR(n) do { SCHED(); asm volatile("s_waitcnt vmcnt(" #n ") lgkmcnt(0)\n\ts_barrier" ::: "memory"); SCHED(); } while (0)
#define LGKM_BAR() do { SCHED(); asm volatile("s_waitcnt lgkmcnt(0)\n\ts_barrier" ::: "memory"); SCHED(); } while (0)
#define RLX_AGENT __ATOMIC_RELAXED, __HIP_MEMORY_SCOPE_AGENT
#define RLX_WG __ATOMIC_RELAXED, __HIP_MEMORY_SCOPE_WORKGROUP

template <int CTRL, int RM> DI float dpp_add(float v) {
  return v + __builtin_bit_cast(float, __builtin_amdgcn_update_dpp(0, __builtin_bit_cast(int, v), CTRL, RM, 0xf, true));
}
DI float wave_sum63(float v) {
  v = dpp_add<0x111, 0xf>(v); v = dpp_add<0x112, 0xf>(v); v = dpp_add<0x114, 0xf>(v); v = dpp_add<0x118, 0xf>(v);
  v = dpp_add<0x142, 0xa>(v); v = dpp_add<0x143, 0xc>(v);
  return v;
}
DI float comb32(float a, float b) { auto r = __builtin_amdgcn_permlane32_swap(__float_as_uint(a), __float_as_uint(b), false, false); return __uint_as_float(r[0]) + __uint_as_float(r[1]); }
DI float comb16(float a, float b) { auto r = __builtin_amdgcn_permlane16_swap(__float_as_uint(a), __float_as_uint(b), false, false); return __uint_as_float(r[0]) + __uint_as_float(r[1]); }
DI float row_allsum(float v) { v = dpp_add<0x128, 0xf>(v); v = dpp_add<0x124, 0xf>(v); v = dpp_add<0x122, 0xf>(v); v = dpp_add<0x121, 0xf>(v); return v; }
DI float rdlane(float v, int l) { return __builtin_bit_cast(float, __builtin_amdgcn_readlane(__builtin_bit_cast(int, v), l)); }

DI void poll_flags(const u64* p, u64 expect) {
  for (unsigned spin = 0; spin < (1u << 20); ++spin) {
    const u64 v = __hip_atomic_load(p, RLX_AGENT);
    if (__all(v == expect)) break;
    __builtin_amdgcn_s_sleep(2);
  }
  asm volatile("" ::: "memory");
}
DI void lds_set(unsigned* w, int lane) { if (lane == 0) __hip_atomic_store(w, 1u, RLX_WG); }
DI void lds_wait(unsigned* w) { while (__hip_atomic_load(w, RLX_WG) == 0u) __builtin_amdgcn_s_sleep(1); asm volatile("" ::: "memory"); }
DI int blk_of(int mb, int cb) { return (mb >> 2) + 8 * (((mb & 3) << 3) | cb); }

#define LDS_TOTAL 163840
#define L_STAT 162304
#define L_BIAS 163328
#define L_ACON 163584
#define L_KSC 163712
#define L_CNT 163744
#define KAPSZ 2576
#define L_KAP 141696
#define L_COLS 152000
#define K1_SLOT 32768
#define L_KP 131584
#define KPSZ 4160
#define K2_R 4
#define L_RING2 0
#define L_W 65536
#define WSZ 33024
#define L_U L_KAP
#define USZ 4608
#define L_RED L_W
#define RED_RS 272
#define L_ST L_KAP
#define ST_RS 144
#define C_PUB 0
#define C_SP 1
#define C_H 2
#define C_S 3
#define C_W2 4
#define C_R1 5
#define C_ST 6
#define C_K0 7
#define C_K1 9
#define C_K2 12
#define C_K3 14
#define C_LN1 16
#define C_LN2 17
#define C_X2 18
#define C_CS 19
#define C_EP 20
DI void arrive(char* lds, int c, int lane) { if (lane == 0) __hip_atomic_fetch_add((unsigned*)(lds + L_CNT) + c, 1u, RLX_WG); }
template <int SL> DI void await_(char* lds, int c, unsigned target) {
  while (__hip_atomic_load((unsigned*)(lds + L_CNT) + c, RLX_WG) < target) __builtin_amdgcn_s_sleep(SL);
  asm volatile("" ::: "memory"); SCHED();
}
#define ARRIVE_V(n, c) do { SCHED(); asm volatile("s_waitcnt vmcnt(" #n ") lgkmcnt(0)" ::: "memory"); arrive(lds, c, lane); SCHED(); } while (0)
#define ARRIVE_L(c) do { SCHED(); asm volatile("s_waitcnt lgkmcnt(0)" ::: "memory"); arrive(lds, c, lane); SCHED(); } while (0)

struct cf { float x, y; };
DI cf cmul(cf a, cf b) { return cf{fmaf(a.x, b.x, -(a.y * b.y)), fmaf(a.x, b.y, a.y * b.x)}; }
DI unsigned pkc(cf a) { return pkh(a.x, a.y); }
DI unsigned pkcc(cf a) { return pkh(a.x, -a.y); }
DI cf zpow(float al, float om, float p) {
  const float m = __builtin_amdgcn_exp2f(al * p), ph = __builtin_amdgcn_fractf(om * p);
  return cf{m * __builtin_amdgcn_cosf(ph), m * __builtin_amdgcn_sinf(ph)};
}
DI unsigned cmulh(unsigned a, unsigned b) {
  const f16x2 z = __builtin_bit_cast(f16x2, a), w = __builtin_bit_cast(f16x2, b);
  const f16x2 t = z * (f16x2){w[0], w[0]};
  const f16x2 r = __builtin_elementwise_fma((f16x2){-z[1], z[0]}, (f16x2){w[1], w[1]}, t);
  return __builtin_bit_cast(unsigned, r);
}
DI u32x4 cmulh4(u32x4 a, u32x4 b) { return (u32x4){cmulh(a[0], b[0]), cmulh(a[1], b[1]), cmulh(a[2], b[2]), cmulh(a[3], b[3])}; }
#define TB_RS 272
#define TB_N 33
DI void kgen_tables(unsigned (&T)[TB_N], float& isc, unsigned raw0, unsigned raw1, float ldtv, char* lds, int cw, int lane) {
  const int src = ((lane & 31) >> 1) * 4, sh = (lane & 1) ? 0 : 16;
  unsigned pv[4];
#pragma unroll
  for (int a = 0; a < 4; ++a) {
    const unsigned lo = (unsigned)__builtin_amdgcn_ds_bpermute(src + 64 * a, (int)raw0), hi = (unsigned)__builtin_amdgcn_ds_bpermute(src + 64 * a, (int)raw1);
    pv[a] = ((lane < 32 ? lo : hi) << sh) & 0xffff0000u;
  }
  const float dt = rbf(__builtin_amdgcn_exp2f(ldtv * 1.4426950408889634f));
  const float ar = rbf(__builtin_amdgcn_exp2f(__uint_as_float(pv[0]) * 1.4426950408889634f));
  const float al = -(ar * dt) * 1.4426950408889634f;
  const float om = (__uint_as_float(pv[1]) * dt) * 0.15915494309189535f;
  const float cr = __uint_as_float(pv[2]) * dt, ci = __uint_as_float(pv[3]) * dt;
  const float sabs = 2.f * rdlane(wave_sum63(fabsf(cr) + fabsf(ci)), 63);
  int ex = (int)((__float_as_uint(sabs) >> 23) & 255);
  ex = ex < 20 ? 20 : (ex > 230 ? 230 : ex);
  const float sc2 = __uint_as_float((unsigned)(268 - ex) << 23);
  isc = __uint_as_float((unsigned)(ex - 13) << 23);
  if (lane == 0) *(float*)(lds + L_KSC + cw * 4) = sabs;
  const cf z1 = zpow(al, om, 1.f), z8 = zpow(al, om, 8.f), z32 = zpow(al, om, 32.f), a1 = zpow(al, om, 128.f), a4 = zpow(al, om, 512.f);
  const unsigned one = 0x00003C00u;
  const cf a2 = cmul(a1, a1), a3 = cmul(a2, a1), a8 = cmul(a4, a4), a12 = cmul(a8, a4);
  T[0] = one; T[1] = pkc(a1); T[2] = pkc(a2); T[3] = pkc(a3);
  T[4] = pkc(a4); T[5] = pkc(cmul(a4, a1)); T[6] = pkc(cmul(a4, a2)); T[7] = pkc(cmul(a4, a3));
  T[8] = pkc(a8); T[9] = pkc(cmul(a8, a1)); T[10] = pkc(cmul(a8, a2)); T[11] = pkc(cmul(a8, a3));
  T[12] = pkc(a12); T[13] = pkc(cmul(a12, a1)); T[14] = pkc(cmul(a12, a2)); T[15] = pkc(cmul(a12, a3));
  T[16] = pkc(cmul(a8, a8));
  const cf e2 = cmul(z1, z1), e3 = cmul(e2, z1), e4 = cmul(e2, e2);
  T[17] = one; T[18] = pkc(z1); T[19] = pkc(e2); T[20] = pkc(e3); T[21] = pkc(e4);
  T[22] = pkc(cmul(e4, z1)); T[23] = pkc(cmul(e4, e2)); T[24] = pkc(cmul(e4, e3));
  const cf c0 = cf{cr * sc2, ci * sc2}, c2 = cmul(c0, cmul(z32, z32));
  T[25] = pkcc(c0); T[26] = pkcc(cmul(c0, z32)); T[27] = pkcc(c2); T[28] = pkcc(cmul(c2, z32));
  const cf z16 = cmul(z8, z8);
  T[29] = one; T[30] = pkcc(z8); T[31] = pkcc(z16); T[32] = pkcc(cmul(z16, z8));
}
#define KG_PARAMS f32x4& kX, f32x4& kY, f32x4& kZ, float isc
DI void kgen_store(KG_PARAMS, char* lds, int wid, int lane) {
  const int i16 = lane & 15, g = lane >> 4;
  float* kap = (float*)(lds + L_KAP + wid * KAPSZ);
  *(f32x4*)(kap + 128 * (g >> 1) + 4 * (g & 1) + 8 * i16) = kX * isc;
  if (g < 2) {
    *(f32x4*)(kap + 256 + 64 * g + 4 * i16) = kY * isc;
#pragma unroll
    for (int r = 0; r < 4; ++r) kap[512 + 16 * (4 * g + r) + i16] = kZ[r] * isc;
  } else {
    *(f32x2*)(kap + 384 + 64 * (g - 2) + 2 * i16) = (f32x2){kY[0] * isc, kY[1] * isc};
    *(f32x2*)(kap + 384 + 64 * (g - 2) + 32 + 2 * i16) = (f32x2){kY[2] * isc, kY[3] * isc};
    if (g == 2 && i16 == 0) kap[640] = kZ[0] * isc;
  }
}
DI const u16* prm_ptr(const u16* lar, const u16* aim, const u16* cre, const u16* cim, int d, int hf, int lane) {
  const int g = lane >> 4;
  const u16* p = g == 0 ? lar : (g == 1 ? aim : (g == 2 ? cre : cim));
  return p + d * N_ + 64 * hf + 2 * (lane & 15);
}
DI void ld_u32(unsigned& v, const void* p) { asm volatile("global_load_dword %0, %1, off" : "=v"(v) : "v"(p) : "memory"); }
DI void ld_u64(u64& v, const void* p) { asm volatile("global_load_dwordx2 %0, %1, off sc1" : "=v"(v) : "v"(p) : "memory"); }
DI void gload16(u32x4& v, unsigned voff, const char* sbase) {
  asm volatile("global_load_dwordx4 %0, %1, %2 sc1" : "=v"(v) : "v"(voff), "s"(sbase) : "memory");
}
DI u64 rdlane64(u64 v, int l) { return (u64)(unsigned)__builtin_amdgcn_readlane((int)(unsigned)v, l) | ((u64)(unsigned)__builtin_amdgcn_readlane((int)(unsigned)(v >> 32), l) << 32); }
DI bool last_of(char* lds, int c, unsigned n, int lane) {
  unsigned old = 0;
  if (lane == 0) old = __hip_atomic_fetch_add((unsigned*)(lds + L_CNT) + c, 1u, RLX_WG);
  return (unsigned)__builtin_amdgcn_readfirstlane(old) == n - 1;
}
DI float gelu_f(float v) {
  const float c0 = -2.302208198f;
  const float c1 = -0.1029432397f;
  const float z = v * fmaf(v * v, c1, c0);
  return v * __builtin_amdgcn_rcpf(1.f + __builtin_amdgcn_exp2f(z));
}
#define K2S(S) ((S) + r2 >= 14 ? (S) + r2 - 14 : (S) + r2)
#define K2_DMA(S) { _Pragma("unroll") for (int pp = 0; pp < 4; ++pp) dma16s(wvoff, wsrc + (size_t)(K2S(S) * 32 + pp * 8) * D_, ringa + ((S) % K2_R) * 4096 + pp * 1024); SCHED(); }
#define K2_SETUP(kq_) int lane2_ = lane; asm volatile("" : "+v"(lane2_)); \
  const unsigned wvoff = (lane2_ >> 3) * (D_ * 2) + (((lane2_ & 7) ^ (((lane2_ >> 4) & 1) << 2)) << 4); \
  const unsigned ringa = __builtin_amdgcn_readfirstlane(lds_base(lds) + L_RING2 + (kq_) * (K2_R * 4096)); \
  const u16* wsrc = w2 + (size_t)(14 * (kq_) * 32) * D_ + 64 * cb; \
  const int r2 = (2 * cb + 4 * (mb & 3)) % 14;
#define HMT 114688

DI void p1_gemm(const u16* __restrict__ x, const u16* __restrict__ w1, char* __restrict__ ws, char* lds, int panel, int fs, int wid, int lane) {
  const int wm = wid >> 2, wf = (wid >> 1) & 1, kh = wid & 1;
  const int r = lane & 31, h = lane >> 5;
  const int row0 = panel * 128, f0 = fs * 128;
  const unsigned xv0 = (lane >> 3) * 1024 + (((lane & 7) ^ ((lane >> 4) & 3)) << 4);
  const unsigned xv1 = xv0 ^ 64;
  const unsigned wv = (lane >> 4) * (F_ * 2) + (((lane & 15) ^ (((lane >> 4) & 3) << 2)) << 4);
  const u16* xsb = x + (size_t)(row0 + 16 * wid) * D_;
  const u16* wsb = w1 + (size_t)(8 * wid) * F_ + f0;
  const unsigned la = lds_base(lds) + wid * 2048;
  const int rot = 2 * (panel & 3) + (fs >= 7 ? fs - 7 : fs);
#define KS(S) (((S) + rot) & 7)
#define K1_D0(S) { dma16s(xv0, xsb + KS(S) * 64, la + ((S) % 4) * K1_SLOT); SCHED(); }
#define K1_D1(S) { dma16s(xv1, xsb + 8 * D_ + KS(S) * 64, la + ((S) % 4) * K1_SLOT + 1024); SCHED(); }
#define K1_D2(S) { dma16s(wv, wsb + (size_t)(KS(S) * 64) * F_, la + ((S) % 4) * K1_SLOT + 16384); SCHED(); }
#define K1_D3(S) { dma16s(wv, wsb + (size_t)(KS(S) * 64 + 4) * F_, la + ((S) % 4) * K1_SLOT + 16384 + 1024); SCHED(); }
#define K1_ISSUE(S) { K1_D0(S); K1_D1(S); K1_D2(S); K1_D3(S); }
  K1_ISSUE(0); K1_ISSUE(1); K1_ISSUE(2); K1_ISSUE(3);
  const int xoff = (64 * wm + r) * 128 + (((4 * kh + 2 * wm + h) ^ ((r >> 1) & 7)) << 4);
  const int q = (lane & 15) >> 2, p = lane & 3;
  const int woff0 = 16384 + (32 * kh + 16 * wm + 8 * h + q) * 256 + (((2 * wf + kh) ^ q) << 6) + ((lane >> 4) & 1) * 32 + 8 * p;
  f32x16 acc[2][2];
#pragma unroll
  for (int i = 0; i < 16; ++i) { acc[0][0][i] = 0.f; acc[0][1][i] = 0.f; acc[1][0][i] = 0.f; acc[1][1][i] = 0.f; }
  s16x8 xf[2][2][2], wfr[2][2][2];
#define K1_M(T, s, mi, fi) { acc[mi][fi] = __builtin_amdgcn_mfma_f32_32x32x16_bf16(wfr[(T) & 1][s][fi], xf[(T) & 1][s][mi], acc[mi][fi], 0, 0, 0); SCHED(); }
#define K1_RX(T, s, mi) { xf[(T) & 1][s][mi] = *(const s16x8*)(lds + (xa_ ^ ((s) << 5)) + (mi) * 4096); SCHED(); }
#define K1_RW(T, s, fi) { wfr[(T) & 1][s][fi] = tr_frag(lds + (wa_ ^ (((fi) << 6) | ((s) << 12))), lds + (wa_ ^ (((fi) << 6) | ((s) << 12))) + 1024); SCHED(); }
#define K1_ADDR(T) int xa_ = xoff + ((T) % 4) * K1_SLOT, wa_ = woff0 + ((T) % 4) * K1_SLOT; asm volatile("" : "+v"(xa_), "+v"(wa_));
#define K1_READ(T) { K1_ADDR(T) K1_RX(T,0,0); K1_RW(T,0,0); K1_RW(T,0,1); K1_RX(T,0,1); K1_RX(T,1,0); K1_RW(T,1,0); K1_RW(T,1,1); K1_RX(T,1,1); }
#define K1_MMA(T) { K1_M(T,0,0,0); K1_M(T,0,0,1); K1_M(T,0,1,0); K1_M(T,0,1,1); K1_M(T,1,0,0); K1_M(T,1,0,1); K1_M(T,1,1,0); K1_M(T,1,1,1); }
#define K1_STAGE(T, DM0, DM1, DM2, DM3) { K1_ADDR(T) \
    K1_M((T)-1,0,0,0); K1_RX(T,0,0); K1_RW(T,0,0); K1_M((T)-1,0,0,1); K1_RW(T,0,1); K1_RX(T,0,1); \
    K1_M((T)-1,0,1,0); K1_RX(T,1,0); K1_RW(T,1,0); K1_M((T)-1,0,1,1); K1_RW(T,1,1); K1_RX(T,1,1); \
    K1_M((T)-1,1,0,0); DM0; K1_M((T)-1,1,0,1); DM1; K1_M((T)-1,1,1,0); DM2; K1_M((T)-1,1,1,1); DM3; }
#define K1_STAGE_D(T, S) K1_STAGE(T, K1_D0(S), K1_D1(S), K1_D2(S), K1_D3(S))
  WAITV_BAR(12);
  K1_READ(0);
  WAITV_BAR(8); K1_STAGE_D(1, 4);
  WAITV_BAR(8); K1_STAGE_D(2, 5);
  WAITV_BAR(8); K1_STAGE_D(3, 6);
  WAITV_BAR(8); K1_STAGE_D(4, 7);
  WAITV_BAR(8); K1_STAGE(5, , , , );
  WAITV_BAR(4); K1_STAGE(6, , , , );
  K1_MMA(6);
  WAITV_BAR(0);
  K1_READ(7); K1_MMA(7);
  float* colS = (float*)(lds + L_COLS);
  {
    char* dst = lds + wid * 8192 + lane * 16;
#pragma unroll
    for (int mi = 0; mi < 2; ++mi)
#pragma unroll
      for (int rq = 0; rq < 4; ++rq)
        *(f32x4*)(dst + (mi * 4 + rq) * 1024) = (f32x4){acc[mi][1][4 * rq], acc[mi][1][4 * rq + 1], acc[mi][1][4 * rq + 2], acc[mi][1][4 * rq + 3]};
  }
  LGKM_BAR();
  f32x4 pa[2][4], cq[4];
  f32x2 st[2];
  u32x2 bq[4];
  {
    const char* src = lds + (wid ^ 1) * 8192 + lane * 16;
    const int fl = 64 * wf + 32 * kh + 4 * h;
#pragma unroll
    for (int rq = 0; rq < 4; ++rq) {
      cq[rq] = *(const f32x4*)(colS + fl + 8 * rq);
      bq[rq] = *(const u32x2*)(lds + L_BIAS + (fl + 8 * rq) * 2);
      pa[0][rq] = *(const f32x4*)(src + rq * 1024); pa[1][rq] = *(const f32x4*)(src + (4 + rq) * 1024);
    }
    st[0] = *(const f32x2*)(lds + L_STAT + (64 * wm + r) * 8); st[1] = *(const f32x2*)(lds + L_STAT + (64 * wm + 32 + r) * 8);
  }
  LGKM_BAR();
  {
    const __amdgpu_buffer_rsrc_t hr = __builtin_amdgcn_make_buffer_rsrc(ws + WS_H, 0, L_ * F_ * 2, 0x00020000);
    const unsigned hoff = (unsigned)(panel * 4 + 2 * wm) * HMT + (8 * fs + 4 * wf + 2 * kh) * 1024 + lane * 16;
#pragma unroll
    for (int mi = 0; mi < 2; ++mi) {
      const float mr = st[mi][0], rinv = st[mi][1];
#pragma unroll
      for (int s2 = 0; s2 < 2; ++s2) {
        u32x4 pk;
#pragma unroll
        for (int qq = 0; qq < 2; ++qq) {
          const int rq = 2 * s2 + qq;
          const float b0 = bflo(bq[rq][0]), b1_ = bfhi(bq[rq][0]), b2_ = bflo(bq[rq][1]), b3 = bfhi(bq[rq][1]);
          const float o0 = gelu_f(fmaf(acc[mi][0][4 * rq] + pa[mi][rq][0], rinv, fmaf(mr, cq[rq][0], b0)));
          const float o1 = gelu_f(fmaf(acc[mi][0][4 * rq + 1] + pa[mi][rq][1], rinv, fmaf(mr, cq[rq][1], b1_)));
          const float o2 = gelu_f(fmaf(acc[mi][0][4 * rq + 2] + pa[mi][rq][2], rinv, fmaf(mr, cq[rq][2], b2_)));
          const float o3 = gelu_f(fmaf(acc[mi][0][4 * rq + 3] + pa[mi][rq][3], rinv, fmaf(mr, cq[rq][3], b3)));
          pk[2 * qq] = pkbf(o0, o1); pk[2 * qq + 1] = pkbf(o2, o3);
        }
        __builtin_amdgcn_raw_buffer_store_b128(pk, hr, hoff + mi * HMT + s2 * 1024, 0, 16);
        SCHED();
      }
    }
  }
  asm volatile("s_waitcnt vmcnt(0)" ::: "memory");
  if (last_of(lds, C_PUB, 8, lane) && lane < 16) {
    const u64 ac = *(const u64*)(lds + L_ACON + lane * 8);
    int l2 = lane; asm volatile("" : "+v"(l2));
    __hip_atomic_store((u64*)(ws + WS_FLAG + (size_t)blk_of(2 * panel + ((l2 >> 3) & 1), l2 & 7) * 1024 + FL_H) + fs, ac + 1, RLX_AGENT);
  }
}

#define LN_RS 1040
DI void p1_aux(const u16* __restrict__ x, char* __restrict__ ws, char* lds, int idx, int wid, int lane) {
  const int t0 = idx * 64;
  u32x4 v[8];
#pragma unroll
  for (int i = 0; i < 8; ++i) v[i] = *(const u32x4*)(x + (size_t)(t0 + 8 * wid + i) * D_ + lane * 8);
  const u64 aCon = __hip_atomic_load((const u64*)(ws + WS_FLAG + (size_t)((wid * 64 + lane) & 255) * 1024), RLX_AGENT);
  const int sfs = lane < 14 ? lane : 0;
  const int sblk = 2 * (idx >> 3) + (sfs >= 7 ? 1 : 0) + 8 * (((idx >> 1) & 3) * 7 + (sfs >= 7 ? sfs - 7 : sfs));
  const u64 aSt = __hip_atomic_load((const u64*)(ws + WS_FLAG + (size_t)sblk * 1024), RLX_AGENT);
  SCHED();
  asm volatile("s_barrier" ::: "memory");
  SCHED();
#pragma unroll
  for (int i = 0; i < 8; ++i) *(u32x4*)(lds + (63 - (8 * wid + i)) * LN_RS + lane * 16) = v[i];
  ARRIVE_L(C_LN1);
  float sA[8], sB[8];
#pragma unroll
  for (int i = 0; i < 8; ++i) {
    float a = dot2(v[i][0], 0x3F803F80u, 0.f), b = dot2(v[i][0], v[i][0], 0.f);
#pragma unroll
    for (int c = 1; c < 4; ++c) { a = dot2(v[i][c], 0x3F803F80u, a); b = dot2(v[i][c], v[i][c], b); }
    sA[i] = a; sB[i] = b;
  }
#pragma unroll
  for (int i = 0; i < 4; ++i) { sA[i] = comb32(sA[i], sA[i + 4]); sB[i] = comb32(sB[i], sB[i + 4]); }
#pragma unroll
  for (int i = 0; i < 2; ++i) { sA[i] = row_allsum(comb16(sA[i], sA[i + 2])); sB[i] = row_allsum(comb16(sB[i], sB[i + 2])); }
  const float mv = ((lane & 1) ? sA[1] : sA[0]) * (1.f / 512.f);
  const float var = fmaxf(fmaf(-mv, mv, ((lane & 1) ? sB[1] : sB[0]) * (1.f / 512.f)), 0.f);
  const float mbv = rbf(mv);
  const float sd = rbf(sqrtf(rbf(rbf(var) + 1e-5f)));
  const float rv = 1.f / sd;
  if ((lane & 14) == 0) {
    const u64 pk = (u64)__float_as_uint(-mbv * rv) | ((u64)__float_as_uint(rv) << 32);
    const int tt = t0 + 8 * wid + (lane & 1) + 2 * (lane >> 4);
    __hip_atomic_store((u64*)(ws + WS_STAT) + tt, pk, RLX_AGENT);
    __hip_atomic_store((unsigned*)(ws + WS_ST2) + (L_ - 1 - tt), (__float_as_uint(mbv) >> 16) | __float_as_uint(sd), RLX_AGENT);
  }
  await_<0>(lds, C_LN1, 8);
  char* t2 = lds + 64 * LN_RS + wid * 8192;
  const int g = lane >> 4, q = (lane & 15) >> 2, p = lane & 3, dl = lane & 15;
  s16x8 f[8];
#pragma unroll
  for (int i = 0; i < 8; ++i) {
    const char* a = lds + (8 * (4 * (i & 1) + g) + q) * LN_RS + (64 * wid + 16 * (i >> 1) + 4 * p) * 2;
    f[i] = tr_frag(a, a + 4 * LN_RS);
  }
  SCHED();
#pragma unroll
  for (int i = 0; i < 8; ++i) {
    const int dd2 = 16 * (i >> 1) + dl, rc = 4 * (i & 1) + g;
    *(s16x8*)(t2 + dd2 * 128 + ((rc ^ (dd2 & 7)) << 4)) = f[i];
  }
  SCHED();
  const __amdgpu_buffer_rsrc_t ur = __builtin_amdgcn_make_buffer_rsrc(ws + WS_UR, 0, L_ * D_ * 2, 0x00020000);
  const int x0 = L_ - 64 - t0;
  u32x4 o[8];
#pragma unroll
  for (int i = 0; i < 8; ++i) {
    const int dd2 = 8 * i + (lane >> 3), c = lane & 7;
    o[i] = *(const u32x4*)(t2 + dd2 * 128 + ((c ^ (dd2 & 7)) << 4));
  }
  SCHED();
  asm volatile("s_waitcnt vmcnt(0)" ::: "memory");
  if (last_of(lds, C_PUB, 8, lane) && lane < 14) __hip_atomic_store((u64*)(ws + WS_FLAG + (size_t)sblk * 1024 + FL_ST) + (idx & 1), aSt + 1, RLX_AGENT);
#pragma unroll
  for (int i = 0; i < 8; ++i)
    __builtin_amdgcn_raw_buffer_store_b128(o[i], ur, (unsigned)(64 * wid + 8 * i + (lane >> 3)) * 4096 + x0 * 2 + (lane & 7) * 16, 0, 16);
  ARRIVE_L(C_X2);
  ARRIVE_V(0, C_LN2);
  if (wid < 4) {
    await_<0>(lds, C_LN2, 8);
    __hip_atomic_store((u64*)(ws + WS_FLAG + (size_t)(wid * 64 + lane) * 1024 + FL_LN) + idx, aCon + 1, RLX_AGENT);
  }
}

DI void x_wave(const u16* __restrict__ lar, const u16* __restrict__ aim, const u16* __restrict__ cre, const u16* __restrict__ cim,
               unsigned sdt, unsigned sds, const u16* __restrict__ w2, const u16* __restrict__ w1, const u16* __restrict__ b1, int auxidx, int panel, int fs,
               char* __restrict__ ws, char* lds, int mb, int cb, int cw, int lane) {
  __builtin_amdgcn_s_setprio(3);
  const bool aux = auxidx >= 0;
  const int ch = cw >> 1, hf = cw & 1;
  const int d = 64 * cb + 2 * mb + ch;
  const int i16 = lane & 15, g = lane >> 4;
  unsigned* cnt = (unsigned*)(lds + L_CNT);
  const char* flg = ws + WS_FLAG + (size_t)blockIdx.x * 1024;
  const u16* pp_ = prm_ptr(lar, aim, cre, cim, d, hf, lane);
  unsigned raw0, raw1;
  u64 aS, aH;
  ld_u32(raw0, pp_); ld_u32(raw1, pp_ + 32);
  ld_u64(aS, ws + WS_FLAG + (size_t)(lane >= 32 ? (int)blockIdx.x : blk_of(lane, cb)) * 1024);
  ld_u64(aH, ws + WS_FLAG + (size_t)blk_of(2 * panel + ((lane >> 3) & 1), lane & 7) * 1024);
  if (!aux && cw == 2) asm volatile("s_mov_b32 m0, %2\n\ts_nop 0\n\tglobal_load_lds_dword %0, %1" :: "v"(lane * 4), "s"(b1 + fs * 128), "s"(lds_base(lds) + L_BIAS) : "memory");
  if (cw == 0 && lane < 24) cnt[lane] = 0u;
  if (aux) {
    asm volatile("s_waitcnt lgkmcnt(0)\n\ts_barrier" ::: "memory");
    __builtin_amdgcn_s_setprio(1);
    if (auxidx < 28) {
      const u16* wp_ = w1 + (size_t)(128 * cw + (lane >> 3)) * F_ + 64 * auxidx + 8 * (lane & 7);
      const int cblk = 2 * ((lane & 15) >> 2) + (auxidx >= 14 ? 1 : 0) + 8 * ((lane & 3) * 7 + ((auxidx >> 1) % 7));
      const u64 aC = __hip_atomic_load((const u64*)(ws + WS_FLAG + (size_t)cblk * 1024), RLX_AGENT);
      float a8[8] = {0.f, 0.f, 0.f, 0.f, 0.f, 0.f, 0.f, 0.f};
      u32x4 wv[16];
#pragma unroll
      for (int i = 0; i < 16; ++i) wv[i] = *(const u32x4*)(wp_ + (size_t)(8 * i) * F_);
      SCHED();
#pragma unroll
      for (int i = 0; i < 16; ++i)
#pragma unroll
        for (int c = 0; c < 4; ++c) { a8[2 * c] += bflo(wv[i][c]); a8[2 * c + 1] += bfhi(wv[i][c]); }
#pragma unroll
      for (int i = 0; i < 4; ++i) a8[i] = comb32(a8[i], a8[i + 4]);
#pragma unroll
      for (int i = 0; i < 2; ++i) a8[i] = dpp_add<0x128, 0xf>(comb16(a8[i], a8[i + 2]));
      if (!(lane & 8)) *(f32x2*)(lds + L_COLS + cw * 256 + (8 * (lane & 7) + 2 * (g & 1) + 4 * (g >> 1)) * 4) = (f32x2){a8[0], a8[1]};
      SCHED(); asm volatile("s_waitcnt lgkmcnt(0)" ::: "memory");
      if (last_of(lds, C_CS, 4, lane)) {
        const float* cp = (const float*)(lds + L_COLS);
        const float tot = (cp[lane] + cp[64 + lane]) + (cp[128 + lane] + cp[192 + lane]);
        __hip_atomic_store((float*)(ws + WS_CS) + 64 * auxidx + lane, tot, RLX_AGENT);
        asm volatile("s_waitcnt vmcnt(0)" ::: "memory");
        if (lane < 16) __hip_atomic_store((u64*)(ws + WS_FLAG + (size_t)cblk * 1024 + FL_ST) + 2 + (auxidx & 1), aC + 1, RLX_AGENT);
      }
    }
  }
  asm volatile("s_waitcnt vmcnt(2)" : "+v"(raw0), "+v"(raw1) :: "memory");
  float isc;
  unsigned T[TB_N];
  kgen_tables(T, isc, raw0, raw1, (d & 1) ? bfhi(sdt) : bflo(sdt), lds, cw, lane);
#pragma unroll
  for (int i = 0; i < TB_N; ++i) asm volatile("" : "+v"(T[i]));
  asm volatile("s_waitcnt vmcnt(0)" : "+v"(aS), "+v"(aH) :: "memory");
  const u64 aOwn = rdlane64(aS, 32);
  if (cw == 1 && lane < 16) *(u64*)(lds + L_ACON + lane * 8) = aH;
  if (cw == 3) { if (lane == 0) *(u64*)(lds + L_KSC + 16) = aOwn; ARRIVE_L(C_EP); }
  __builtin_amdgcn_s_setprio(1);
  const u64* lnfl = (const u64*)(flg + FL_LN) + (lane & 31);
  const char* up = ws + WS_UR + (size_t)d * 4096 + hf * 2048;
  u64 fv;
  if (aux) {
    await_<2>(lds, C_X2, 8);
  } else {
    const u64* stfl = (const u64*)(flg + FL_ST) + (lane & 3);
    const char* stp = cw == 1 ? ws + WS_CS + fs * 512 : ws + WS_STAT + (size_t)panel * 1024;
    const unsigned svo = cw == 1 ? (lane & 31) * 16 : lane * 16;
    u32x4 stv;
#define XBAR() do { SCHED(); asm volatile("s_waitcnt lgkmcnt(0)\n\ts_barrier" ::: "memory"); SCHED(); } while (0)
    if (cw < 2) {
      bool ok = false, got = false;
      XBAR(); XBAR(); XBAR(); XBAR();
      ld_u64(fv, stfl); gload16(stv, svo, stp);
#define XPOLL() do { XBAR(); asm volatile("s_waitcnt vmcnt(0)" : "+v"(fv), "+v"(stv) :: "memory"); \
        if (ok && !got) { *(u32x4*)(lds + (cw == 1 ? L_COLS : L_STAT) + svo) = stv; got = true; } \
        ok = __all(fv == aOwn + 1); SCHED(); ld_u64(fv, stfl); gload16(stv, svo, stp); } while (0)
      XPOLL(); XPOLL(); XPOLL(); XPOLL();
      asm volatile("s_waitcnt vmcnt(0)" : "+v"(fv), "+v"(stv) :: "memory");
      if (!got) {
        if (!ok) {
          poll_flags(stfl, aOwn + 1);
          asm volatile("global_load_dwordx4 %0, %1, %2 sc1\n\ts_waitcnt vmcnt(0)" : "=&v"(stv) : "v"(svo), "s"(stp) : "memory");
        }
        *(u32x4*)(lds + (cw == 1 ? L_COLS : L_STAT) + svo) = stv;
      }
    } else {
      XBAR(); XBAR(); XBAR(); XBAR(); XBAR(); XBAR(); XBAR(); XBAR();
    }
    XBAR();
    XBAR();
  }
  __builtin_amdgcn_s_setprio(3);
  K2_SETUP(cw);
  K2_DMA(0); K2_DMA(1);
  char* wl = lds + L_W + ch * WSZ;
  {
    char* tb = wl + hf * 9216;
#pragma unroll
    for (int i = 0; i < TB_N; ++i) *(unsigned*)(tb + i * TB_RS + lane * 4) = T[i];
    const char* tr = tb + g * 16;
    const int rXa = (i16 >> 3) * TB_RS, rXe = (17 + (i16 & 7)) * TB_RS;
    const int rYa = (i16 < 8 ? 2 + (i16 >> 2) : 4 + ((i16 - 8) >> 1)) * TB_RS, rYe = (17 + (i16 < 8 ? 2 * (i16 & 3) : 4 * (i16 & 1))) * TB_RS;
    const int rZa = (i16 < 8 ? 8 + i16 : 16) * TB_RS;
    const int rB1 = (25 + (i16 >> 2)) * TB_RS, rB0 = (29 + (i16 & 3)) * TB_RS;
    f32x4 kX = {0.f, 0.f, 0.f, 0.f}, kY = kX, kZ = kX;
#pragma unroll
    for (int sb = 0; sb < 4; sb += 2) {
      u32x4 t_[2][7];
#pragma unroll
      for (int s = 0; s < 2; ++s) {
        const char* tq = tr + (sb + s) * 64;
        t_[s][0] = *(const u32x4*)(tq + rB1); t_[s][1] = *(const u32x4*)(tq + rB0); t_[s][2] = *(const u32x4*)(tq + rXa); t_[s][3] = *(const u32x4*)(tq + rXe);
        t_[s][4] = *(const u32x4*)(tq + rYa); t_[s][5] = *(const u32x4*)(tq + rYe); t_[s][6] = *(const u32x4*)(tq + rZa);
      }
      SCHED();
#pragma unroll
      for (int s = 0; s < 2; ++s) {
        const f16x8 hb = __builtin_bit_cast(f16x8, cmulh4(t_[s][0], t_[s][1]));
        kX = __builtin_amdgcn_mfma_f32_16x16x32_f16(__builtin_bit_cast(f16x8, cmulh4(t_[s][2], t_[s][3])), hb, kX, 0, 0, 0);
        kY = __builtin_amdgcn_mfma_f32_16x16x32_f16(__builtin_bit_cast(f16x8, cmulh4(t_[s][4], t_[s][5])), hb, kY, 0, 0, 0);
        kZ = __builtin_amdgcn_mfma_f32_16x16x32_f16(__builtin_bit_cast(f16x8, t_[s][6]), hb, kZ, 0, 0, 0);
      }
      SCHED();
    }
    kgen_store(kX, kY, kZ, isc, lds, cw, lane);
  }
  ARRIVE_L(C_K0 + ch);
  ld_u64(fv, lnfl);
  await_<0>(lds, C_K0 + ch, 2);
  const f32x2 ks = *(const f32x2*)(lds + L_KSC + ch * 8);
  int ex = (int)((__float_as_uint(ks[0] + ks[1]) >> 23) & 255);
  ex = ex < 20 ? 20 : (ex > 230 ? 230 : ex);
  const float scale = __uint_as_float((unsigned)(267 - ex) << 23);
  const float iscale = __uint_as_float((unsigned)(ex - 13) << 23);
  char* kp = lds + L_KP + ch * KPSZ;
  {
    const float* k0 = (const float*)(lds + L_KAP + (2 * ch) * KAPSZ);
#define KSUM2(T, i) ((*(const T*)(k0 + (i)) + *(const T*)(k0 + 644 + (i))) * scale)
    const int A = 64 * hf + lane, A2 = 128 + A;
    const float e0 = KSUM2(float, 512 + A), e1 = KSUM2(float, 513 + A);
    u32x4 o;
    if (hf == 0) {
      const int i0 = lane < 32 ? 8 * A : 256 + 4 * (A - 32);
      const f32x4 a = KSUM2(f32x4, i0), b = KSUM2(f32x4, i0 + 4);
      const bool lo = lane < 32;
      o = (u32x4){pkh(a[0], lo ? a[1] : a[0]), pkh(lo ? a[2] : a[1], lo ? a[3] : a[2]), pkh(lo ? b[0] : a[2], lo ? b[1] : a[2]), pkh(lo ? b[2] : a[3], lo ? b[3] : b[0])};
      if (lane < 2) *(u32x4*)(kp + lane * 16) = (u32x4){0u, 0u, 0u, 0u};
    } else {
      const int i0 = 384 + 2 * lane;
      const f32x2 a = KSUM2(f32x2, i0), b = KSUM2(f32x2, i0 + 2);
      o = (u32x4){pkh(a[0], a[0]), pkh(a[0], a[1]), pkh(a[1], a[1]), pkh(b[0], b[0])};
    }
    *(u32x4*)(kp + 32 + A * 16) = o;
    const unsigned p00 = pkh(e0, e0);
    *(u32x4*)(kp + 32 + A2 * 16) = (u32x4){p00, p00, pkh((A2 & 1) ? e1 : e0, e1), pkh(e1, e1)};
  }
  ARRIVE_L(C_K1);
  asm volatile("s_waitcnt vmcnt(0)" : "+v"(fv) :: "memory");
  arrive(lds, C_W2, lane);
  const bool urok = __all(fv == aOwn + 1);
  u32x4 ua_, ub_, q0, q1, q2, q3;
  const char* qp = ws + WS_ST2 + hf * 4096;
#define U_LOADS() { gload16(ua_, lane * 16, up); gload16(ub_, lane * 16, up + 1024); \
    gload16(q0, lane * 32, qp); gload16(q1, lane * 32, qp + 16); gload16(q2, lane * 32, qp + 2048); gload16(q3, lane * 32, qp + 2064); }
#define U_WAIT() asm volatile("s_waitcnt vmcnt(0)" : "+v"(ua_), "+v"(ub_), "+v"(q0), "+v"(q1), "+v"(q2), "+v"(q3) :: "memory")
  U_LOADS();
  await_<0>(lds, C_K1, 4);
#pragma unroll
  for (int rnd = 0; rnd < 3; ++rnd) {
    const int a = rnd < 2 ? 128 * rnd + 64 * hf + lane : 256;
    if (rnd == 2 && !(hf == 0 && lane == 0)) break;
    const u32x4 c0 = *(const u32x4*)(kp + a * 16), c1 = *(const u32x4*)(kp + a * 16 + 16);
    const unsigned e[8] = {c0[0], c0[1], c0[2], c0[3], c1[0], c1[1], c1[2], c1[3]};
    unsigned o[7];
#pragma unroll
    for (int i = 0; i < 7; ++i) o[i] = __builtin_amdgcn_alignbit(e[i + 1], e[i], 16);
    const int m = (a & 1) | (((a >> 2) & 1) << 1) | (((a >> 1) & 1) << 2);
    char* base = wl + a * 128;
#pragma unroll
    for (int c = 0; c < 8; ++c) {
      u32x4 wv;
      if (c & 1) { const int s = (c + 1) >> 1; wv = (u32x4){e[s], e[s + 1], e[s + 2], e[s + 3]}; }
      else { const int s = c >> 1; wv = (u32x4){o[s], o[s + 1], o[s + 2], o[s + 3]}; }
      *(u32x4*)(base + ((c ^ m) << 4)) = wv;
    }
  }
  SCHED();
  U_WAIT();
  if (!urok) { poll_flags(lnfl, aOwn + 1); U_LOADS(); U_WAIT(); }
  char* ul = lds + L_U + ch * USZ;
  {
#define UN2(xw, s0, s1) ({ const unsigned a_ = pkbf(bflo(xw) - bflo(s0), bfhi(xw) - bflo(s1)); \
      const unsigned b_ = pkbf(bflo(a_) * __builtin_amdgcn_rcpf(bfhi(s0)), bfhi(a_) * __builtin_amdgcn_rcpf(bfhi(s1))); pkh(bflo(b_), bfhi(b_)); })
    const u32x4 o0 = {UN2(ua_[0], q0[0], q0[1]), UN2(ua_[1], q0[2], q0[3]), UN2(ua_[2], q1[0], q1[1]), UN2(ua_[3], q1[2], q1[3])};
    const u32x4 o1 = {UN2(ub_[0], q2[0], q2[1]), UN2(ub_[1], q2[2], q2[3]), UN2(ub_[2], q3[0], q3[1]), UN2(ub_[3], q3[2], q3[3])};
    *(u32x4*)(ul + hf * 2048 + lane * 16) = o0;
    *(u32x4*)(ul + hf * 2048 + 1024 + lane * 16) = o1;
  }
  if (hf == 0 && lane < 32) *(u32x4*)(ul + 4096 + lane * 16) = (u32x4){0u, 0u, 0u, 0u};
  ARRIVE_L(C_K2 + ch);
  await_<2>(lds, C_PUB, 8);
  await_<0>(lds, C_K2 + ch, 2);
  __builtin_amdgcn_s_setprio(3);
  f32x4 acc[8];
#pragma unroll
  for (int i = 0; i < 8; ++i) acc[i] = (f32x4){0.f, 0.f, 0.f, 0.f};
  {
    const int c = i16 + 8 * g;
    int fo[2];
#pragma unroll
    for (int par = 0; par < 2; ++par) {
      const int w = 32 * par + c;
      const int m = ((w >> 3) & 1) | (((w >> 5) & 1) << 1) | (((w >> 4) & 1) << 2);
      fo[par] = (w ^ m) << 4;
    }
    const char* fbase = wl + hf * (128 * 16);
    const char* gbase = ul + (2032 - 16 * i16 + 8 * g + 128 * hf) * 2 - 7 * 512;
#define CF(s, e) (*(const f16x8*)(fbase + fo[(s) & 1] + ((s) >> 1) * 1024 + (e) * 4096))
#define CG(s, mu) (*(const f16x8*)(gbase + (s) * 64 + (7 - (mu)) * 512))
    f16x8 F[2][8], G[2];
#pragma unroll
    for (int e = 0; e < 8; ++e) F[0][e] = CF(0, e);
    G[0] = CG(0, 0);
#pragma unroll
    for (int s = 0; s < 4; ++s) {
#pragma unroll
      for (int mu = 0; mu < 8; ++mu) {
        const int it = s * 8 + mu;
        SCHED();
        if (it + 1 < 32) G[(it + 1) & 1] = CG((it + 1) >> 3, (it + 1) & 7);
        if (s < 3) F[(s + 1) & 1][mu] = CF(s + 1, mu);
        SCHED();
#pragma unroll
        for (int e = 0; e + mu < 8; ++e) acc[e + mu] = __builtin_amdgcn_mfma_f32_16x16x32_f16(F[s & 1][e], G[it & 1], acc[e + mu], 0, 0, 0);
      }
    }
  }
  ARRIVE_L(C_K1 + 1 + ch); await_<0>(lds, C_K1 + 1 + ch, 2);
  {
    char* dst = wl + hf * 4096 + lane * 16;
#pragma unroll
    for (int i = 0; i < 4; ++i) *(f32x4*)(dst + i * 1024) = sel(hf, acc[i], acc[4 + i]);
  }
  ARRIVE_L(C_K3 + ch); await_<0>(lds, C_K3 + ch, 2);
  {
    const float dskip = (d & 1) ? bfhi(sds) : bflo(sds);
    const char* src = wl + (hf ^ 1) * 4096 + lane * 16;
    char* sdst = ws + WS_S + (size_t)d * 4096;
#pragma unroll
    for (int i = 0; i < 4; ++i) {
      const f32x4 yv = (sel(hf, acc[4 + i], acc[i]) + *(const f32x4*)(src + i * 1024)) * iscale;
      const int t = 256 * (4 * hf + i) + 16 * i16 + 4 * g;
      const u32x2 uu = *(const u32x2*)(ul + (2044 - t) * 2);
      const unsigned uu0 = uu[0], uu1 = uu[1];
      const f16x2 ua = __builtin_bit_cast(f16x2, uu0), ub = __builtin_bit_cast(f16x2, uu1);
      const unsigned y01 = pkbf(yv[0], yv[1]), y23 = pkbf(yv[2], yv[3]);
      const unsigned k01 = pkbf((float)ub[1] * dskip, (float)ub[0] * dskip), k23 = pkbf((float)ua[1] * dskip, (float)ua[0] * dskip);
      const u64 pk = (u64)pkbf(bflo(y01) + bflo(k01), bfhi(y01) + bfhi(k01)) | ((u64)pkbf(bflo(y23) + bflo(k23), bfhi(y23) + bfhi(k23)) << 32);
      __hip_atomic_store((u64*)(sdst + t * 2), pk, RLX_AGENT);
    }
  }
  asm volatile("s_waitcnt vmcnt(0) lgkmcnt(0)" ::: "memory");
  if (last_of(lds, C_SP, 4, lane) && lane < 32) __hip_atomic_store((u64*)(ws + WS_FLAG + (size_t)blk_of(lane, cb) * 1024 + FL_S) + mb, aS + 1, RLX_AGENT);
}

#define FIN_LOADS() { \
  asm volatile("global_load_dwordx4 %0, %1, off" : "=v"(xv) : "v"(x + (size_t)(64 * mb + (tid >> 3)) * D_ + 64 * cb + 8 * (tid & 7)) : "memory"); \
  asm volatile("global_load_dwordx4 %0, %1, off" : "=v"(bv) : "v"(b2 + 64 * cb + 8 * (tid & 7)) : "memory"); }
DI void final_stage(u32x4& xv, u32x4& bv, u16* __restrict__ out, char* lds, int mb, int cb, int tid) {
  await_<0>(lds, C_R1, 4);
  await_<0>(lds, C_ST, 4);
  const int t = tid >> 3, dq = tid & 7;
  const char* src = lds + L_RED + t * RED_RS + dq * 32;
  f32x4 ra[4], rb[4];
#pragma unroll
  for (int k = 0; k < 4; ++k) { ra[k] = *(const f32x4*)(src + k * 64 * RED_RS); rb[k] = *(const f32x4*)(src + k * 64 * RED_RS + 16); }
  const u32x4 sv = *(const u32x4*)(lds + L_ST + t * ST_RS + dq * 16);
  SCHED();
  const f32x4 a = (ra[0] + ra[1]) + (ra[2] + ra[3]), b = (rb[0] + rb[1]) + (rb[2] + rb[3]);
  asm volatile("s_waitcnt vmcnt(0)" : "+v"(xv), "+v"(bv) :: "memory");
  u32x4 o;
#pragma unroll
  for (int c = 0; c < 4; ++c) {
    const float v0 = (c < 2 ? a[2 * c] : b[2 * c - 4]) + bflo(bv[c]), v1 = (c < 2 ? a[2 * c + 1] : b[2 * c - 3]) + bfhi(bv[c]);
    const float g0 = v0 * __builtin_amdgcn_rcpf(1.f + __builtin_amdgcn_exp2f(-1.4426950408889634f * v0));
    const float g1 = v1 * __builtin_amdgcn_rcpf(1.f + __builtin_amdgcn_exp2f(-1.4426950408889634f * v1));
    const unsigned pv = pkbf(bflo(sv[c]) * g0, bfhi(sv[c]) * g1);
    o[c] = pkbf(bflo(xv[c]) + bflo(pv), bfhi(xv[c]) + bfhi(pv));
  }
  *(u32x4*)(out + (size_t)(64 * mb + t) * D_ + 64 * cb + 8 * dq) = o;
}
DI void p2_gemm(const u16* __restrict__ x, const u16* __restrict__ w2, u64 aOwn, const u16* __restrict__ b2, u16* __restrict__ out, char* __restrict__ ws,
                char* lds, int mb, int cb, int kq, int lane) {
  const int r = lane & 31, h = lane >> 5;
  unsigned* cnt = (unsigned*)(lds + L_CNT);
  const int tid = kq * 64 + lane;
  K2_SETUP(kq);
  char* ring = lds + L_RING2 + kq * (K2_R * 4096);
  const char* flg = ws + WS_FLAG + (size_t)blockIdx.x * 1024;
  const char* hbase = ws + WS_H + (size_t)(2 * mb) * HMT + 28 * kq * 1024;
  const unsigned hoff0 = lane * 16, hoff1 = lane * 16 + HMT;
  const int q = (lane & 15) >> 2;
  const int toff0 = (4 * h + q) * 128 + ((q >> 1) << 6) + ((lane >> 4) & 1) * 32 + 8 * (lane & 3), toff1 = toff0 ^ 64;
  u32x4 hb[3][4];
  s16x8 wf[2][4];
  f32x16 acc[2][2];
#pragma unroll
  for (int i = 0; i < 16; ++i) { acc[0][0][i] = 0.f; acc[0][1][i] = 0.f; acc[1][0][i] = 0.f; acc[1][1][i] = 0.f; }
  await_<1>(lds, C_PUB, 8);
  K2_DMA(2); K2_DMA(3);
  if (kq == 0) {
    const u64* fp = (const u64*)(flg + FL_H) + (lane < 14 ? lane : 0);
    for (unsigned spin = 0; spin < (1u << 20); ++spin) {
      u64 v;
      asm volatile("global_load_dwordx2 %0, %1, off sc1\n\ts_waitcnt vmcnt(0)" : "=&v"(v) : "v"(fp) : "memory");
      if (__all(v == aOwn + 1)) break;
      __builtin_amdgcn_s_sleep(2);
    }
    lds_set(cnt + C_H, lane);
  } else {
    lds_wait(cnt + C_H);
  }
#define K2_HL(S, s2) { const char* hs_ = hbase + K2S(S) * 2048 + (s2) * 1024; gload16(hb[(S) % 3][2 * (s2)], hoff0, hs_); gload16(hb[(S) % 3][2 * (s2) + 1], hoff1, hs_); SCHED(); }
#define K2_HLD(S) { K2_HL(S, 0); K2_HL(S, 1); }
#define K2_TR(S, i) { const char* sl_ = ring + ((S) % K2_R) * 4096 + ((i) >> 1) * 2048 + (((i) & 1) ? toff1 : toff0); wf[(S) & 1][i] = tr_frag(sl_, sl_ + 1024); SCHED(); }
#define K2_D1(S, pp) { dma16s(wvoff, wsrc + (size_t)(K2S(S) * 32 + (pp) * 8) * D_, ringa + ((S) % K2_R) * 4096 + (pp) * 1024); SCHED(); }
#define K2_M(S, s2, mi, di) { acc[mi][di] = __builtin_amdgcn_mfma_f32_32x32x16_bf16(wf[(S) & 1][2 * (s2) + (di)], __builtin_bit_cast(s16x8, hb[(S) % 3][2 * (s2) + (mi)]), acc[mi][di], 0, 0, 0); SCHED(); }
#define K2_STEP(S, W, TRN, HN, DN) { \
    asm volatile("s_waitcnt vmcnt(" #W ")" : "+v"(hb[(S) % 3][0]), "+v"(hb[(S) % 3][1]), "+v"(hb[(S) % 3][2]), "+v"(hb[(S) % 3][3]) :: "memory"); SCHED(); \
    K2_M(S,0,0,0); if (TRN) K2_TR((S)+1,0); K2_M(S,0,1,0); if (TRN) K2_TR((S)+1,1); K2_M(S,0,0,1); if (TRN) K2_TR((S)+1,2); K2_M(S,0,1,1); if (TRN) K2_TR((S)+1,3); \
    K2_M(S,1,0,0); if (DN) { K2_D1((S)+4,0); K2_D1((S)+4,1); } K2_M(S,1,1,0); if (DN) { K2_D1((S)+4,2); K2_D1((S)+4,3); } K2_M(S,1,0,1); if (HN) K2_HL((S)+3,0); K2_M(S,1,1,1); if (HN) K2_HL((S)+3,1); }
  K2_HLD(0); K2_HLD(1); K2_HLD(2);
  await_<0>(lds, C_W2, 4);
  K2_TR(0,0); K2_TR(0,1); K2_TR(0,2); K2_TR(0,3);
  K2_STEP(0, 8, 1, 1, 1);
  K2_STEP(1, 12, 1, 1, 1);
  K2_STEP(2, 16, 1, 1, 1);
  K2_STEP(3, 16, 1, 1, 1);
  K2_STEP(4, 16, 1, 1, 1);
  K2_STEP(5, 16, 1, 1, 1);
  K2_STEP(6, 16, 1, 1, 1);
  K2_STEP(7, 16, 1, 1, 1);
  K2_STEP(8, 16, 1, 1, 1);
  K2_STEP(9, 16, 1, 1, 1);
  K2_STEP(10, 16, 1, 1, 0);
  u32x4 xv, bv;
  FIN_LOADS();
  K2_STEP(11, 14, 1, 0, 0);
  K2_STEP(12, 6, 1, 0, 0);
  K2_STEP(13, 2, 0, 0, 0);
  await_<0>(lds, C_SP, 4);
  {
    char* dst = lds + L_RED + (kq * 64 + r) * RED_RS + h * 16;
#pragma unroll
    for (int mi = 0; mi < 2; ++mi)
#pragma unroll
      for (int di = 0; di < 2; ++di)
#pragma unroll
        for (int rq = 0; rq < 4; ++rq)
          *(f32x4*)(dst + mi * 32 * RED_RS + di * 128 + rq * 32) = (f32x4){acc[mi][di][4 * rq], acc[mi][di][4 * rq + 1], acc[mi][di][4 * rq + 2], acc[mi][di][4 * rq + 3]};
  }
  ARRIVE_L(C_R1);
  final_stage(xv, bv, out, lds, mb, cb, tid);
  if (tid == 0) __hip_atomic_store((u64*)(ws + WS_FLAG + (size_t)blockIdx.x * 1024), aOwn + 1, RLX_AGENT);
}

DI void p2_helper(const u16* __restrict__ x, const u16* __restrict__ w2, u64 aOwn, const u16* __restrict__ b2, u16* __restrict__ out, char* __restrict__ ws,
                  char* lds, int mb, int cb, int gw, int lane) {
  unsigned* cnt = (unsigned*)(lds + L_CNT);
  const int tid = gw * 64 + lane, hw = gw - 4;
  u32x4 xv, bv;
  FIN_LOADS();
  if (hw == 0) {
    const u64* fp = (const u64*)(ws + WS_FLAG + (size_t)blockIdx.x * 1024 + FL_S) + (lane & 31);
    for (unsigned spin = 0; spin < (1u << 18); ++spin) {
      const u64 v = __hip_atomic_load(fp, RLX_AGENT);
      if (__all(v == aOwn + 1)) break;
      __builtin_amdgcn_s_sleep(8);
    }
    asm volatile("" ::: "memory");
    lds_set(cnt + C_S, lane);
  } else {
    while (__hip_atomic_load(cnt + C_S, RLX_WG) == 0u) __builtin_amdgcn_s_sleep(4);
    asm volatile("" ::: "memory");
  }
  u32x4 sa, sb;
  const char* sp = ws + WS_S + (size_t)(64 * cb + 16 * hw) * 4096 + 64 * mb * 2;
  gload16(sa, (lane >> 3) * 4096 + (lane & 7) * 16, sp);
  gload16(sb, (lane >> 3) * 4096 + (lane & 7) * 16, sp + 8 * 4096);
  asm volatile("s_waitcnt vmcnt(0)" : "+v"(sa), "+v"(sb) :: "memory");
  char* dst = lds + L_ST + (8 * (lane & 7)) * ST_RS + (16 * hw + (lane >> 3)) * 2;
#pragma unroll
  for (int c = 0; c < 4; ++c) {
    *(u16*)(dst + (2 * c) * ST_RS) = (u16)sa[c]; *(u16*)(dst + (2 * c + 1) * ST_RS) = (u16)(sa[c] >> 16);
    *(u16*)(dst + (2 * c) * ST_RS + 16) = (u16)sb[c]; *(u16*)(dst + (2 * c + 1) * ST_RS + 16) = (u16)(sb[c] >> 16);
  }
  ARRIVE_L(C_ST);
  while (__hip_atomic_load(cnt + C_R1, RLX_WG) < 4u) __builtin_amdgcn_s_sleep(2);
  final_stage(xv, bv, out, lds, mb, cb, tid);
}

__global__ __launch_bounds__(768, 3) void fused_kernel(const u16* __restrict__ x, const u16* __restrict__ lar, const u16* __restrict__ aim,
                                                       const u16* __restrict__ cre, const u16* __restrict__ cim, const u16* __restrict__ ldt,
                                                       const u16* __restrict__ dsk, const u16* __restrict__ w1, const u16* __restrict__ b1,
                                                       const u16* __restrict__ w2, const u16* __restrict__ b2, u16* __restrict__ out,
                                                       char* __restrict__ ws) {
  extern __shared__ __attribute__((aligned(16))) char lds[];
  const int lane = threadIdx.x & 63;
  const int wid = __builtin_amdgcn_readfirstlane(threadIdx.x >> 6);
  const int b = blockIdx.x, xcd = b & 7, j = b >> 3;
  const int mb = 4 * xcd + (j >> 3), cb = j & 7;
  const int jj = j < 28 ? j : 0;
  const int panel = 4 * (xcd >> 1) + jj / 7, fs = 7 * (xcd & 1) + jj % 7;
  if (wid < 4) {
    const int de = 64 * cb + 2 * mb;
    const unsigned sdt = *(const __attribute__((address_space(4))) unsigned*)(ldt + de);
    const unsigned sds = *(const __attribute__((address_space(4))) unsigned*)(dsk + de);
    x_wave(lar, aim, cre, cim, sdt, sds, w2, w1, b1, j >= 28 ? xcd * 4 + (j - 28) : -1, panel, fs, ws, lds, mb, cb, wid, lane);
    return;
  }
  const int gw = wid - 4;
  __builtin_amdgcn_s_setprio(2);
  if (j < 28) p1_gemm(x, w1, ws, lds, panel, fs, gw, lane);
  else p1_aux(x, ws, lds, xcd * 4 + (j - 28), gw, lane);
  __builtin_amdgcn_s_setprio(0);
  await_<0>(lds, C_EP, 1);
  const u64 aOwn = *(const u64*)(lds + L_KSC + 16);
  if (gw < 4) p2_gemm(x, w2, aOwn, b2, out, ws, lds, mb, cb, gw, lane);
  else p2_helper(x, w2, aOwn, b2, out, ws, lds, mb, cb, gw, lane);
}

extern "C" void kernel_launch(void* const* d_in, const int* in_sizes, int n_in, void* d_out, int out_size, void* d_ws, size_t ws_size,
                              hipStream_t stream) {
  const u16* x = (const u16*)d_in[0];
  const u16* lar = (const u16*)d_in[1];
  const u16* aim = (const u16*)d_in[2];
  const u16* cre = (const u16*)d_in[3];
  const u16* cim = (const u16*)d_in[4];
  const u16* ldt = (const u16*)d_in[5];
  const u16* dsk = (const u16*)d_in[6];
  const u16* w1 = (const u16*)d_in[8];
  const u16* b1 = (const u16*)d_in[9];
  const u16* w2 = (const u16*)d_in[10];
  const u16* b2 = (const u16*)d_in[11];
  (void)hipFuncSetAttribute((const void*)fused_kernel, hipFuncAttributeMaxDynamicSharedMemorySize, LDS_TOTAL);
  fused_kernel<<<256, 768, LDS_TOTAL, stream>>>(x, lar, aim, cre, cim, ldt, dsk, w1, b1, w2, b2, (u16*)d_out, (char*)d_ws);
}
```

```cpp
#include <hip/hip_runtime.h>
#include <stdint.h>

typedef unsigned short u16;
typedef unsigned long long u64;
typedef __attribute__((ext_vector_type(4))) short s16x4;
typedef __attribute__((ext_vector_type(8))) short s16x8;
typedef __attribute__((ext_vector_type(8))) _Float16 f16x8;
typedef __attribute__((ext_vector_type(2))) _Float16 f16x2;
typedef __attribute__((ext_vector_type(2))) __bf16 bf16x2;
typedef __attribute__((ext_vector_type(2))) float f32x2;
typedef __attribute__((ext_vector_type(4))) float f32x4;
typedef __attribute__((ext_vector_type(16))) float f32x16;
typedef __attribute__((ext_vector_type(4))) unsigned u32x4;
typedef __attribute__((ext_vector_type(2))) unsigned u32x2;
#define LDSP __attribute__((address_space(3)))
#define GLBP __attribute__((address_space(1)))
#define DI __device__ __forceinline__

#define NLAUNCH 1

#define XPRIO 3
#define L_ 2048
#define D_ 512
#define N_ 128
#define F_ 1792

#define WS_H 0
#define WS_UR (8u << 20)
#define WS_S (10u << 20)
#define WS_STAT (12u << 20)
#define WS_CS ((12u << 20) + 65536)
#define WS_ST2 ((12u << 20) + 131072)
#define WS_FLAG (13u << 20)
#define WS_KAP (16u << 20)
#define FL_LN 128
#define FL_H 384
#define FL_S 512
#define FL_ST 768

#define STAMP_INIT(kid)
#define STAMP()
#define STAMP0()
#define STAMP_DECL
#define STAMP_FLUSH()
#define TK0_ARG
#define TK0_PASS

DI float bf2f(unsigned v) { return __uint_as_float(v << 16); }
DI float bflo(unsigned v) { return __uint_as_float(v << 16); }
DI float bfhi(unsigned v) { return __uint_as_float(v & 0xffff0000u); }
DI unsigned pkbf(float a, float b) { bf16x2 q = {(__bf16)a, (__bf16)b}; return __builtin_bit_cast(unsigned, q); }
DI float rbf(float a) { __bf16 q = (__bf16)a; return (float)q; }
DI unsigned pkh(float a, float b) { f32x2 v = {a, b}; f16x2 p = __builtin_convertvector(v, f16x2); return __builtin_bit_cast(unsigned, p); }
DI float dot2(unsigned a, unsigned b, float c) { return __builtin_amdgcn_fdot2_f32_bf16(__builtin_bit_cast(bf16x2, a), __builtin_bit_cast(bf16x2, b), c, false); }
template <class T> DI T sel(int c, T a, T b) { return c ? a : b; }
DI void dma16(const void* g, char* l) { __builtin_amdgcn_global_load_lds((const GLBP unsigned*)g, (LDSP unsigned*)l, 16, 0, 0); }
DI void dma16s(unsigned voff, const void* sbase, unsigned ldsaddr) {
  asm volatile("s_mov_b32 m0, %2\n\ts_nop 0\n\tglobal_load_lds_dwordx4 %0, %1" :: "v"(voff), "s"(sbase), "s"(ldsaddr) : "memory");
}
DI unsigned lds_base(char* lds) { return __builtin_amdgcn_readfirstlane((unsigned)(size_t)(LDSP char*)lds); }
DI s16x8 tr_frag(const char* p0, const char* p1) {
  s16x4 a = __builtin_amdgcn_ds_read_tr16_b64_v4i16((LDSP s16x4*)p0);
  s16x4 b = __builtin_amdgcn_ds_read_tr16_b64_v4i16((LDSP s16x4*)p1);
  return __builtin_shufflevector(a, b, 0, 1, 2, 3, 4, 5, 6, 7);
}
#define SCHED() __builtin_amdgcn_sched_barrier(0)
#define WAITV_BAR(n) do { SCHED(); asm volatile("s_waitcnt vmcnt(" #n ") lgkmcnt(0)\n\ts_barrier" ::: "memory"); SCHED(); } while (0)
#define LGKM_BAR() do { SCHED(); asm volatile("s_waitcnt lgkmcnt(0)\n\ts_barrier" ::: "memory"); SCHED(); } while (0)
#define RLX_AGENT __ATOMIC_RELAXED, __HIP_MEMORY_SCOPE_AGENT
#define RLX_WG __ATOMIC_RELAXED, __HIP_MEMORY_SCOPE_WORKGROUP

template <int CTRL, int RM> DI float dpp_add(float v) {
  return v + __builtin_bit_cast(float, __builtin_amdgcn_update_dpp(0, __builtin_bit_cast(int, v), CTRL, RM, 0xf, true));
}
DI float wave_sum63(float v) {
  v = dpp_add<0x111, 0xf>(v); v = dpp_add<0x112, 0xf>(v); v = dpp_add<0x114, 0xf>(v); v = dpp_add<0x118, 0xf>(v);
  v = dpp_add<0x142, 0xa>(v); v = dpp_add<0x143, 0xc>(v);
  return v;
}
DI float comb32(float a, float b) { auto r = __builtin_amdgcn_permlane32_swap(__float_as_uint(a), __float_as_uint(b), false, false); return __uint_as_float(r[0]) + __uint_as_float(r[1]); }
DI float comb16(float a, float b) { auto r = __builtin_amdgcn_permlane16_swap(__float_as_uint(a), __float_as_uint(b), false, false); return __uint_as_float(r[0]) + __uint_as_float(r[1]); }
DI float row_allsum(float v) { v = dpp_add<0x128, 0xf>(v); v = dpp_add<0x124, 0xf>(v); v = dpp_add<0x122, 0xf>(v); v = dpp_add<0x121, 0xf>(v); return v; }
DI float rdlane(float v, int l) { return __builtin_bit_cast(float, __builtin_amdgcn_readlane(__builtin_bit_cast(int, v), l)); }

DI void group_barrier(unsigned* cnt, unsigned target, int lane) {
  asm volatile("s_waitcnt lgkmcnt(0)" ::: "memory");
  if (lane == 0) __hip_atomic_fetch_add(cnt, 1u, RLX_WG);
  while (__hip_atomic_load(cnt, RLX_WG) < target) __builtin_amdgcn_s_sleep(1);
  asm volatile("" ::: "memory");
}
DI void poll_flags(const u64* p, u64 expect) {
  for (unsigned spin = 0; spin < (1u << 20); ++spin) {
    const u64 v = __hip_atomic_load(p, RLX_AGENT);
    if (__all(v == expect)) break;
    __builtin_amdgcn_s_sleep(2);
  }
  asm volatile("" ::: "memory");
}
DI void lds_set(unsigned* w, int lane) { if (lane == 0) __hip_atomic_store(w, 1u, RLX_WG); }
DI void lds_wait(unsigned* w) { while (__hip_atomic_load(w, RLX_WG) == 0u) __builtin_amdgcn_s_sleep(1); asm volatile("" ::: "memory"); }
DI int blk_of(int mb, int cb) { return (mb >> 2) + 8 * (((mb & 3) << 3) | cb); }

#define LDS_TOTAL 163840
#define L_STAT 162304
#define L_BIAS 163328
#define L_ACON 163584
#define L_KSC 163712
#define L_CNT 163744
#define KAPSZ 2576
#define L_KAP 141696
#define L_COLS 152000
#define K1_SLOT 32768
#define L_TB 98304
#define L_KP 131584
#define KPSZ 4160
#define K2_R 4
#define L_RING2 0
#define L_W 65536
#define WSZ 33024
#define L_U L_KAP
#define USZ 4608
#define L_RED L_W
#define RED_RS 272
#define L_ST L_KAP
#define ST_RS 144
#define C_PUB 0
#define C_SP 1
#define C_H 2
#define C_S 3
#define C_W2 4
#define C_R1 5
#define C_ST 6
#define C_K0 7
#define C_K1 9
#define C_K2 12
#define C_K3 14
#define C_LN1 16
#define C_LN2 17
#define C_X2 18
#define C_CS 19
#define C_EP 20
#define C_G8 21
#define L_XT 153088
DI void arrive(char* lds, int c, int lane) { if (lane == 0) __hip_atomic_fetch_add((unsigned*)(lds + L_CNT) + c, 1u, RLX_WG); }
template <int SL> DI void await_(char* lds, int c, unsigned target) {
  while (__hip_atomic_load((unsigned*)(lds + L_CNT) + c, RLX_WG) < target) __builtin_amdgcn_s_sleep(SL);
  asm volatile("" ::: "memory"); SCHED();
}
#define ARRIVE_V(n, c) do { SCHED(); asm volatile("s_waitcnt vmcnt(" #n ") lgkmcnt(0)" ::: "memory"); arrive(lds, c, lane); SCHED(); } while (0)
#define ARRIVE_L(c) do { SCHED(); asm volatile("s_waitcnt lgkmcnt(0)" ::: "memory"); arrive(lds, c, lane); SCHED(); } while (0)

struct cf { float x, y; };
DI cf cmul(cf a, cf b) { return cf{fmaf(a.x, b.x, -(a.y * b.y)), fmaf(a.x, b.y, a.y * b.x)}; }
DI unsigned pkc(cf a) { return pkh(a.x, a.y); }
DI unsigned pkcc(cf a) { return pkh(a.x, -a.y); }
DI cf zpow(float al, float om, float p) {
  const float m = __builtin_amdgcn_exp2f(al * p), ph = __builtin_amdgcn_fractf(om * p);
  return cf{m * __builtin_amdgcn_cosf(ph), m * __builtin_amdgcn_sinf(ph)};
}
DI unsigned cmulh(unsigned a, unsigned b) {
  const f16x2 z = __builtin_bit_cast(f16x2, a), w = __builtin_bit_cast(f16x2, b);
  const f16x2 t = z * (f16x2){w[0], w[0]};
  const f16x2 r = __builtin_elementwise_fma((f16x2){-z[1], z[0]}, (f16x2){w[1], w[1]}, t);
  return __builtin_bit_cast(unsigned, r);
}
DI u32x4 cmulh4(u32x4 a, u32x4 b) { return (u32x4){cmulh(a[0], b[0]), cmulh(a[1], b[1]), cmulh(a[2], b[2]), cmulh(a[3], b[3])}; }
#define TB_RS 272
#define TB_N 33
DI void kgen_tables(unsigned (&T)[TB_N], float& isc, unsigned raw0, unsigned raw1, float ldtv, char* lds, int cw, int lane) {
  const int src = ((lane & 31) >> 1) * 4, sh = (lane & 1) ? 0 : 16;
  unsigned pv[4];
#pragma unroll
  for (int a = 0; a < 4; ++a) {
    const unsigned lo = (unsigned)__builtin_amdgcn_ds_bpermute(src + 64 * a, (int)raw0), hi = (unsigned)__builtin_amdgcn_ds_bpermute(src + 64 * a, (int)raw1);
    pv[a] = ((lane < 32 ? lo : hi) << sh) & 0xffff0000u;
  }
  const float dt = rbf(__builtin_amdgcn_exp2f(ldtv * 1.4426950408889634f));
  const float ar = rbf(__builtin_amdgcn_exp2f(__uint_as_float(pv[0]) * 1.4426950408889634f));
  const float al = -(ar * dt) * 1.4426950408889634f;
  const float om = (__uint_as_float(pv[1]) * dt) * 0.15915494309189535f;
  const float cr = __uint_as_float(pv[2]) * dt, ci = __uint_as_float(pv[3]) * dt;
  const float sabs = 2.f * rdlane(wave_sum63(fabsf(cr) + fabsf(ci)), 63);
  int ex = (int)((__float_as_uint(sabs) >> 23) & 255);
  ex = ex < 20 ? 20 : (ex > 230 ? 230 : ex);
  const float sc2 = __uint_as_float((unsigned)(268 - ex) << 23);
  isc = __uint_as_float((unsigned)(ex - 13) << 23);
  if (lane == 0) *(float*)(lds + L_KSC + cw * 4) = sabs;
  const cf z1 = zpow(al, om, 1.f), z8 = zpow(al, om, 8.f), z32 = zpow(al, om, 32.f), a1 = zpow(al, om, 128.f), a4 = zpow(al, om, 512.f);
  const unsigned one = 0x00003C00u;
  const cf a2 = cmul(a1, a1), a3 = cmul(a2, a1), a8 = cmul(a4, a4), a12 = cmul(a8, a4);
  T[0] = one; T[1] = pkc(a1); T[2] = pkc(a2); T[3] = pkc(a3);
  T[4] = pkc(a4); T[5] = pkc(cmul(a4, a1)); T[6] = pkc(cmul(a4, a2)); T[7] = pkc(cmul(a4, a3));
  T[8] = pkc(a8); T[9] = pkc(cmul(a8, a1)); T[10] = pkc(cmul(a8, a2)); T[11] = pkc(cmul(a8, a3));
  T[12] = pkc(a12); T[13] = pkc(cmul(a12, a1)); T[14] = pkc(cmul(a12, a2)); T[15] = pkc(cmul(a12, a3));
  T[16] = pkc(cmul(a8, a8));
  const cf e2 = cmul(z1, z1), e3 = cmul(e2, z1), e4 = cmul(e2, e2);
  T[17] = one; T[18] = pkc(z1); T[19] = pkc(e2); T[20] = pkc(e3); T[21] = pkc(e4);
  T[22] = pkc(cmul(e4, z1)); T[23] = pkc(cmul(e4, e2)); T[24] = pkc(cmul(e4, e3));
  const cf c0 = cf{cr * sc2, ci * sc2}, c2 = cmul(c0, cmul(z32, z32));
  T[25] = pkcc(c0); T[26] = pkcc(cmul(c0, z32)); T[27] = pkcc(c2); T[28] = pkcc(cmul(c2, z32));
  const cf z16 = cmul(z8, z8);
  T[29] = one; T[30] = pkcc(z8); T[31] = pkcc(z16); T[32] = pkcc(cmul(z16, z8));
}
#define KG_PARAMS f32x4& kX, f32x4& kY, f32x4& kZ, float isc
DI void kgen_store(KG_PARAMS, char* lds, int wid, int lane) {
  const int i16 = lane & 15, g = lane >> 4;
  float* kap = (float*)(lds + L_KAP + wid * KAPSZ);
  *(f32x4*)(kap + 128 * (g >> 1) + 4 * (g & 1) + 8 * i16) = kX * isc;
  if (g < 2) {
    *(f32x4*)(kap + 256 + 64 * g + 4 * i16) = kY * isc;
#pragma unroll
    for (int r = 0; r < 4; ++r) kap[512 + 16 * (4 * g + r) + i16] = kZ[r] * isc;
  } else {
    *(f32x2*)(kap + 384 + 64 * (g - 2) + 2 * i16) = (f32x2){kY[0] * isc, kY[1] * isc};
    *(f32x2*)(kap + 384 + 64 * (g - 2) + 32 + 2 * i16) = (f32x2){kY[2] * isc, kY[3] * isc};
    if (g == 2 && i16 == 0) kap[640] = kZ[0] * isc;
  }
}
DI const u16* prm_ptr(const u16* lar, const u16* aim, const u16* cre, const u16* cim, int d, int hf, int lane) {
  const int g = lane >> 4;
  const u16* p = g == 0 ? lar : (g == 1 ? aim : (g == 2 ? cre : cim));
  return p + d * N_ + 64 * hf + 2 * (lane & 15);
}
DI void ld_u32(unsigned& v, const void* p) { asm volatile("global_load_dword %0, %1, off" : "=v"(v) : "v"(p) : "memory"); }
DI void ld_u64(u64& v, const void* p) { asm volatile("global_load_dwordx2 %0, %1, off sc1" : "=v"(v) : "v"(p) : "memory"); }
DI void gload16(u32x4& v, unsigned voff, const char* sbase) {
  asm volatile("global_load_dwordx4 %0, %1, %2 sc1" : "=v"(v) : "v"(voff), "s"(sbase) : "memory");
}
DI u64 rdlane64(u64 v, int l) { return (u64)(unsigned)__builtin_amdgcn_readlane((int)(unsigned)v, l) | ((u64)(unsigned)__builtin_amdgcn_readlane((int)(unsigned)(v >> 32), l) << 32); }
DI bool last_of(char* lds, int c, unsigned n, int lane) {
  unsigned old = 0;
  if (lane == 0) old = __hip_atomic_fetch_add((unsigned*)(lds + L_CNT) + c, 1u, RLX_WG);
  return (unsigned)__builtin_amdgcn_readfirstlane(old) == n - 1;
}
DI float gelu_f(float v) {
  const float c0 = -2.302208198f;
  const float c1 = -0.1029432397f;
  const float z = v * fmaf(v * v, c1, c0);
  return v * __builtin_amdgcn_rcpf(1.f + __builtin_amdgcn_exp2f(z));
}
#define K2S(S) ((S) + r2 >= 14 ? (S) + r2 - 14 : (S) + r2)
#define K2_DMA(S) { _Pragma("unroll") for (int pp = 0; pp < 4; ++pp) dma16s(wvoff, wsrc + (size_t)(K2S(S) * 32 + pp * 8) * D_, ringa + ((S) % K2_R) * 4096 + pp * 1024); SCHED(); }
#define K2_SETUP(kq_) int lane2_ = lane; asm volatile("" : "+v"(lane2_)); \
  const unsigned wvoff = (lane2_ >> 3) * (D_ * 2) + (((lane2_ & 7) ^ (((lane2_ >> 4) & 1) << 2)) << 4); \
  const unsigned ringa = __builtin_amdgcn_readfirstlane(lds_base(lds) + L_RING2 + (kq_) * (K2_R * 4096)); \
  const u16* wsrc = w2 + (size_t)(14 * (kq_) * 32) * D_ + 64 * cb; \
  const int r2 = (2 * cb + 4 * (mb & 3)) % 14;
#define HMT 114688

DI void p1_gemm(const u16* __restrict__ x, const u16* __restrict__ w1, char* __restrict__ ws, char* lds, int panel, int fs, int wid, int lane TK0_ARG) {
  STAMP_DECL STAMP_INIT(0) STAMP0();
  const int wm = wid >> 2, wf = (wid >> 1) & 1, kh = wid & 1;
  const int r = lane & 31, h = lane >> 5;
  const int row0 = panel * 128, f0 = fs * 128;
  const unsigned xv0 = (lane >> 3) * 1024 + (((lane & 7) ^ ((lane >> 4) & 3)) << 4);
  const unsigned xv1 = xv0 ^ 64;
  const unsigned wv = (lane >> 4) * (F_ * 2) + (((lane & 15) ^ (((lane >> 4) & 3) << 2)) << 4);
  const u16* xsb = x + (size_t)(row0 + 16 * wid) * D_;
  const u16* wsb = w1 + (size_t)(8 * wid) * F_ + f0;
  const unsigned la = lds_base(lds) + wid * 2048;
  const int rot = 2 * (panel & 3) + (fs >= 7 ? fs - 7 : fs);
#define KS(S) (((S) + rot) & 7)
#define K1_D0(S) { dma16s(xv0, xsb + KS(S) * 64, la + ((S) % 4) * K1_SLOT); SCHED(); }
#define K1_D1(S) { dma16s(xv1, xsb + 8 * D_ + KS(S) * 64, la + ((S) % 4) * K1_SLOT + 1024); SCHED(); }
#define K1_D2(S) { dma16s(wv, wsb + (size_t)(KS(S) * 64) * F_, la + ((S) % 4) * K1_SLOT + 16384); SCHED(); }
#define K1_D3(S) { dma16s(wv, wsb + (size_t)(KS(S) * 64 + 4) * F_, la + ((S) % 4) * K1_SLOT + 16384 + 1024); SCHED(); }
#define K1_ISSUE(S) { K1_D0(S); K1_D1(S); K1_D2(S); K1_D3(S); }
  K1_ISSUE(0); K1_ISSUE(1); K1_ISSUE(2); K1_ISSUE(3);
  const int xoff = (64 * wm + r) * 128 + (((4 * kh + 2 * wm + h) ^ ((r >> 1) & 7)) << 4);
  const int q = (lane & 15) >> 2, p = lane & 3;
  const int woff0 = 16384 + (32 * kh + 16 * wm + 8 * h + q) * 256 + (((2 * wf + kh) ^ q) << 6) + ((lane >> 4) & 1) * 32 + 8 * p;
  f32x16 acc[2][2];
#pragma unroll
  for (int i = 0; i < 16; ++i) { acc[0][0][i] = 0.f; acc[0][1][i] = 0.f; acc[1][0][i] = 0.f; acc[1][1][i] = 0.f; }
  STAMP();
  s16x8 xf[2][2][2], wfr[2][2][2];
#define K1_M(T, s, mi, fi) { acc[mi][fi] = __builtin_amdgcn_mfma_f32_32x32x16_bf16(wfr[(T) & 1][s][fi], xf[(T) & 1][s][mi], acc[mi][fi], 0, 0, 0); SCHED(); }
#define K1_RX(T, s, mi) { xf[(T) & 1][s][mi] = *(const s16x8*)(lds + (xa_ ^ ((s) << 5)) + (mi) * 4096); SCHED(); }
#define K1_RW(T, s, fi) { wfr[(T) & 1][s][fi] = tr_frag(lds + (wa_ ^ (((fi) << 6) | ((s) << 12))), lds + (wa_ ^ (((fi) << 6) | ((s) << 12))) + 1024); SCHED(); }
#define K1_ADDR(T) int xa_ = xoff + ((T) % 4) * K1_SLOT, wa_ = woff0 + ((T) % 4) * K1_SLOT; asm volatile("" : "+v"(xa_), "+v"(wa_));
#define K1_READ(T) { K1_ADDR(T) K1_RX(T,0,0); K1_RW(T,0,0); K1_RW(T,0,1); K1_RX(T,0,1); K1_RX(T,1,0); K1_RW(T,1,0); K1_RW(T,1,1); K1_RX(T,1,1); }
#define K1_MMA(T) { K1_M(T,0,0,0); K1_M(T,0,0,1); K1_M(T,0,1,0); K1_M(T,0,1,1); K1_M(T,1,0,0); K1_M(T,1,0,1); K1_M(T,1,1,0); K1_M(T,1,1,1); }
#define K1_STAGE(T, DM0, DM1, DM2, DM3) { K1_ADDR(T) \
    K1_M((T)-1,0,0,0); K1_RX(T,0,0); K1_RW(T,0,0); K1_M((T)-1,0,0,1); K1_RW(T,0,1); K1_RX(T,0,1); \
    K1_M((T)-1,0,1,0); K1_RX(T,1,0); K1_RW(T,1,0); K1_M((T)-1,0,1,1); K1_RW(T,1,1); K1_RX(T,1,1); \
    K1_M((T)-1,1,0,0); DM0; K1_M((T)-1,1,0,1); DM1; K1_M((T)-1,1,1,0); DM2; K1_M((T)-1,1,1,1); DM3; }
#define K1_STAGE_D(T, S) K1_STAGE(T, K1_D0(S), K1_D1(S), K1_D2(S), K1_D3(S))
  WAITV_BAR(12); STAMP();
  K1_READ(0);
  WAITV_BAR(8); K1_STAGE_D(1, 4);
  WAITV_BAR(8); K1_STAGE_D(2, 5);
  WAITV_BAR(8); K1_STAGE_D(3, 6);
  WAITV_BAR(8); STAMP(); K1_STAGE_D(4, 7);
  WAITV_BAR(8); K1_STAGE(5, , , , );
  WAITV_BAR(4); K1_STAGE(6, , , , );
  K1_MMA(6);
  WAITV_BAR(0);
  K1_READ(7); K1_MMA(7);
  STAMP();
  float* colS = (float*)(lds + L_COLS);
  {
    char* dst = lds + wid * 8192 + lane * 16;
#pragma unroll
    for (int mi = 0; mi < 2; ++mi)
#pragma unroll
      for (int rq = 0; rq < 4; ++rq)
        *(f32x4*)(dst + (mi * 4 + rq) * 1024) = (f32x4){acc[mi][1][4 * rq], acc[mi][1][4 * rq + 1], acc[mi][1][4 * rq + 2], acc[mi][1][4 * rq + 3]};
  }
  LGKM_BAR();
  STAMP();
  f32x4 pa[2][4], cq[4];
  f32x2 st[2];
  u32x2 bq[4];
  {
    const char* src = lds + (wid ^ 1) * 8192 + lane * 16;
    const int fl = 64 * wf + 32 * kh + 4 * h;
#pragma unroll
    for (int rq = 0; rq < 4; ++rq) {
      cq[rq] = *(const f32x4*)(colS + fl + 8 * rq);
      bq[rq] = *(const u32x2*)(lds + L_BIAS + (fl + 8 * rq) * 2);
      pa[0][rq] = *(const f32x4*)(src + rq * 1024); pa[1][rq] = *(const f32x4*)(src + (4 + rq) * 1024);
    }
    st[0] = *(const f32x2*)(lds + L_STAT + (64 * wm + r) * 8); st[1] = *(const f32x2*)(lds + L_STAT + (64 * wm + 32 + r) * 8);
  }
  LGKM_BAR();
  {
    const __amdgpu_buffer_rsrc_t hr = __builtin_amdgcn_make_buffer_rsrc(ws + WS_H, 0, L_ * F_ * 2, 0x00020000);
    const unsigned hoff = (unsigned)(panel * 4 + 2 * wm) * HMT + (8 * fs + 4 * wf + 2 * kh) * 1024 + lane * 16;
#pragma unroll
    for (int mi = 0; mi < 2; ++mi) {
      const float mr = st[mi][0], rinv = st[mi][1];
#pragma unroll
      for (int s2 = 0; s2 < 2; ++s2) {
        u32x4 pk;
#pragma unroll
        for (int qq = 0; qq < 2; ++qq) {
          const int rq = 2 * s2 + qq;
          const float b0 = bflo(bq[rq][0]), b1_ = bfhi(bq[rq][0]), b2_ = bflo(bq[rq][1]), b3 = bfhi(bq[rq][1]);
          const float o0 = gelu_f(fmaf(acc[mi][0][4 * rq] + pa[mi][rq][0], rinv, fmaf(mr, cq[rq][0], b0)));
          const float o1 = gelu_f(fmaf(acc[mi][0][4 * rq + 1] + pa[mi][rq][1], rinv, fmaf(mr, cq[rq][1], b1_)));
          const float o2 = gelu_f(fmaf(acc[mi][0][4 * rq + 2] + pa[mi][rq][2], rinv, fmaf(mr, cq[rq][2], b2_)));
          const float o3 = gelu_f(fmaf(acc[mi][0][4 * rq + 3] + pa[mi][rq][3], rinv, fmaf(mr, cq[rq][3], b3)));
          pk[2 * qq] = pkbf(o0, o1); pk[2 * qq + 1] = pkbf(o2, o3);
        }
        __builtin_amdgcn_raw_buffer_store_b128(pk, hr, hoff + mi * HMT + s2 * 1024, 0, 16);
        SCHED();
      }
    }
  }
  STAMP();
  asm volatile("s_waitcnt vmcnt(0)" ::: "memory");
  if (last_of(lds, C_PUB, 8, lane) && lane < 16) {
    const u64 ac = *(const u64*)(lds + L_ACON + lane * 8);
    int l2 = lane; asm volatile("" : "+v"(l2));
    __hip_atomic_store((u64*)(ws + WS_FLAG + (size_t)blk_of(2 * panel + ((l2 >> 3) & 1), l2 & 7) * 1024 + FL_H) + fs, ac + 1, RLX_AGENT);
  }
  STAMP(); STAMP_FLUSH();
}

#define LN_RS 1040
DI void p1_aux(const u16* __restrict__ x, char* __restrict__ ws, char* lds, int idx, int wid, int lane TK0_ARG) {
  STAMP_DECL STAMP_INIT(0) STAMP0();
  const int t0 = idx * 64;
  u32x4 v[8];
#pragma unroll
  for (int i = 0; i < 8; ++i) v[i] = *(const u32x4*)(x + (size_t)(t0 + 8 * wid + i) * D_ + lane * 8);
  const u64 aCon = __hip_atomic_load((const u64*)(ws + WS_FLAG + (size_t)((wid * 64 + lane) & 255) * 1024), RLX_AGENT);
  const int sfs = lane < 14 ? lane : 0;
  const int sblk = 2 * (idx >> 3) + (sfs >= 7 ? 1 : 0) + 8 * (((idx >> 1) & 3) * 7 + (sfs >= 7 ? sfs - 7 : sfs));
  const u64 aSt = __hip_atomic_load((const u64*)(ws + WS_FLAG + (size_t)sblk * 1024), RLX_AGENT);
  SCHED();
  asm volatile("s_barrier" ::: "memory");
  SCHED();
#pragma unroll
  for (int i = 0; i < 8; ++i) *(u32x4*)(lds + (63 - (8 * wid + i)) * LN_RS + lane * 16) = v[i];
  ARRIVE_L(C_LN1);
  float sA[8], sB[8];
#pragma unroll
  for (int i = 0; i < 8; ++i) {
    float a = dot2(v[i][0], 0x3F803F80u, 0.f), b = dot2(v[i][0], v[i][0], 0.f);
#pragma unroll
    for (int c = 1; c < 4; ++c) { a = dot2(v[i][c], 0x3F803F80u, a); b = dot2(v[i][c], v[i][c], b); }
    sA[i] = a; sB[i] = b;
  }
#pragma unroll
  for (int i = 0; i < 4; ++i) { sA[i] = comb32(sA[i], sA[i + 4]); sB[i] = comb32(sB[i], sB[i + 4]); }
#pragma unroll
  for (int i = 0; i < 2; ++i) { sA[i] = row_allsum(comb16(sA[i], sA[i + 2])); sB[i] = row_allsum(comb16(sB[i], sB[i + 2])); }
  const float mv = ((lane & 1) ? sA[1] : sA[0]) * (1.f / 512.f);
  const float var = fmaxf(fmaf(-mv, mv, ((lane & 1) ? sB[1] : sB[0]) * (1.f / 512.f)), 0.f);
  const float mbv = rbf(mv);
  const float sd = rbf(sqrtf(rbf(rbf(var) + 1e-5f)));
  const float rv = 1.f / sd;
  if ((lane & 14) == 0) {
    const u64 pk = (u64)__float_as_uint(-mbv * rv) | ((u64)__float_as_uint(rv) << 32);
    const int tt = t0 + 8 * wid + (lane & 1) + 2 * (lane >> 4);
    __hip_atomic_store((u64*)(ws + WS_STAT) + tt, pk, RLX_AGENT);
    __hip_atomic_store((unsigned*)(ws + WS_ST2) + (L_ - 1 - tt), (__float_as_uint(mbv) >> 16) | __float_as_uint(sd), RLX_AGENT);
  }
  STAMP();
  await_<0>(lds, C_LN1, 8);
  char* t2 = lds + 64 * LN_RS + wid * 8192;
  const int g = lane >> 4, q = (lane & 15) >> 2, p = lane & 3, dl = lane & 15;
  s16x8 f[8];
#pragma unroll
  for (int i = 0; i < 8; ++i) {
    const char* a = lds + (8 * (4 * (i & 1) + g) + q) * LN_RS + (64 * wid + 16 * (i >> 1) + 4 * p) * 2;
    f[i] = tr_frag(a, a + 4 * LN_RS);
  }
  SCHED();
#pragma unroll
  for (int i = 0; i < 8; ++i) {
    const int dd2 = 16 * (i >> 1) + dl, rc = 4 * (i & 1) + g;
    *(s16x8*)(t2 + dd2 * 128 + ((rc ^ (dd2 & 7)) << 4)) = f[i];
  }
  SCHED();
  const __amdgpu_buffer_rsrc_t ur = __builtin_amdgcn_make_buffer_rsrc(ws + WS_UR, 0, L_ * D_ * 2, 0x00020000);
  const int x0 = L_ - 64 - t0;
  u32x4 o[8];
#pragma unroll
  for (int i = 0; i < 8; ++i) {
    const int dd2 = 8 * i + (lane >> 3), c = lane & 7;
    o[i] = *(const u32x4*)(t2 + dd2 * 128 + ((c ^ (dd2 & 7)) << 4));
  }
  SCHED();
  asm volatile("s_waitcnt vmcnt(0)" ::: "memory");
  if (last_of(lds, C_PUB, 8, lane) && lane < 14) __hip_atomic_store((u64*)(ws + WS_FLAG + (size_t)sblk * 1024 + FL_ST) + (idx & 1), aSt + 1, RLX_AGENT);
  STAMP();
#pragma unroll
  for (int i = 0; i < 8; ++i)
    __builtin_amdgcn_raw_buffer_store_b128(o[i], ur, (unsigned)(64 * wid + 8 * i + (lane >> 3)) * 4096 + x0 * 2 + (lane & 7) * 16, 0, 16);
  STAMP();
  ARRIVE_L(C_X2);
  ARRIVE_V(0, C_LN2);
  if (wid < 4) {
    await_<0>(lds, C_LN2, 8);
    __hip_atomic_store((u64*)(ws + WS_FLAG + (size_t)(wid * 64 + lane) * 1024 + FL_LN) + idx, aCon + 1, RLX_AGENT);
  }
  STAMP(); STAMP_FLUSH();
}

DI void x_wave(const u16* __restrict__ lar, const u16* __restrict__ aim, const u16* __restrict__ cre, const u16* __restrict__ cim,
               unsigned sdt, unsigned sds, const u16* __restrict__ w2, const u16* __restrict__ w1, const u16* __restrict__ b1, int auxidx, int panel, int fs,
               char* __restrict__ ws, char* lds, int mb, int cb, int cw, int lane TK0_ARG) {
  STAMP_DECL STAMP_INIT(1) STAMP0();
  __builtin_amdgcn_s_setprio(3);
  const bool aux = auxidx >= 0;
  const int ch = cw >> 1, hf = cw & 1;
  const int d = 64 * cb + 2 * mb + ch;
  const int i16 = lane & 15, g = lane >> 4;
  unsigned* cnt = (unsigned*)(lds + L_CNT);
  const char* flg = ws + WS_FLAG + (size_t)blockIdx.x * 1024;
  const u16* pp_ = prm_ptr(lar, aim, cre, cim, d, hf, lane);
  unsigned raw0, raw1;
  u64 aS, aH;
  ld_u32(raw0, pp_); ld_u32(raw1, pp_ + 32);
  ld_u64(aS, ws + WS_FLAG + (size_t)(lane >= 32 ? (int)blockIdx.x : blk_of(lane, cb)) * 1024);
  ld_u64(aH, ws + WS_FLAG + (size_t)blk_of(2 * panel + ((lane >> 3) & 1), lane & 7) * 1024);
  if (!aux && cw == 2) asm volatile("s_mov_b32 m0, %2\n\ts_nop 0\n\tglobal_load_lds_dword %0, %1" :: "v"(lane * 4), "s"(b1 + fs * 128), "s"(lds_base(lds) + L_BIAS) : "memory");
  if (cw == 0 && lane < 24) cnt[lane] = 0u;
  if (aux) {
    asm volatile("s_waitcnt lgkmcnt(0)\n\ts_barrier" ::: "memory");
    __builtin_amdgcn_s_setprio(1);
    if (auxidx < 28) {
      const u16* wp_ = w1 + (size_t)(128 * cw + (lane >> 3)) * F_ + 64 * auxidx + 8 * (lane & 7);
      const int cblk = 2 * ((lane & 15) >> 2) + (auxidx >= 14 ? 1 : 0) + 8 * ((lane & 3) * 7 + ((auxidx >> 1) % 7));
      const u64 aC = __hip_atomic_load((const u64*)(ws + WS_FLAG + (size_t)cblk * 1024), RLX_AGENT);
      float a8[8] = {0.f, 0.f, 0.f, 0.f, 0.f, 0.f, 0.f, 0.f};
      u32x4 wv[16];
#pragma unroll
      for (int i = 0; i < 16; ++i) wv[i] = *(const u32x4*)(wp_ + (size_t)(8 * i) * F_);
      SCHED();
#pragma unroll
      for (int i = 0; i < 16; ++i)
#pragma unroll
        for (int c = 0; c < 4; ++c) { a8[2 * c] += bflo(wv[i][c]); a8[2 * c + 1] += bfhi(wv[i][c]); }
#pragma unroll
      for (int i = 0; i < 4; ++i) a8[i] = comb32(a8[i], a8[i + 4]);
#pragma unroll
      for (int i = 0; i < 2; ++i) a8[i] = dpp_add<0x128, 0xf>(comb16(a8[i], a8[i + 2]));
      if (!(lane & 8)) *(f32x2*)(lds + L_COLS + cw * 256 + (8 * (lane & 7) + 2 * (g & 1) + 4 * (g >> 1)) * 4) = (f32x2){a8[0], a8[1]};
      SCHED(); asm volatile("s_waitcnt lgkmcnt(0)" ::: "memory");
      if (last_of(lds, C_CS, 4, lane)) {
        const float* cp = (const float*)(lds + L_COLS);
        const float tot = (cp[lane] + cp[64 + lane]) + (cp[128 + lane] + cp[192 + lane]);
        __hip_atomic_store((float*)(ws + WS_CS) + 64 * auxidx + lane, tot, RLX_AGENT);
        asm volatile("s_waitcnt vmcnt(0)" ::: "memory");
        if (lane < 16) __hip_atomic_store((u64*)(ws + WS_FLAG + (size_t)cblk * 1024 + FL_ST) + 2 + (auxidx & 1), aC + 1, RLX_AGENT);
      }
    }
  }
  asm volatile("s_waitcnt vmcnt(2)" : "+v"(raw0), "+v"(raw1) :: "memory");
  float isc;
  unsigned T[TB_N];
  kgen_tables(T, isc, raw0, raw1, (d & 1) ? bfhi(sdt) : bflo(sdt), lds, cw, lane);
#pragma unroll
  for (int i = 0; i < TB_N; ++i) asm volatile("" : "+v"(T[i]));
  asm volatile("s_waitcnt vmcnt(0)" : "+v"(aS), "+v"(aH) :: "memory");
  const u64 aOwn = rdlane64(aS, 32);
  if (cw == 1 && lane < 16) *(u64*)(lds + L_ACON + lane * 8) = aH;
  if (cw == 3) { if (lane == 0) *(u64*)(lds + L_KSC + 16) = aOwn; ARRIVE_L(C_EP); }
  __builtin_amdgcn_s_setprio(1);
  STAMP();
  const u64* lnfl = (const u64*)(flg + FL_LN) + (lane & 31);
  const char* up = ws + WS_UR + (size_t)d * 4096 + hf * 2048;
  u64 fv;
  if (aux) {
    await_<2>(lds, C_X2, 8);
  } else {
    const u64* stfl = (const u64*)(flg + FL_ST) + (lane & 3);
    const char* stp = cw == 1 ? ws + WS_CS + fs * 512 : ws + WS_STAT + (size_t)panel * 1024;
    const unsigned svo = cw == 1 ? (lane & 31) * 16 : lane * 16;
    u32x4 stv;
#define XBAR() do { SCHED(); asm volatile("s_waitcnt lgkmcnt(0)\n\ts_barrier" ::: "memory"); SCHED(); } while (0)
    if (cw < 2) {
      bool ok = false, got = false;
      XBAR(); XBAR(); XBAR(); XBAR(); XBAR(); XBAR();
      ld_u64(fv, stfl); gload16(stv, svo, stp);
#define XPOLL() do { XBAR(); asm volatile("s_waitcnt vmcnt(0)" : "+v"(fv), "+v"(stv) :: "memory"); \
        if (ok && !got) { *(u32x4*)(lds + (cw == 1 ? L_COLS : L_STAT) + svo) = stv; got = true; } \
        ok = __all(fv == aOwn + 1); SCHED(); ld_u64(fv, stfl); gload16(stv, svo, stp); } while (0)
      XPOLL(); XPOLL();
      asm volatile("s_waitcnt vmcnt(0)" : "+v"(fv), "+v"(stv) :: "memory");
      if (!got) {
        if (!ok) {
          poll_flags(stfl, aOwn + 1);
          asm volatile("global_load_dwordx4 %0, %1, %2 sc1\n\ts_waitcnt vmcnt(0)" : "=&v"(stv) : "v"(svo), "s"(stp) : "memory");
        }
        *(u32x4*)(lds + (cw == 1 ? L_COLS : L_STAT) + svo) = stv;
      }
    } else {
      XBAR(); XBAR(); XBAR(); XBAR(); XBAR(); XBAR(); XBAR(); XBAR();
    }
    XBAR();
    XBAR();
  }
  STAMP();
  __builtin_amdgcn_s_setprio(3);
  K2_SETUP(cw);
  K2_DMA(0); K2_DMA(1);
  char* wl = lds + L_W + ch * WSZ;
  {
    char* tb = wl + hf * 9216;
#pragma unroll
    for (int i = 0; i < TB_N; ++i) *(unsigned*)(tb + i * TB_RS + lane * 4) = T[i];
    const char* tr = tb + g * 16;
    const int rXa = (i16 >> 3) * TB_RS, rXe = (17 + (i16 & 7)) * TB_RS;
    const int rYa = (i16 < 8 ? 2 + (i16 >> 2) : 4 + ((i16 - 8) >> 1)) * TB_RS, rYe = (17 + (i16 < 8 ? 2 * (i16 & 3) : 4 * (i16 & 1))) * TB_RS;
    const int rZa = (i16 < 8 ? 8 + i16 : 16) * TB_RS;
    const int rB1 = (25 + (i16 >> 2)) * TB_RS, rB0 = (29 + (i16 & 3)) * TB_RS;
    f32x4 kX = {0.f, 0.f, 0.f, 0.f}, kY = kX, kZ = kX;
#pragma unroll
    for (int sb = 0; sb < 4; sb += 2) {
      u32x4 t_[2][7];
#pragma unroll
      for (int s = 0; s < 2; ++s) {
        const char* tq = tr + (sb + s) * 64;
        t_[s][0] = *(const u32x4*)(tq + rB1); t_[s][1] = *(const u32x4*)(tq + rB0); t_[s][2] = *(const u32x4*)(tq + rXa); t_[s][3] = *(const u32x4*)(tq + rXe);
        t_[s][4] = *(const u32x4*)(tq + rYa); t_[s][5] = *(const u32x4*)(tq + rYe); t_[s][6] = *(const u32x4*)(tq + rZa);
      }
      SCHED();
#pragma unroll
      for (int s = 0; s < 2; ++s) {
        const f16x8 hb = __builtin_bit_cast(f16x8, cmulh4(t_[s][0], t_[s][1]));
        kX = __builtin_amdgcn_mfma_f32_16x16x32_f16(__builtin_bit_cast(f16x8, cmulh4(t_[s][2], t_[s][3])), hb, kX, 0, 0, 0);
        kY = __builtin_amdgcn_mfma_f32_16x16x32_f16(__builtin_bit_cast(f16x8, cmulh4(t_[s][4], t_[s][5])), hb, kY, 0, 0, 0);
        kZ = __builtin_amdgcn_mfma_f32_16x16x32_f16(__builtin_bit_cast(f16x8, t_[s][6]), hb, kZ, 0, 0, 0);
      }
      SCHED();
    }
    kgen_store(kX, kY, kZ, isc, lds, cw, lane);
  }
  ARRIVE_L(C_K0 + ch);
  ld_u64(fv, lnfl);
  await_<0>(lds, C_K0 + ch, 2);
  const f32x2 ks = *(const f32x2*)(lds + L_KSC + ch * 8);
  int ex = (int)((__float_as_uint(ks[0] + ks[1]) >> 23) & 255);
  ex = ex < 20 ? 20 : (ex > 230 ? 230 : ex);
  const float scale = __uint_as_float((unsigned)(267 - ex) << 23);
  const float iscale = __uint_as_float((unsigned)(ex - 13) << 23);
  char* kp = lds + L_KP + ch * KPSZ;
  {
    const float* k0 = (const float*)(lds + L_KAP + (2 * ch) * KAPSZ);
#define KSUM2(T, i) ((*(const T*)(k0 + (i)) + *(const T*)(k0 + 644 + (i))) * scale)
    const int A = 64 * hf + lane, A2 = 128 + A;
    const float e0 = KSUM2(float, 512 + A), e1 = KSUM2(float, 513 + A);
    u32x4 o;
    if (hf == 0) {
      const int i0 = lane < 32 ? 8 * A : 256 + 4 * (A - 32);
      const f32x4 a = KSUM2(f32x4, i0), b = KSUM2(f32x4, i0 + 4);
      const bool lo = lane < 32;
      o = (u32x4){pkh(a[0], lo ? a[1] : a[0]), pkh(lo ? a[2] : a[1], lo ? a[3] : a[2]), pkh(lo ? b[0] : a[2], lo ? b[1] : a[2]), pkh(lo ? b[2] : a[3], lo ? b[3] : b[0])};
      if (lane < 2) *(u32x4*)(kp + lane * 16) = (u32x4){0u, 0u, 0u, 0u};
    } else {
      const int i0 = 384 + 2 * lane;
      const f32x2 a = KSUM2(f32x2, i0), b = KSUM2(f32x2, i0 + 2);
      o = (u32x4){pkh(a[0], a[0]), pkh(a[0], a[1]), pkh(a[1], a[1]), pkh(b[0], b[0])};
    }
    *(u32x4*)(kp + 32 + A * 16) = o;
    const unsigned p00 = pkh(e0, e0);
    *(u32x4*)(kp + 32 + A2 * 16) = (u32x4){p00, p00, pkh((A2 & 1) ? e1 : e0, e1), pkh(e1, e1)};
  }
  ARRIVE_L(C_K1);
  asm volatile("s_waitcnt vmcnt(0)" : "+v"(fv) :: "memory");
  arrive(lds, C_W2, lane);
  const bool urok = __all(fv == aOwn + 1);
  u32x4 ua_, ub_, q0, q1, q2, q3;
  const char* qp = ws + WS_ST2 + hf * 4096;
#define U_LOADS() { gload16(ua_, lane * 16, up); gload16(ub_, lane * 16, up + 1024); \
    gload16(q0, lane * 32, qp); gload16(q1, lane * 32, qp + 16); gload16(q2, lane * 32, qp + 2048); gload16(q3, lane * 32, qp + 2064); }
#define U_WAIT() asm volatile("s_waitcnt vmcnt(0)" : "+v"(ua_), "+v"(ub_), "+v"(q0), "+v"(q1), "+v"(q2), "+v"(q3) :: "memory")
  U_LOADS();
  await_<0>(lds, C_K1, 4);
  STAMP();
#pragma unroll
  for (int rnd = 0; rnd < 3; ++rnd) {
    const int a = rnd < 2 ? 128 * rnd + 64 * hf + lane : 256;
    if (rnd == 2 && !(hf == 0 && lane == 0)) break;
    const u32x4 c0 = *(const u32x4*)(kp + a * 16), c1 = *(const u32x4*)(kp + a * 16 + 16);
    const unsigned e[8] = {c0[0], c0[1], c0[2], c0[3], c1[0], c1[1], c1[2], c1[3]};
    unsigned o[7];
#pragma unroll
    for (int i = 0; i < 7; ++i) o[i] = __builtin_amdgcn_alignbit(e[i + 1], e[i], 16);
    const int m = (a & 1) | (((a >> 2) & 1) << 1) | (((a >> 1) & 1) << 2);
    char* base = wl + a * 128;
#pragma unroll
    for (int c = 0; c < 8; ++c) {
      u32x4 wv;
      if (c & 1) { const int s = (c + 1) >> 1; wv = (u32x4){e[s], e[s + 1], e[s + 2], e[s + 3]}; }
      else { const int s = c >> 1; wv = (u32x4){o[s], o[s + 1], o[s + 2], o[s + 3]}; }
      *(u32x4*)(base + ((c ^ m) << 4)) = wv;
    }
  }
  SCHED();
  U_WAIT();
  if (!urok) { poll_flags(lnfl, aOwn + 1); U_LOADS(); U_WAIT(); }
  char* ul = lds + L_U + ch * USZ;
  {
#define UN2(xw, s0, s1) ({ const unsigned a_ = pkbf(bflo(xw) - bflo(s0), bfhi(xw) - bflo(s1)); \
      const unsigned b_ = pkbf(bflo(a_) * __builtin_amdgcn_rcpf(bfhi(s0)), bfhi(a_) * __builtin_amdgcn_rcpf(bfhi(s1))); pkh(bflo(b_), bfhi(b_)); })
    const u32x4 o0 = {UN2(ua_[0], q0[0], q0[1]), UN2(ua_[1], q0[2], q0[3]), UN2(ua_[2], q1[0], q1[1]), UN2(ua_[3], q1[2], q1[3])};
    const u32x4 o1 = {UN2(ub_[0], q2[0], q2[1]), UN2(ub_[1], q2[2], q2[3]), UN2(ub_[2], q3[0], q3[1]), UN2(ub_[3], q3[2], q3[3])};
    *(u32x4*)(ul + hf * 2048 + lane * 16) = o0;
    *(u32x4*)(ul + hf * 2048 + 1024 + lane * 16) = o1;
  }
  if (hf == 0 && lane < 32) *(u32x4*)(ul + 4096 + lane * 16) = (u32x4){0u, 0u, 0u, 0u};
  ARRIVE_L(C_K2 + ch);
  STAMP();
  await_<2>(lds, C_PUB, 8);
  await_<0>(lds, C_K2 + ch, 2);
  __builtin_amdgcn_s_setprio(3);
  STAMP();
  f32x4 acc[8];
#pragma unroll
  for (int i = 0; i < 8; ++i) acc[i] = (f32x4){0.f, 0.f, 0.f, 0.f};
  {
    const int c = i16 + 8 * g;
    int fo[2];
#pragma unroll
    for (int par = 0; par < 2; ++par) {
      const int w = 32 * par + c;
      const int m = ((w >> 3) & 1) | (((w >> 5) & 1) << 1) | (((w >> 4) & 1) << 2);
      fo[par] = (w ^ m) << 4;
    }
    const char* fbase = wl + hf * (128 * 16);
    const char* gbase = ul + (2032 - 16 * i16 + 8 * g + 128 * hf) * 2 - 7 * 512;
#define CF(s, e) (*(const f16x8*)(fbase + fo[(s) & 1] + ((s) >> 1) * 1024 + (e) * 4096))
#define CG(s, mu) (*(const f16x8*)(gbase + (s) * 64 + (7 - (mu)) * 512))
    f16x8 F[2][8], G[2];
#pragma unroll
    for (int e = 0; e < 8; ++e) F[0][e] = CF(0, e);
    G[0] = CG(0, 0);
#pragma unroll
    for (int s = 0; s < 4; ++s) {
#pragma unroll
      for (int mu = 0; mu < 8; ++mu) {
        const int it = s * 8 + mu;
        SCHED();
        if (it + 1 < 32) G[(it + 1) & 1] = CG((it + 1) >> 3, (it + 1) & 7);
        if (s < 3) F[(s + 1) & 1][mu] = CF(s + 1, mu);
        SCHED();
#pragma unroll
        for (int e = 0; e + mu < 8; ++e) acc[e + mu] = __builtin_amdgcn_mfma_f32_16x16x32_f16(F[s & 1][e], G[it & 1], acc[e + mu], 0, 0, 0);
      }
    }
  }
  STAMP();
  ARRIVE_L(C_K1 + 1 + ch); await_<0>(lds, C_K1 + 1 + ch, 2);
  {
    char* dst = wl + hf * 4096 + lane * 16;
#pragma unroll
    for (int i = 0; i < 4; ++i) *(f32x4*)(dst + i * 1024) = sel(hf, acc[i], acc[4 + i]);
  }
  ARRIVE_L(C_K3 + ch); await_<0>(lds, C_K3 + ch, 2);
  STAMP();
  {
    const float dskip = (d & 1) ? bfhi(sds) : bflo(sds);
    const char* src = wl + (hf ^ 1) * 4096 + lane * 16;
    char* sdst = ws + WS_S + (size_t)d * 4096;
#pragma unroll
    for (int i = 0; i < 4; ++i) {
      const f32x4 yv = (sel(hf, acc[4 + i], acc[i]) + *(const f32x4*)(src + i * 1024)) * iscale;
      const int t = 256 * (4 * hf + i) + 16 * i16 + 4 * g;
      const u32x2 uu = *(const u32x2*)(ul + (2044 - t) * 2);
      const unsigned uu0 = uu[0], uu1 = uu[1];
      const f16x2 ua = __builtin_bit_cast(f16x2, uu0), ub = __builtin_bit_cast(f16x2, uu1);
      const unsigned y01 = pkbf(yv[0], yv[1]), y23 = pkbf(yv[2], yv[3]);
      const unsigned k01 = pkbf((float)ub[1] * dskip, (float)ub[0] * dskip), k23 = pkbf((float)ua[1] * dskip, (float)ua[0] * dskip);
      const u64 pk = (u64)pkbf(bflo(y01) + bflo(k01), bfhi(y01) + bfhi(k01)) | ((u64)pkbf(bflo(y23) + bflo(k23), bfhi(y23) + bfhi(k23)) << 32);
      __hip_atomic_store((u64*)(sdst + t * 2), pk, RLX_AGENT);
    }
  }
  asm volatile("s_waitcnt vmcnt(0) lgkmcnt(0)" ::: "memory");
  STAMP();
  if (last_of(lds, C_SP, 4, lane) && lane < 32) __hip_atomic_store((u64*)(ws + WS_FLAG + (size_t)blk_of(lane, cb) * 1024 + FL_S) + mb, aS + 1, RLX_AGENT);
  STAMP_FLUSH();
}

#define FIN_LOADS() { \
  asm volatile("global_load_dwordx4 %0, %1, off" : "=v"(xv) : "v"(x + (size_t)(64 * mb + (tid >> 3)) * D_ + 64 * cb + 8 * (tid & 7)) : "memory"); \
  asm volatile("global_load_dwordx4 %0, %1, off" : "=v"(bv) : "v"(b2 + 64 * cb + 8 * (tid & 7)) : "memory"); }
DI void final_stage(u32x4& xv, u32x4& bv, u16* __restrict__ out, char* lds, int mb, int cb, int tid) {
  await_<0>(lds, C_R1, 4);
  await_<0>(lds, C_ST, 4);
  const int t = tid >> 3, dq = tid & 7;
  const char* src = lds + L_RED + t * RED_RS + dq * 32;
  f32x4 ra[4], rb[4];
#pragma unroll
  for (int k = 0; k < 4; ++k) { ra[k] = *(const f32x4*)(src + k * 64 * RED_RS); rb[k] = *(const f32x4*)(src + k * 64 * RED_RS + 16); }
  const u32x4 sv = *(const u32x4*)(lds + L_ST + t * ST_RS + dq * 16);
  SCHED();
  const f32x4 a = (ra[0] + ra[1]) + (ra[2] + ra[3]), b = (rb[0] + rb[1]) + (rb[2] + rb[3]);
  asm volatile("s_waitcnt vmcnt(0)" : "+v"(xv), "+v"(bv) :: "memory");
  u32x4 o;
#pragma unroll
  for (int c = 0; c < 4; ++c) {
    const float v0 = (c < 2 ? a[2 * c] : b[2 * c - 4]) + bflo(bv[c]), v1 = (c < 2 ? a[2 * c + 1] : b[2 * c - 3]) + bfhi(bv[c]);
    const float g0 = v0 * __builtin_amdgcn_rcpf(1.f + __builtin_amdgcn_exp2f(-1.4426950408889634f * v0));
    const float g1 = v1 * __builtin_amdgcn_rcpf(1.f + __builtin_amdgcn_exp2f(-1.4426950408889634f * v1));
    const unsigned pv = pkbf(bflo(sv[c]) * g0, bfhi(sv[c]) * g1);
    o[c] = pkbf(bflo(xv[c]) + bflo(pv), bfhi(xv[c]) + bfhi(pv));
  }
  *(u32x4*)(out + (size_t)(64 * mb + t) * D_ + 64 * cb + 8 * dq) = o;
}
DI void p2_gemm(const u16* __restrict__ x, const u16* __restrict__ w2, u64 aOwn, const u16* __restrict__ b2, u16* __restrict__ out, char* __restrict__ ws,
                char* lds, int mb, int cb, int kq, int lane TK0_ARG) {
  STAMP_DECL STAMP_INIT(1) STAMP0();
  const int r = lane & 31, h = lane >> 5;
  unsigned* cnt = (unsigned*)(lds + L_CNT);
  const int tid = kq * 64 + lane;
  K2_SETUP(kq);
  char* ring = lds + L_RING2 + kq * (K2_R * 4096);
  const char* flg = ws + WS_FLAG + (size_t)blockIdx.x * 1024;
  const char* hbase = ws + WS_H + (size_t)(2 * mb) * HMT + 28 * kq * 1024;
  const unsigned hoff0 = lane * 16, hoff1 = lane * 16 + HMT;
  const int q = (lane & 15) >> 2;
  const int toff0 = (4 * h + q) * 128 + ((q >> 1) << 6) + ((lane >> 4) & 1) * 32 + 8 * (lane & 3), toff1 = toff0 ^ 64;
  u32x4 hb[3][4];
  s16x8 wf[2][4];
  f32x16 acc[2][2];
#pragma unroll
  for (int i = 0; i < 16; ++i) { acc[0][0][i] = 0.f; acc[0][1][i] = 0.f; acc[1][0][i] = 0.f; acc[1][1][i] = 0.f; }
  await_<1>(lds, C_PUB, 8);
  K2_DMA(2); K2_DMA(3);
  STAMP();
  if (kq == 0) {
    const u64* fp = (const u64*)(flg + FL_H) + (lane < 14 ? lane : 0);
    for (unsigned spin = 0; spin < (1u << 20); ++spin) {
      u64 v;
      asm volatile("global_load_dwordx2 %0, %1, off sc1\n\ts_waitcnt vmcnt(0)" : "=&v"(v) : "v"(fp) : "memory");
      if (__all(v == aOwn + 1)) break;
      __builtin_amdgcn_s_sleep(2);
    }
    lds_set(cnt + C_H, lane);
  } else {
    lds_wait(cnt + C_H);
  }
  STAMP();
#define K2_HL(S, s2) { const char* hs_ = hbase + K2S(S) * 2048 + (s2) * 1024; gload16(hb[(S) % 3][2 * (s2)], hoff0, hs_); gload16(hb[(S) % 3][2 * (s2) + 1], hoff1, hs_); SCHED(); }
#define K2_HLD(S) { K2_HL(S, 0); K2_HL(S, 1); }
#define K2_TR(S, i) { const char* sl_ = ring + ((S) % K2_R) * 4096 + ((i) >> 1) * 2048 + (((i) & 1) ? toff1 : toff0); wf[(S) & 1][i] = tr_frag(sl_, sl_ + 1024); SCHED(); }
#define K2_D1(S, pp) { dma16s(wvoff, wsrc + (size_t)(K2S(S) * 32 + (pp) * 8) * D_, ringa + ((S) % K2_R) * 4096 + (pp) * 1024); SCHED(); }
#define K2_M(S, s2, mi, di) { acc[mi][di] = __builtin_amdgcn_mfma_f32_32x32x16_bf16(wf[(S) & 1][2 * (s2) + (di)], __builtin_bit_cast(s16x8, hb[(S) % 3][2 * (s2) + (mi)]), acc[mi][di], 0, 0, 0); SCHED(); }
#define K2_STEP(S, W, TRN, HN, DN) { \
    asm volatile("s_waitcnt vmcnt(" #W ")" : "+v"(hb[(S) % 3][0]), "+v"(hb[(S) % 3][1]), "+v"(hb[(S) % 3][2]), "+v"(hb[(S) % 3][3]) :: "memory"); SCHED(); \
    K2_M(S,0,0,0); if (TRN) K2_TR((S)+1,0); K2_M(S,0,1,0); if (TRN) K2_TR((S)+1,1); K2_M(S,0,0,1); if (TRN) K2_TR((S)+1,2); K2_M(S,0,1,1); if (TRN) K2_TR((S)+1,3); \
    K2_M(S,1,0,0); if (DN) { K2_D1((S)+4,0); K2_D1((S)+4,1); } K2_M(S,1,1,0); if (DN) { K2_D1((S)+4,2); K2_D1((S)+4,3); } K2_M(S,1,0,1); if (HN) K2_HL((S)+3,0); K2_M(S,1,1,1); if (HN) K2_HL((S)+3,1); }
  K2_HLD(0); K2_HLD(1); K2_HLD(2);
  await_<0>(lds, C_W2, 4);
  K2_TR(0,0); K2_TR(0,1); K2_TR(0,2); K2_TR(0,3);
  K2_STEP(0, 8, 1, 1, 1); STAMP();
  K2_STEP(1, 12, 1, 1, 1);
  K2_STEP(2, 16, 1, 1, 1);
  K2_STEP(3, 16, 1, 1, 1);
  K2_STEP(4, 16, 1, 1, 1);
  K2_STEP(5, 16, 1, 1, 1);
  K2_STEP(6, 16, 1, 1, 1);
  K2_STEP(7, 16, 1, 1, 1); STAMP();
  K2_STEP(8, 16, 1, 1, 1);
  if (kq == 0) lds_set(cnt + C_G8, lane);
  K2_STEP(9, 16, 1, 1, 1);
  K2_STEP(10, 16, 1, 1, 0);
  K2_STEP(11, 12, 1, 0, 0);
  K2_STEP(12, 4, 1, 0, 0);
  K2_STEP(13, 0, 0, 0, 0);
  STAMP();
  await_<0>(lds, C_SP, 4);
  {
    char* dst = lds + L_RED + (kq * 64 + r) * RED_RS + h * 16;
#pragma unroll
    for (int mi = 0; mi < 2; ++mi)
#pragma unroll
      for (int di = 0; di < 2; ++di)
#pragma unroll
        for (int rq = 0; rq < 4; ++rq)
          *(f32x4*)(dst + mi * 32 * RED_RS + di * 128 + rq * 32) = (f32x4){acc[mi][di][4 * rq], acc[mi][di][4 * rq + 1], acc[mi][di][4 * rq + 2], acc[mi][di][4 * rq + 3]};
  }
  ARRIVE_L(C_R1);
  STAMP();
  await_<0>(lds, C_ST, 4);
  u32x4 xv = *(const u32x4*)(lds + L_XT + tid * 16), bv = *(const u32x4*)(lds + L_XT + 4096 + (tid & 7) * 16);
  final_stage(xv, bv, out, lds, mb, cb, tid);
  if (tid == 0) __hip_atomic_store((u64*)(ws + WS_FLAG + (size_t)blockIdx.x * 1024), aOwn + 1, RLX_AGENT);
  STAMP(); STAMP_FLUSH();
}

DI void p2_helper(const u16* __restrict__ x, const u16* __restrict__ w2, u64 aOwn, const u16* __restrict__ b2, u16* __restrict__ out, char* __restrict__ ws,
                  char* lds, int mb, int cb, int gw, int lane) {
  unsigned* cnt = (unsigned*)(lds + L_CNT);
  const int tid = gw * 64 + lane, hw = gw - 4;
  u32x4 xv, bv, x2;
  await_<2>(lds, C_PUB, 8);
  FIN_LOADS();
  asm volatile("global_load_dwordx4 %0, %3, off\n\ts_waitcnt vmcnt(0)" : "=&v"(x2), "+v"(xv), "+v"(bv) : "v"(x + (size_t)(64 * mb + ((tid - 256) >> 3)) * D_ + 64 * cb + 8 * (tid & 7)) : "memory");
  *(u32x4*)(lds + L_XT + (tid - 256) * 16) = x2;
  if (tid < 264) *(u32x4*)(lds + L_XT + 4096 + (tid & 7) * 16) = bv;
  if (hw == 0) {
    await_<8>(lds, C_SP, 4);
    while (__hip_atomic_load(cnt + C_G8, RLX_WG) == 0u) __builtin_amdgcn_s_sleep(8);
    const u64* fp = (const u64*)(ws + WS_FLAG + (size_t)blockIdx.x * 1024 + FL_S) + (lane & 31);
    for (unsigned spin = 0; spin < (1u << 18); ++spin) {
      const u64 v = __hip_atomic_load(fp, RLX_AGENT);
      if (__all(v == aOwn + 1)) break;
      __builtin_amdgcn_s_sleep(8);
    }
    asm volatile("" ::: "memory");
    lds_set(cnt + C_S, lane);
  } else {
    while (__hip_atomic_load(cnt + C_S, RLX_WG) == 0u) __builtin_amdgcn_s_sleep(4);
    asm volatile("" ::: "memory");
  }
  u32x4 sa, sb;
  const char* sp = ws + WS_S + (size_t)(64 * cb + 16 * hw) * 4096 + 64 * mb * 2;
  gload16(sa, (lane >> 3) * 4096 + (lane & 7) * 16, sp);
  gload16(sb, (lane >> 3) * 4096 + (lane & 7) * 16, sp + 8 * 4096);
  asm volatile("s_waitcnt vmcnt(0)" : "+v"(sa), "+v"(sb) :: "memory");
  char* dst = lds + L_ST + (8 * (lane & 7)) * ST_RS + (16 * hw + (lane >> 3)) * 2;
#pragma unroll
  for (int c = 0; c < 4; ++c) {
    *(u16*)(dst + (2 * c) * ST_RS) = (u16)sa[c]; *(u16*)(dst + (2 * c + 1) * ST_RS) = (u16)(sa[c] >> 16);
    *(u16*)(dst + (2 * c) * ST_RS + 16) = (u16)sb[c]; *(u16*)(dst + (2 * c + 1) * ST_RS + 16) = (u16)(sb[c] >> 16);
  }
  ARRIVE_L(C_ST);
  while (__hip_atomic_load(cnt + C_R1, RLX_WG) < 4u) __builtin_amdgcn_s_sleep(2);
  final_stage(xv, bv, out, lds, mb, cb, tid);
}

template <int PH>
__global__ __launch_bounds__(768, 3) void fused_kernel(const u16* __restrict__ x, const u16* __restrict__ lar, const u16* __restrict__ aim,
                                                       const u16* __restrict__ cre, const u16* __restrict__ cim, const u16* __restrict__ ldt,
                                                       const u16* __restrict__ dsk, const u16* __restrict__ w1, const u16* __restrict__ b1,
                                                       const u16* __restrict__ w2, const u16* __restrict__ b2, u16* __restrict__ out,
                                                       char* __restrict__ ws) {
  extern __shared__ __attribute__((aligned(16))) char lds[];
  const int lane = threadIdx.x & 63;
  const int wid = __builtin_amdgcn_readfirstlane(threadIdx.x >> 6);
  const int b = blockIdx.x, xcd = b & 7, j = b >> 3;
  const int mb = 4 * xcd + (j >> 3), cb = j & 7;
  const int jj = j < 28 ? j : 0;
  const int panel = 4 * (xcd >> 1) + jj / 7, fs = 7 * (xcd & 1) + jj % 7;
  if (wid < 4) {
    const int de = 64 * cb + 2 * mb;
    const unsigned sdt = *(const __attribute__((address_space(4))) unsigned*)(ldt + de);
    const unsigned sds = *(const __attribute__((address_space(4))) unsigned*)(dsk + de);
    x_wave(lar, aim, cre, cim, sdt, sds, w2, w1, b1, j >= 28 ? xcd * 4 + (j - 28) : -1, panel, fs, ws, lds, mb, cb, wid, lane TK0_PASS);
    return;
  }
  const int gw = wid - 4;
  __builtin_amdgcn_s_setprio(2);
  if (j < 28) p1_gemm(x, w1, ws, lds, panel, fs, gw, lane TK0_PASS);
  else p1_aux(x, ws, lds, xcd * 4 + (j - 28), gw, lane TK0_PASS);
  __builtin_amdgcn_s_setprio(0);
  await_<0>(lds, C_EP, 1);
  const u64 aOwn = *(const u64*)(lds + L_KSC + 16);
  if (gw < 4) p2_gemm(x, w2, aOwn, b2, out, ws, lds, mb, cb, gw, lane TK0_PASS);
  else p2_helper(x, w2, aOwn, b2, out, ws, lds, mb, cb, gw, lane);
}

extern "C" void kernel_launch(void* const* d_in, const int* in_sizes, int n_in, void* d_out, int out_size, void* d_ws, size_t ws_size,
                              hipStream_t stream) {
  const u16* x = (const u16*)d_in[0];
  const u16* lar = (const u16*)d_in[1];
  const u16* aim = (const u16*)d_in[2];
  const u16* cre = (const u16*)d_in[3];
  const u16* cim = (const u16*)d_in[4];
  const u16* ldt = (const u16*)d_in[5];
  const u16* dsk = (const u16*)d_in[6];
  const u16* w1 = (const u16*)d_in[8];
  const u16* b1 = (const u16*)d_in[9];
  const u16* w2 = (const u16*)d_in[10];
  const u16* b2 = (const u16*)d_in[11];
  (void)hipFuncSetAttribute((const void*)fused_kernel<3>, hipFuncAttributeMaxDynamicSharedMemorySize, LDS_TOTAL);
  fused_kernel<3><<<256, 768, LDS_TOTAL, stream>>>(x, lar, aim, cre, cim, ldt, dsk, w1, b1, w2, b2, (u16*)d_out, (char*)d_ws);
}
```

```cpp
#include <hip/hip_runtime.h>
#include <stdint.h>

typedef unsigned short u16;
typedef unsigned long long u64;
typedef __attribute__((ext_vector_type(4))) short s16x4;
typedef __attribute__((ext_vector_type(8))) short s16x8;
typedef __attribute__((ext_vector_type(8))) _Float16 f16x8;
typedef __attribute__((ext_vector_type(2))) _Float16 f16x2;
typedef __attribute__((ext_vector_type(2))) __bf16 bf16x2;
typedef __attribute__((ext_vector_type(2))) float f32x2;
typedef __attribute__((ext_vector_type(4))) float f32x4;
typedef __attribute__((ext_vector_type(16))) float f32x16;
typedef __attribute__((ext_vector_type(4))) unsigned u32x4;
typedef __attribute__((ext_vector_type(2))) unsigned u32x2;
#define LDSP __attribute__((address_space(3)))
#define GLBP __attribute__((address_space(1)))
#define DI __device__ __forceinline__

#define NLAUNCH 1

#define XPRIO 3
#define L_ 2048
#define D_ 512
#define N_ 128
#define F_ 1792

#define WS_H 0
#define WS_UR (8u << 20)
#define WS_S (10u << 20)
#define WS_STAT (12u << 20)
#define WS_CS ((12u << 20) + 65536)
#define WS_ST2 ((12u << 20) + 131072)
#define WS_FLAG (13u << 20)
#define WS_EP (14u << 20)
#define WS_KAP (16u << 20)
#define FL_LN 128
#define FL_H 384
#define FL_S 512
#define FL_ST 768

#define STAMP_INIT(kid)
#define STAMP()
#define STAMP0()
#define STAMP_DECL
#define STAMP_FLUSH()
#define TK0_ARG
#define TK0_PASS

DI float bf2f(unsigned v) { return __uint_as_float(v << 16); }
DI float bflo(unsigned v) { return __uint_as_float(v << 16); }
DI float bfhi(unsigned v) { return __uint_as_float(v & 0xffff0000u); }
DI unsigned pkbf(float a, float b) { bf16x2 q = {(__bf16)a, (__bf16)b}; return __builtin_bit_cast(unsigned, q); }
DI float rbf(float a) { __bf16 q = (__bf16)a; return (float)q; }
DI unsigned pkh(float a, float b) { f32x2 v = {a, b}; f16x2 p = __builtin_convertvector(v, f16x2); return __builtin_bit_cast(unsigned, p); }
DI float dot2(unsigned a, unsigned b, float c) { return __builtin_amdgcn_fdot2_f32_bf16(__builtin_bit_cast(bf16x2, a), __builtin_bit_cast(bf16x2, b), c, false); }
template <class T> DI T sel(int c, T a, T b) { return c ? a : b; }
DI void dma16(const void* g, char* l) { __builtin_amdgcn_global_load_lds((const GLBP unsigned*)g, (LDSP unsigned*)l, 16, 0, 0); }
DI void dma16s(unsigned voff, const void* sbase, unsigned ldsaddr) {
  asm volatile("s_mov_b32 m0, %2\n\ts_nop 0\n\tglobal_load_lds_dwordx4 %0, %1" :: "v"(voff), "s"(sbase), "s"(ldsaddr) : "memory");
}
DI unsigned lds_base(char* lds) { return __builtin_amdgcn_readfirstlane((unsigned)(size_t)(LDSP char*)lds); }
DI s16x8 tr_frag(const char* p0, const char* p1) {
  s16x4 a = __builtin_amdgcn_ds_read_tr16_b64_v4i16((LDSP s16x4*)p0);
  s16x4 b = __builtin_amdgcn_ds_read_tr16_b64_v4i16((LDSP s16x4*)p1);
  return __builtin_shufflevector(a, b, 0, 1, 2, 3, 4, 5, 6, 7);
}
#define SCHED() __builtin_amdgcn_sched_barrier(0)
#define WAITV_BAR(n) do { SCHED(); asm volatile("s_waitcnt vmcnt(" #n ") lgkmcnt(0)\n\ts_barrier" ::: "memory"); SCHED(); } while (0)
#define LGKM_BAR() do { SCHED(); asm volatile("s_waitcnt lgkmcnt(0)\n\ts_barrier" ::: "memory"); SCHED(); } while (0)
#define RLX_AGENT __ATOMIC_RELAXED, __HIP_MEMORY_SCOPE_AGENT
#define RLX_WG __ATOMIC_RELAXED, __HIP_MEMORY_SCOPE_WORKGROUP

template <int CTRL, int RM> DI float dpp_add(float v) {
  return v + __builtin_bit_cast(float, __builtin_amdgcn_update_dpp(0, __builtin_bit_cast(int, v), CTRL, RM, 0xf, true));
}
DI float wave_sum63(float v) {
  v = dpp_add<0x111, 0xf>(v); v = dpp_add<0x112, 0xf>(v); v = dpp_add<0x114, 0xf>(v); v = dpp_add<0x118, 0xf>(v);
  v = dpp_add<0x142, 0xa>(v); v = dpp_add<0x143, 0xc>(v);
  return v;
}
DI float comb32(float a, float b) { auto r = __builtin_amdgcn_permlane32_swap(__float_as_uint(a), __float_as_uint(b), false, false); return __uint_as_float(r[0]) + __uint_as_float(r[1]); }
DI float comb16(float a, float b) { auto r = __builtin_amdgcn_permlane16_swap(__float_as_uint(a), __float_as_uint(b), false, false); return __uint_as_float(r[0]) + __uint_as_float(r[1]); }
DI float row_allsum(float v) { v = dpp_add<0x128, 0xf>(v); v = dpp_add<0x124, 0xf>(v); v = dpp_add<0x122, 0xf>(v); v = dpp_add<0x121, 0xf>(v); return v; }
DI float rdlane(float v, int l) { return __builtin_bit_cast(float, __builtin_amdgcn_readlane(__builtin_bit_cast(int, v), l)); }

DI void group_barrier(unsigned* cnt, unsigned target, int lane) {
  asm volatile("s_waitcnt lgkmcnt(0)" ::: "memory");
  if (lane == 0) __hip_atomic_fetch_add(cnt, 1u, RLX_WG);
  while (__hip_atomic_load(cnt, RLX_WG) < target) __builtin_amdgcn_s_sleep(1);
  asm volatile("" ::: "memory");
}
DI void poll_flags(const u64* p, u64 expect) {
  for (unsigned spin = 0; spin < (1u << 20); ++spin) {
    const u64 v = __hip_atomic_load(p, RLX_AGENT);
    if (__all(v == expect)) break;
    __builtin_amdgcn_s_sleep(2);
  }
  asm volatile("" ::: "memory");
}
DI void lds_set(unsigned* w, int lane) { if (lane == 0) __hip_atomic_store(w, 1u, RLX_WG); }
DI void lds_wait(unsigned* w) { while (__hip_atomic_load(w, RLX_WG) == 0u) __builtin_amdgcn_s_sleep(1); asm volatile("" ::: "memory"); }
DI int blk_of(int mb, int cb) { return (mb >> 2) + 8 * (((mb & 3) << 3) | cb); }

#define LDS_TOTAL 163840
#define L_STAT 162304
#define L_BIAS 163328
#define L_ACON 163584
#define L_KSC 163712
#define L_CNT 163744
#define KAPSZ 2576
#define L_KAP 141696
#define L_COLS 152000
#define K1_SLOT 32768
#define L_TB 98304
#define L_KP 131584
#define KPSZ 4160
#define K2_R 4
#define L_RING2 0
#define L_W 65536
#define WSZ 33024
#define L_U L_KAP
#define USZ 4608
#define L_RED L_W
#define RED_RS 272
#define L_ST L_KAP
#define ST_RS 144
#define C_PUB 0
#define C_SP 1
#define C_H 2
#define C_S 3
#define C_W2 4
#define C_R1 5
#define C_ST 6
#define C_K0 7
#define C_K1 9
#define C_K2 12
#define C_K3 14
#define C_LN1 16
#define C_LN2 17
#define C_X2 18
#define C_CS 19
#define C_EP 20
#define C_G8 21
#define L_XT 153088
DI void arrive(char* lds, int c, int lane) { if (lane == 0) __hip_atomic_fetch_add((unsigned*)(lds + L_CNT) + c, 1u, RLX_WG); }
template <int SL> DI void await_(char* lds, int c, unsigned target) {
  while (__hip_atomic_load((unsigned*)(lds + L_CNT) + c, RLX_WG) < target) __builtin_amdgcn_s_sleep(SL);
  asm volatile("" ::: "memory"); SCHED();
}
#define ARRIVE_V(n, c) do { SCHED(); asm volatile("s_waitcnt vmcnt(" #n ") lgkmcnt(0)" ::: "memory"); arrive(lds, c, lane); SCHED(); } while (0)
#define ARRIVE_L(c) do { SCHED(); asm volatile("s_waitcnt lgkmcnt(0)" ::: "memory"); arrive(lds, c, lane); SCHED(); } while (0)

struct cf { float x, y; };
DI cf cmul(cf a, cf b) { return cf{fmaf(a.x, b.x, -(a.y * b.y)), fmaf(a.x, b.y, a.y * b.x)}; }
DI unsigned pkc(cf a) { return pkh(a.x, a.y); }
DI unsigned pkcc(cf a) { return pkh(a.x, -a.y); }
DI cf zpow(float al, float om, float p) {
  const float m = __builtin_amdgcn_exp2f(al * p), ph = __builtin_amdgcn_fractf(om * p);
  return cf{m * __builtin_amdgcn_cosf(ph), m * __builtin_amdgcn_sinf(ph)};
}
DI unsigned cmulh(unsigned a, unsigned b) {
  const f16x2 z = __builtin_bit_cast(f16x2, a), w = __builtin_bit_cast(f16x2, b);
  const f16x2 t = z * (f16x2){w[0], w[0]};
  const f16x2 r = __builtin_elementwise_fma((f16x2){-z[1], z[0]}, (f16x2){w[1], w[1]}, t);
  return __builtin_bit_cast(unsigned, r);
}
DI u32x4 cmulh4(u32x4 a, u32x4 b) { return (u32x4){cmulh(a[0], b[0]), cmulh(a[1], b[1]), cmulh(a[2], b[2]), cmulh(a[3], b[3])}; }
#define TB_RS 272
#define TB_N 33
DI void kgen_tables(unsigned (&T)[TB_N], float& isc, unsigned raw0, unsigned raw1, float ldtv, char* lds, int cw, int lane) {
  const int src = ((lane & 31) >> 1) * 4, sh = (lane & 1) ? 0 : 16;
  unsigned pv[4];
#pragma unroll
  for (int a = 0; a < 4; ++a) {
    const unsigned lo = (unsigned)__builtin_amdgcn_ds_bpermute(src + 64 * a, (int)raw0), hi = (unsigned)__builtin_amdgcn_ds_bpermute(src + 64 * a, (int)raw1);
    pv[a] = ((lane < 32 ? lo : hi) << sh) & 0xffff0000u;
  }
  const float dt = rbf(__builtin_amdgcn_exp2f(ldtv * 1.4426950408889634f));
  const float ar = rbf(__builtin_amdgcn_exp2f(__uint_as_float(pv[0]) * 1.4426950408889634f));
  const float al = -(ar * dt) * 1.4426950408889634f;
  const float om = (__uint_as_float(pv[1]) * dt) * 0.15915494309189535f;
  const float cr = __uint_as_float(pv[2]) * dt, ci = __uint_as_float(pv[3]) * dt;
  const float sabs = 2.f * rdlane(wave_sum63(fabsf(cr) + fabsf(ci)), 63);
  int ex = (int)((__float_as_uint(sabs) >> 23) & 255);
  ex = ex < 20 ? 20 : (ex > 230 ? 230 : ex);
  const float sc2 = __uint_as_float((unsigned)(268 - ex) << 23);
  isc = __uint_as_float((unsigned)(ex - 13) << 23);
  if (lane == 0) *(float*)(lds + L_KSC + cw * 4) = sabs;
  const cf z1 = zpow(al, om, 1.f), z8 = zpow(al, om, 8.f), z32 = zpow(al, om, 32.f), a1 = zpow(al, om, 128.f), a4 = zpow(al, om, 512.f);
  const unsigned one = 0x00003C00u;
  const cf a2 = cmul(a1, a1), a3 = cmul(a2, a1), a8 = cmul(a4, a4), a12 = cmul(a8, a4);
  T[0] = one; T[1] = pkc(a1); T[2] = pkc(a2); T[3] = pkc(a3);
  T[4] = pkc(a4); T[5] = pkc(cmul(a4, a1)); T[6] = pkc(cmul(a4, a2)); T[7] = pkc(cmul(a4, a3));
  T[8] = pkc(a8); T[9] = pkc(cmul(a8, a1)); T[10] = pkc(cmul(a8, a2)); T[11] = pkc(cmul(a8, a3));
  T[12] = pkc(a12); T[13] = pkc(cmul(a12, a1)); T[14] = pkc(cmul(a12, a2)); T[15] = pkc(cmul(a12, a3));
  T[16] = pkc(cmul(a8, a8));
  const cf e2 = cmul(z1, z1), e3 = cmul(e2, z1), e4 = cmul(e2, e2);
  T[17] = one; T[18] = pkc(z1); T[19] = pkc(e2); T[20] = pkc(e3); T[21] = pkc(e4);
  T[22] = pkc(cmul(e4, z1)); T[23] = pkc(cmul(e4, e2)); T[24] = pkc(cmul(e4, e3));
  const cf c0 = cf{cr * sc2, ci * sc2}, c2 = cmul(c0, cmul(z32, z32));
  T[25] = pkcc(c0); T[26] = pkcc(cmul(c0, z32)); T[27] = pkcc(c2); T[28] = pkcc(cmul(c2, z32));
  const cf z16 = cmul(z8, z8);
  T[29] = one; T[30] = pkcc(z8); T[31] = pkcc(z16); T[32] = pkcc(cmul(z16, z8));
}
#define KG_PARAMS f32x4& kX, f32x4& kY, f32x4& kZ, float isc
DI void kgen_store(KG_PARAMS, char* lds, int wid, int lane) {
  const int i16 = lane & 15, g = lane >> 4;
  float* kap = (float*)(lds + L_KAP + wid * KAPSZ);
  *(f32x4*)(kap + 128 * (g >> 1) + 4 * (g & 1) + 8 * i16) = kX * isc;
  if (g < 2) {
    *(f32x4*)(kap + 256 + 64 * g + 4 * i16) = kY * isc;
#pragma unroll
    for (int r = 0; r < 4; ++r) kap[512 + 16 * (4 * g + r) + i16] = kZ[r] * isc;
  } else {
    *(f32x2*)(kap + 384 + 64 * (g - 2) + 2 * i16) = (f32x2){kY[0] * isc, kY[1] * isc};
    *(f32x2*)(kap + 384 + 64 * (g - 2) + 32 + 2 * i16) = (f32x2){kY[2] * isc, kY[3] * isc};
    if (g == 2 && i16 == 0) kap[640] = kZ[0] * isc;
  }
}
DI const u16* prm_ptr(const u16* lar, const u16* aim, const u16* cre, const u16* cim, int d, int hf, int lane) {
  const int g = lane >> 4;
  const u16* p = g == 0 ? lar : (g == 1 ? aim : (g == 2 ? cre : cim));
  return p + d * N_ + 64 * hf + 2 * (lane & 15);
}
DI void ld_u32(unsigned& v, const void* p) { asm volatile("global_load_dword %0, %1, off" : "=v"(v) : "v"(p) : "memory"); }
DI void ld_u64(u64& v, const void* p) { asm volatile("global_load_dwordx2 %0, %1, off sc1" : "=v"(v) : "v"(p) : "memory"); }
DI void gload16(u32x4& v, unsigned voff, const char* sbase) {
  asm volatile("global_load_dwordx4 %0, %1, %2 sc1" : "=v"(v) : "v"(voff), "s"(sbase) : "memory");
}
DI u64 rdlane64(u64 v, int l) { return (u64)(unsigned)__builtin_amdgcn_readlane((int)(unsigned)v, l) | ((u64)(unsigned)__builtin_amdgcn_readlane((int)(unsigned)(v >> 32), l) << 32); }
DI bool last_of(char* lds, int c, unsigned n, int lane) {
  unsigned old = 0;
  if (lane == 0) old = __hip_atomic_fetch_add((unsigned*)(lds + L_CNT) + c, 1u, RLX_WG);
  return (unsigned)__builtin_amdgcn_readfirstlane(old) == n - 1;
}
DI float gelu_f(float v) {
  const float c0 = -2.302208198f;
  const float c1 = -0.1029432397f;
  const float z = v * fmaf(v * v, c1, c0);
  return v * __builtin_amdgcn_rcpf(1.f + __builtin_amdgcn_exp2f(z));
}
#define K2S(S) ((S) + r2 >= 14 ? (S) + r2 - 14 : (S) + r2)
#define K2_DMA(S) { _Pragma("unroll") for (int pp = 0; pp < 4; ++pp) dma16s(wvoff, wsrc + (size_t)(K2S(S) * 32 + pp * 8) * D_, ringa + ((S) % K2_R) * 4096 + pp * 1024); SCHED(); }
#define K2_SETUP(kq_) int lane2_ = lane; asm volatile("" : "+v"(lane2_)); \
  const unsigned wvoff = (lane2_ >> 3) * (D_ * 2) + (((lane2_ & 7) ^ (((lane2_ >> 4) & 1) << 2)) << 4); \
  const unsigned ringa = __builtin_amdgcn_readfirstlane(lds_base(lds) + L_RING2 + (kq_) * (K2_R * 4096)); \
  const u16* wsrc = w2 + (size_t)(14 * (kq_) * 32) * D_ + 64 * cb; \
  const int r2 = (2 * cb + 4 * (mb & 3)) % 14;
#define HMT 114688

DI void p1_gemm(const u16* __restrict__ x, const u16* __restrict__ w1, char* __restrict__ ws, char* lds, int panel, int fs, int wid, int lane TK0_ARG) {
  STAMP_DECL STAMP_INIT(0) STAMP0();
  const int wm = wid >> 2, wf = (wid >> 1) & 1, kh = wid & 1;
  const int r = lane & 31, h = lane >> 5;
  const int row0 = panel * 128, f0 = fs * 128;
  const unsigned xv0 = (lane >> 3) * 1024 + (((lane & 7) ^ ((lane >> 4) & 3)) << 4);
  const unsigned xv1 = xv0 ^ 64;
  const unsigned wv = (lane >> 4) * (F_ * 2) + (((lane & 15) ^ (((lane >> 4) & 3) << 2)) << 4);
  const u16* xsb = x + (size_t)(row0 + 16 * wid) * D_;
  const u16* wsb = w1 + (size_t)(8 * wid) * F_ + f0;
  const unsigned la = lds_base(lds) + wid * 2048;
  const int rot = 2 * (panel & 3) + (fs >= 7 ? fs - 7 : fs);
#define KS(S) (((S) + rot) & 7)
#define K1_D0(S) { dma16s(xv0, xsb + KS(S) * 64, la + ((S) % 4) * K1_SLOT); SCHED(); }
#define K1_D1(S) { dma16s(xv1, xsb + 8 * D_ + KS(S) * 64, la + ((S) % 4) * K1_SLOT + 1024); SCHED(); }
#define K1_D2(S) { dma16s(wv, wsb + (size_t)(KS(S) * 64) * F_, la + ((S) % 4) * K1_SLOT + 16384); SCHED(); }
#define K1_D3(S) { dma16s(wv, wsb + (size_t)(KS(S) * 64 + 4) * F_, la + ((S) % 4) * K1_SLOT + 16384 + 1024); SCHED(); }
#define K1_ISSUE(S) { K1_D0(S); K1_D1(S); K1_D2(S); K1_D3(S); }
  K1_ISSUE(0); K1_ISSUE(1); K1_ISSUE(2); K1_ISSUE(3);
  const int xoff = (64 * wm + r) * 128 + (((4 * kh + 2 * wm + h) ^ ((r >> 1) & 7)) << 4);
  const int q = (lane & 15) >> 2, p = lane & 3;
  const int woff0 = 16384 + (32 * kh + 16 * wm + 8 * h + q) * 256 + (((2 * wf + kh) ^ q) << 6) + ((lane >> 4) & 1) * 32 + 8 * p;
  f32x16 acc[2][2];
#pragma unroll
  for (int i = 0; i < 16; ++i) { acc[0][0][i] = 0.f; acc[0][1][i] = 0.f; acc[1][0][i] = 0.f; acc[1][1][i] = 0.f; }
  STAMP();
  s16x8 xf[2][2][2], wfr[2][2][2];
#define K1_M(T, s, mi, fi) { acc[mi][fi] = __builtin_amdgcn_mfma_f32_32x32x16_bf16(wfr[(T) & 1][s][fi], xf[(T) & 1][s][mi], acc[mi][fi], 0, 0, 0); SCHED(); }
#define K1_RX(T, s, mi) { xf[(T) & 1][s][mi] = *(const s16x8*)(lds + (xa_ ^ ((s) << 5)) + (mi) * 4096); SCHED(); }
#define K1_RW(T, s, fi) { wfr[(T) & 1][s][fi] = tr_frag(lds + (wa_ ^ (((fi) << 6) | ((s) << 12))), lds + (wa_ ^ (((fi) << 6) | ((s) << 12))) + 1024); SCHED(); }
#define K1_ADDR(T) int xa_ = xoff + ((T) % 4) * K1_SLOT, wa_ = woff0 + ((T) % 4) * K1_SLOT; asm volatile("" : "+v"(xa_), "+v"(wa_));
#define K1_READ(T) { K1_ADDR(T) K1_RX(T,0,0); K1_RW(T,0,0); K1_RW(T,0,1); K1_RX(T,0,1); K1_RX(T,1,0); K1_RW(T,1,0); K1_RW(T,1,1); K1_RX(T,1,1); }
#define K1_MMA(T) { K1_M(T,0,0,0); K1_M(T,0,0,1); K1_M(T,0,1,0); K1_M(T,0,1,1); K1_M(T,1,0,0); K1_M(T,1,0,1); K1_M(T,1,1,0); K1_M(T,1,1,1); }
#define K1_STAGE(T, DM0, DM1, DM2, DM3) { K1_ADDR(T) \
    K1_M((T)-1,0,0,0); K1_RX(T,0,0); K1_RW(T,0,0); K1_M((T)-1,0,0,1); K1_RW(T,0,1); K1_RX(T,0,1); \
    K1_M((T)-1,0,1,0); K1_RX(T,1,0); K1_RW(T,1,0); K1_M((T)-1,0,1,1); K1_RW(T,1,1); K1_RX(T,1,1); \
    K1_M((T)-1,1,0,0); DM0; K1_M((T)-1,1,0,1); DM1; K1_M((T)-1,1,1,0); DM2; K1_M((T)-1,1,1,1); DM3; }
#define K1_STAGE_D(T, S) K1_STAGE(T, K1_D0(S), K1_D1(S), K1_D2(S), K1_D3(S))
  WAITV_BAR(12); STAMP();
  K1_READ(0);
  WAITV_BAR(8); K1_STAGE_D(1, 4);
  WAITV_BAR(8); K1_STAGE_D(2, 5);
  WAITV_BAR(8); K1_STAGE_D(3, 6);
  WAITV_BAR(8); STAMP(); K1_STAGE_D(4, 7);
  WAITV_BAR(8); K1_STAGE(5, , , , );
  WAITV_BAR(4); K1_STAGE(6, , , , );
  K1_MMA(6);
  WAITV_BAR(0);
  K1_READ(7); K1_MMA(7);
  STAMP();
  float* colS = (float*)(lds + L_COLS);
  {
    char* dst = lds + wid * 8192 + lane * 16;
#pragma unroll
    for (int mi = 0; mi < 2; ++mi)
#pragma unroll
      for (int rq = 0; rq < 4; ++rq)
        *(f32x4*)(dst + (mi * 4 + rq) * 1024) = (f32x4){acc[mi][1][4 * rq], acc[mi][1][4 * rq + 1], acc[mi][1][4 * rq + 2], acc[mi][1][4 * rq + 3]};
  }
  LGKM_BAR();
  STAMP();
  f32x4 pa[2][4], cq[4];
  f32x2 st[2];
  u32x2 bq[4];
  {
    const char* src = lds + (wid ^ 1) * 8192 + lane * 16;
    const int fl = 64 * wf + 32 * kh + 4 * h;
#pragma unroll
    for (int rq = 0; rq < 4; ++rq) {
      cq[rq] = *(const f32x4*)(colS + fl + 8 * rq);
      bq[rq] = *(const u32x2*)(lds + L_BIAS + (fl + 8 * rq) * 2);
      pa[0][rq] = *(const f32x4*)(src + rq * 1024); pa[1][rq] = *(const f32x4*)(src + (4 + rq) * 1024);
    }
    st[0] = *(const f32x2*)(lds + L_STAT + (64 * wm + r) * 8); st[1] = *(const f32x2*)(lds + L_STAT + (64 * wm + 32 + r) * 8);
  }
  LGKM_BAR();
  {
    const __amdgpu_buffer_rsrc_t hr = __builtin_amdgcn_make_buffer_rsrc(ws + WS_H, 0, L_ * F_ * 2, 0x00020000);
    const unsigned hoff = (unsigned)(panel * 4 + 2 * wm) * HMT + (8 * fs + 4 * wf + 2 * kh) * 1024 + lane * 16;
#pragma unroll
    for (int mi = 0; mi < 2; ++mi) {
      const float mr = st[mi][0], rinv = st[mi][1];
#pragma unroll
      for (int s2 = 0; s2 < 2; ++s2) {
        u32x4 pk;
#pragma unroll
        for (int qq = 0; qq < 2; ++qq) {
          const int rq = 2 * s2 + qq;
          const float b0 = bflo(bq[rq][0]), b1_ = bfhi(bq[rq][0]), b2_ = bflo(bq[rq][1]), b3 = bfhi(bq[rq][1]);
          const float o0 = gelu_f(fmaf(acc[mi][0][4 * rq] + pa[mi][rq][0], rinv, fmaf(mr, cq[rq][0], b0)));
          const float o1 = gelu_f(fmaf(acc[mi][0][4 * rq + 1] + pa[mi][rq][1], rinv, fmaf(mr, cq[rq][1], b1_)));
          const float o2 = gelu_f(fmaf(acc[mi][0][4 * rq + 2] + pa[mi][rq][2], rinv, fmaf(mr, cq[rq][2], b2_)));
          const float o3 = gelu_f(fmaf(acc[mi][0][4 * rq + 3] + pa[mi][rq][3], rinv, fmaf(mr, cq[rq][3], b3)));
          pk[2 * qq] = pkbf(o0, o1); pk[2 * qq + 1] = pkbf(o2, o3);
        }
        __builtin_amdgcn_raw_buffer_store_b128(pk, hr, hoff + mi * HMT + s2 * 1024, 0, 16);
        SCHED();
      }
    }
  }
  STAMP();
  asm volatile("s_waitcnt vmcnt(0)" ::: "memory");
  if (last_of(lds, C_PUB, 8, lane) && lane < 16) {
    const u64 ac = *(const u64*)(lds + L_ACON + lane * 8);
    int l2 = lane; asm volatile("" : "+v"(l2));
    __hip_atomic_store((u64*)(ws + WS_FLAG + (size_t)blk_of(2 * panel + ((l2 >> 3) & 1), l2 & 7) * 1024 + FL_H) + fs, ac + 1, RLX_AGENT);
  }
  STAMP(); STAMP_FLUSH();
}

#define LN_RS 1040
DI void p1_aux(const u16* __restrict__ x, char* __restrict__ ws, char* lds, int idx, int wid, int lane TK0_ARG) {
  STAMP_DECL STAMP_INIT(0) STAMP0();
  const int t0 = idx * 64;
  u32x4 v[8];
#pragma unroll
  for (int i = 0; i < 8; ++i) v[i] = *(const u32x4*)(x + (size_t)(t0 + 8 * wid + i) * D_ + lane * 8);
  const u64 aCon = __hip_atomic_load((const u64*)(ws + WS_EP) + ((wid * 64 + lane) & 255), RLX_AGENT);
  const int sfs = lane < 14 ? lane : 0;
  const int sblk = 2 * (idx >> 3) + (sfs >= 7 ? 1 : 0) + 8 * (((idx >> 1) & 3) * 7 + (sfs >= 7 ? sfs - 7 : sfs));
  const u64 aSt = __hip_atomic_load((const u64*)(ws + WS_EP) + sblk, RLX_AGENT);
  SCHED();
  asm volatile("s_barrier" ::: "memory");
  SCHED();
#pragma unroll
  for (int i = 0; i < 8; ++i) *(u32x4*)(lds + (63 - (8 * wid + i)) * LN_RS + lane * 16) = v[i];
  ARRIVE_L(C_LN1);
  float sA[8], sB[8];
#pragma unroll
  for (int i = 0; i < 8; ++i) {
    float a = dot2(v[i][0], 0x3F803F80u, 0.f), b = dot2(v[i][0], v[i][0], 0.f);
#pragma unroll
    for (int c = 1; c < 4; ++c) { a = dot2(v[i][c], 0x3F803F80u, a); b = dot2(v[i][c], v[i][c], b); }
    sA[i] = a; sB[i] = b;
  }
#pragma unroll
  for (int i = 0; i < 4; ++i) { sA[i] = comb32(sA[i], sA[i + 4]); sB[i] = comb32(sB[i], sB[i + 4]); }
#pragma unroll
  for (int i = 0; i < 2; ++i) { sA[i] = row_allsum(comb16(sA[i], sA[i + 2])); sB[i] = row_allsum(comb16(sB[i], sB[i + 2])); }
  const float mv = ((lane & 1) ? sA[1] : sA[0]) * (1.f / 512.f);
  const float var = fmaxf(fmaf(-mv, mv, ((lane & 1) ? sB[1] : sB[0]) * (1.f / 512.f)), 0.f);
  const float mbv = rbf(mv);
  const float sd = rbf(sqrtf(rbf(rbf(var) + 1e-5f)));
  const float rv = 1.f / sd;
  if ((lane & 14) == 0) {
    const u64 pk = (u64)__float_as_uint(-mbv * rv) | ((u64)__float_as_uint(rv) << 32);
    const int tt = t0 + 8 * wid + (lane & 1) + 2 * (lane >> 4);
    __hip_atomic_store((u64*)(ws + WS_STAT) + tt, pk, RLX_AGENT);
    __hip_atomic_store((unsigned*)(ws + WS_ST2) + (L_ - 1 - tt), (__float_as_uint(mbv) >> 16) | __float_as_uint(sd), RLX_AGENT);
  }
  STAMP();
  await_<0>(lds, C_LN1, 8);
  char* t2 = lds + 64 * LN_RS + wid * 8192;
  const int g = lane >> 4, q = (lane & 15) >> 2, p = lane & 3, dl = lane & 15;
  s16x8 f[8];
#pragma unroll
  for (int i = 0; i < 8; ++i) {
    const char* a = lds + (8 * (4 * (i & 1) + g) + q) * LN_RS + (64 * wid + 16 * (i >> 1) + 4 * p) * 2;
    f[i] = tr_frag(a, a + 4 * LN_RS);
  }
  SCHED();
#pragma unroll
  for (int i = 0; i < 8; ++i) {
    const int dd2 = 16 * (i >> 1) + dl, rc = 4 * (i & 1) + g;
    *(s16x8*)(t2 + dd2 * 128 + ((rc ^ (dd2 & 7)) << 4)) = f[i];
  }
  SCHED();
  const __amdgpu_buffer_rsrc_t ur = __builtin_amdgcn_make_buffer_rsrc(ws + WS_UR, 0, L_ * D_ * 2, 0x00020000);
  const int x0 = L_ - 64 - t0;
  u32x4 o[8];
#pragma unroll
  for (int i = 0; i < 8; ++i) {
    const int dd2 = 8 * i + (lane >> 3), c = lane & 7;
    o[i] = *(const u32x4*)(t2 + dd2 * 128 + ((c ^ (dd2 & 7)) << 4));
  }
  SCHED();
  asm volatile("s_waitcnt vmcnt(0)" ::: "memory");
  if (last_of(lds, C_PUB, 8, lane) && lane < 14) __hip_atomic_store((u64*)(ws + WS_FLAG + (size_t)sblk * 1024 + FL_ST) + (idx & 1), aSt + 1, RLX_AGENT);
  STAMP();
#pragma unroll
  for (int i = 0; i < 8; ++i)
    __builtin_amdgcn_raw_buffer_store_b128(o[i], ur, (unsigned)(64 * wid + 8 * i + (lane >> 3)) * 4096 + x0 * 2 + (lane & 7) * 16, 0, 16);
  STAMP();
  ARRIVE_L(C_X2);
  ARRIVE_V(0, C_LN2);
  if (wid < 4) {
    await_<0>(lds, C_LN2, 8);
    __hip_atomic_store((u64*)(ws + WS_FLAG + (size_t)(wid * 64 + lane) * 1024 + FL_LN) + idx, aCon + 1, RLX_AGENT);
  }
  STAMP(); STAMP_FLUSH();
}

DI void x_wave(const u16* __restrict__ lar, const u16* __restrict__ aim, const u16* __restrict__ cre, const u16* __restrict__ cim,
               unsigned sdt, unsigned sds, const u16* __restrict__ w2, const u16* __restrict__ w1, const u16* __restrict__ b1, int auxidx, int panel, int fs,
               char* __restrict__ ws, char* lds, int mb, int cb, int cw, int lane TK0_ARG) {
  STAMP_DECL STAMP_INIT(1) STAMP0();
  __builtin_amdgcn_s_setprio(3);
  const bool aux = auxidx >= 0;
  const int ch = cw >> 1, hf = cw & 1;
  const int d = 64 * cb + 2 * mb + ch;
  const int i16 = lane & 15, g = lane >> 4;
  unsigned* cnt = (unsigned*)(lds + L_CNT);
  const char* flg = ws + WS_FLAG + (size_t)blockIdx.x * 1024;
  const u16* pp_ = prm_ptr(lar, aim, cre, cim, d, hf, lane);
  unsigned raw0, raw1;
  u64 aS, aH;
  ld_u32(raw0, pp_); ld_u32(raw1, pp_ + 32);
  ld_u64(aS, ws + WS_EP + (lane >= 32 ? (int)blockIdx.x : blk_of(lane, cb)) * 8);
  ld_u64(aH, ws + WS_EP + blk_of(2 * panel + ((lane >> 3) & 1), lane & 7) * 8);
  if (!aux && cw == 2) asm volatile("s_mov_b32 m0, %2\n\ts_nop 0\n\tglobal_load_lds_dword %0, %1" :: "v"(lane * 4), "s"(b1 + fs * 128), "s"(lds_base(lds) + L_BIAS) : "memory");
  if (cw == 0 && lane < 24) cnt[lane] = 0u;
  if (aux) {
    asm volatile("s_waitcnt lgkmcnt(0)\n\ts_barrier" ::: "memory");
    __builtin_amdgcn_s_setprio(1);
    if (auxidx < 28) {
      const u16* wp_ = w1 + (size_t)(128 * cw + (lane >> 3)) * F_ + 64 * auxidx + 8 * (lane & 7);
      const int cblk = 2 * ((lane & 15) >> 2) + (auxidx >= 14 ? 1 : 0) + 8 * ((lane & 3) * 7 + ((auxidx >> 1) % 7));
      const u64 aC = __hip_atomic_load((const u64*)(ws + WS_EP) + cblk, RLX_AGENT);
      float a8[8] = {0.f, 0.f, 0.f, 0.f, 0.f, 0.f, 0.f, 0.f};
      u32x4 wv[16];
#pragma unroll
      for (int i = 0; i < 16; ++i) wv[i] = *(const u32x4*)(wp_ + (size_t)(8 * i) * F_);
      SCHED();
#pragma unroll
      for (int i = 0; i < 16; ++i)
#pragma unroll
        for (int c = 0; c < 4; ++c) { a8[2 * c] += bflo(wv[i][c]); a8[2 * c + 1] += bfhi(wv[i][c]); }
#pragma unroll
      for (int i = 0; i < 4; ++i) a8[i] = comb32(a8[i], a8[i + 4]);
#pragma unroll
      for (int i = 0; i < 2; ++i) a8[i] = dpp_add<0x128, 0xf>(comb16(a8[i], a8[i + 2]));
      if (!(lane & 8)) *(f32x2*)(lds + L_COLS + cw * 256 + (8 * (lane & 7) + 2 * (g & 1) + 4 * (g >> 1)) * 4) = (f32x2){a8[0], a8[1]};
      SCHED(); asm volatile("s_waitcnt lgkmcnt(0)" ::: "memory");
      if (last_of(lds, C_CS, 4, lane)) {
        const float* cp = (const float*)(lds + L_COLS);
        const float tot = (cp[lane] + cp[64 + lane]) + (cp[128 + lane] + cp[192 + lane]);
        __hip_atomic_store((float*)(ws + WS_CS) + 64 * auxidx + lane, tot, RLX_AGENT);
        asm volatile("s_waitcnt vmcnt(0)" ::: "memory");
        if (lane < 16) __hip_atomic_store((u64*)(ws + WS_FLAG + (size_t)cblk * 1024 + FL_ST) + 2 + (auxidx & 1), aC + 1, RLX_AGENT);
      }
    }
  }
  asm volatile("s_waitcnt vmcnt(2)" : "+v"(raw0), "+v"(raw1) :: "memory");
  float isc;
  unsigned T[TB_N];
  kgen_tables(T, isc, raw0, raw1, (d & 1) ? bfhi(sdt) : bflo(sdt), lds, cw, lane);
#pragma unroll
  for (int i = 0; i < TB_N; ++i) asm volatile("" : "+v"(T[i]));
  asm volatile("s_waitcnt vmcnt(0)" : "+v"(aS), "+v"(aH) :: "memory");
  const u64 aOwn = rdlane64(aS, 32);
  if (cw == 1 && lane < 16) *(u64*)(lds + L_ACON + lane * 8) = aH;
  if (cw == 3) { if (lane == 0) *(u64*)(lds + L_KSC + 16) = aOwn; ARRIVE_L(C_EP); }
  __builtin_amdgcn_s_setprio(1);
  STAMP();
  const u64* lnfl = (const u64*)(flg + FL_LN) + (lane & 31);
  const char* up = ws + WS_UR + (size_t)d * 4096 + hf * 2048;
  u64 fv;
  if (aux) {
    await_<2>(lds, C_X2, 8);
  } else {
    const u64* stfl = (const u64*)(flg + FL_ST) + (lane & 3);
    const char* stp = cw == 1 ? ws + WS_CS + fs * 512 : ws + WS_STAT + (size_t)panel * 1024;
    const unsigned svo = cw == 1 ? (lane & 31) * 16 : lane * 16;
    u32x4 stv;
#define XBAR() do { SCHED(); asm volatile("s_waitcnt lgkmcnt(0)\n\ts_barrier" ::: "memory"); SCHED(); } while (0)
    if (cw < 2) {
      bool ok = false, got = false;
      XBAR(); XBAR(); XBAR(); XBAR(); XBAR(); XBAR();
      ld_u64(fv, stfl); gload16(stv, svo, stp);
#define XPOLL() do { XBAR(); asm volatile("s_waitcnt vmcnt(0)" : "+v"(fv), "+v"(stv) :: "memory"); \
        if (ok && !got) { *(u32x4*)(lds + (cw == 1 ? L_COLS : L_STAT) + svo) = stv; got = true; } \
        ok = __all(fv == aOwn + 1); SCHED(); ld_u64(fv, stfl); gload16(stv, svo, stp); } while (0)
      XPOLL(); XPOLL();
      asm volatile("s_waitcnt vmcnt(0)" : "+v"(fv), "+v"(stv) :: "memory");
      if (!got) {
        if (!ok) {
          poll_flags(stfl, aOwn + 1);
          asm volatile("global_load_dwordx4 %0, %1, %2 sc1\n\ts_waitcnt vmcnt(0)" : "=&v"(stv) : "v"(svo), "s"(stp) : "memory");
        }
        *(u32x4*)(lds + (cw == 1 ? L_COLS : L_STAT) + svo) = stv;
      }
    } else {
      XBAR(); XBAR(); XBAR(); XBAR(); XBAR(); XBAR(); XBAR(); XBAR();
    }
    XBAR();
    XBAR();
  }
  STAMP();
  __builtin_amdgcn_s_setprio(3);
  K2_SETUP(cw);
  K2_DMA(0); K2_DMA(1);
  char* wl = lds + L_W + ch * WSZ;
  {
    char* tb = wl + hf * 9216;
#pragma unroll
    for (int i = 0; i < TB_N; ++i) *(unsigned*)(tb + i * TB_RS + lane * 4) = T[i];
    const char* tr = tb + g * 16;
    const int rXa = (i16 >> 3) * TB_RS, rXe = (17 + (i16 & 7)) * TB_RS;
    const int rYa = (i16 < 8 ? 2 + (i16 >> 2) : 4 + ((i16 - 8) >> 1)) * TB_RS, rYe = (17 + (i16 < 8 ? 2 * (i16 & 3) : 4 * (i16 & 1))) * TB_RS;
    const int rZa = (i16 < 8 ? 8 + i16 : 16) * TB_RS;
    const int rB1 = (25 + (i16 >> 2)) * TB_RS, rB0 = (29 + (i16 & 3)) * TB_RS;
    f32x4 kX = {0.f, 0.f, 0.f, 0.f}, kY = kX, kZ = kX;
#pragma unroll
    for (int sb = 0; sb < 4; sb += 2) {
      u32x4 t_[2][7];
#pragma unroll
      for (int s = 0; s < 2; ++s) {
        const char* tq = tr + (sb + s) * 64;
        t_[s][0] = *(const u32x4*)(tq + rB1); t_[s][1] = *(const u32x4*)(tq + rB0); t_[s][2] = *(const u32x4*)(tq + rXa); t_[s][3] = *(const u32x4*)(tq + rXe);
        t_[s][4] = *(const u32x4*)(tq + rYa); t_[s][5] = *(const u32x4*)(tq + rYe); t_[s][6] = *(const u32x4*)(tq + rZa);
      }
      SCHED();
#pragma unroll
      for (int s = 0; s < 2; ++s) {
        const f16x8 hb = __builtin_bit_cast(f16x8, cmulh4(t_[s][0], t_[s][1]));
        kX = __builtin_amdgcn_mfma_f32_16x16x32_f16(__builtin_bit_cast(f16x8, cmulh4(t_[s][2], t_[s][3])), hb, kX, 0, 0, 0);
        kY = __builtin_amdgcn_mfma_f32_16x16x32_f16(__builtin_bit_cast(f16x8, cmulh4(t_[s][4], t_[s][5])), hb, kY, 0, 0, 0);
        kZ = __builtin_amdgcn_mfma_f32_16x16x32_f16(__builtin_bit_cast(f16x8, t_[s][6]), hb, kZ, 0, 0, 0);
      }
      SCHED();
    }
    kgen_store(kX, kY, kZ, isc, lds, cw, lane);
  }
  ARRIVE_L(C_K0 + ch);
  ld_u64(fv, lnfl);
  await_<0>(lds, C_K0 + ch, 2);
  const f32x2 ks = *(const f32x2*)(lds + L_KSC + ch * 8);
  int ex = (int)((__float_as_uint(ks[0] + ks[1]) >> 23) & 255);
  ex = ex < 20 ? 20 : (ex > 230 ? 230 : ex);
  const float scale = __uint_as_float((unsigned)(267 - ex) << 23);
  const float iscale = __uint_as_float((unsigned)(ex - 13) << 23);
  char* kp = lds + L_KP + ch * KPSZ;
  {
    const float* k0 = (const float*)(lds + L_KAP + (2 * ch) * KAPSZ);
#define KSUM2(T, i) ((*(const T*)(k0 + (i)) + *(const T*)(k0 + 644 + (i))) * scale)
    const int A = 64 * hf + lane, A2 = 128 + A;
    const float e0 = KSUM2(float, 512 + A), e1 = KSUM2(float, 513 + A);
    u32x4 o;
    if (hf == 0) {
      const int i0 = lane < 32 ? 8 * A : 256 + 4 * (A - 32);
      const f32x4 a = KSUM2(f32x4, i0), b = KSUM2(f32x4, i0 + 4);
      const bool lo = lane < 32;
      o = (u32x4){pkh(a[0], lo ? a[1] : a[0]), pkh(lo ? a[2] : a[1], lo ? a[3] : a[2]), pkh(lo ? b[0] : a[2], lo ? b[1] : a[2]), pkh(lo ? b[2] : a[3], lo ? b[3] : b[0])};
      if (lane < 2) *(u32x4*)(kp + lane * 16) = (u32x4){0u, 0u, 0u, 0u};
    } else {
      const int i0 = 384 + 2 * lane;
      const f32x2 a = KSUM2(f32x2, i0), b = KSUM2(f32x2, i0 + 2);
      o = (u32x4){pkh(a[0], a[0]), pkh(a[0], a[1]), pkh(a[1], a[1]), pkh(b[0], b[0])};
    }
    *(u32x4*)(kp + 32 + A * 16) = o;
    const unsigned p00 = pkh(e0, e0);
    *(u32x4*)(kp + 32 + A2 * 16) = (u32x4){p00, p00, pkh((A2 & 1) ? e1 : e0, e1), pkh(e1, e1)};
  }
  ARRIVE_L(C_K1);
  asm volatile("s_waitcnt vmcnt(0)" : "+v"(fv) :: "memory");
  arrive(lds, C_W2, lane);
  const bool urok = __all(fv == aOwn + 1);
  u32x4 ua_, ub_, q0, q1, q2, q3;
  const char* qp = ws + WS_ST2 + hf * 4096;
#define U_LOADS() { gload16(ua_, lane * 16, up); gload16(ub_, lane * 16, up + 1024); \
    gload16(q0, lane * 32, qp); gload16(q1, lane * 32, qp + 16); gload16(q2, lane * 32, qp + 2048); gload16(q3, lane * 32, qp + 2064); }
#define U_WAIT() asm volatile("s_waitcnt vmcnt(0)" : "+v"(ua_), "+v"(ub_), "+v"(q0), "+v"(q1), "+v"(q2), "+v"(q3) :: "memory")
  U_LOADS();
  await_<0>(lds, C_K1, 4);
  STAMP();
#pragma unroll
  for (int rnd = 0; rnd < 3; ++rnd) {
    const int a = rnd < 2 ? 128 * rnd + 64 * hf + lane : 256;
    if (rnd == 2 && !(hf == 0 && lane == 0)) break;
    const u32x4 c0 = *(const u32x4*)(kp + a * 16), c1 = *(const u32x4*)(kp + a * 16 + 16);
    const unsigned e[8] = {c0[0], c0[1], c0[2], c0[3], c1[0], c1[1], c1[2], c1[3]};
    unsigned o[7];
#pragma unroll
    for (int i = 0; i < 7; ++i) o[i] = __builtin_amdgcn_alignbit(e[i + 1], e[i], 16);
    const int m = (a & 1) | (((a >> 2) & 1) << 1) | (((a >> 1) & 1) << 2);
    char* base = wl + a * 128;
#pragma unroll
    for (int c = 0; c < 8; ++c) {
      u32x4 wv;
      if (c & 1) { const int s = (c + 1) >> 1; wv = (u32x4){e[s], e[s + 1], e[s + 2], e[s + 3]}; }
      else { const int s = c >> 1; wv = (u32x4){o[s], o[s + 1], o[s + 2], o[s + 3]}; }
      *(u32x4*)(base + ((c ^ m) << 4)) = wv;
    }
  }
  SCHED();
  U_WAIT();
  if (!urok) { poll_flags(lnfl, aOwn + 1); U_LOADS(); U_WAIT(); }
  char* ul = lds + L_U + ch * USZ;
  {
#define UN2(xw, s0, s1) ({ const unsigned a_ = pkbf(bflo(xw) - bflo(s0), bfhi(xw) - bflo(s1)); \
      const unsigned b_ = pkbf(bflo(a_) * __builtin_amdgcn_rcpf(bfhi(s0)), bfhi(a_) * __builtin_amdgcn_rcpf(bfhi(s1))); pkh(bflo(b_), bfhi(b_)); })
    const u32x4 o0 = {UN2(ua_[0], q0[0], q0[1]), UN2(ua_[1], q0[2], q0[3]), UN2(ua_[2], q1[0], q1[1]), UN2(ua_[3], q1[2], q1[3])};
    const u32x4 o1 = {UN2(ub_[0], q2[0], q2[1]), UN2(ub_[1], q2[2], q2[3]), UN2(ub_[2], q3[0], q3[1]), UN2(ub_[3], q3[2], q3[3])};
    *(u32x4*)(ul + hf * 2048 + lane * 16) = o0;
    *(u32x4*)(ul + hf * 2048 + 1024 + lane * 16) = o1;
  }
  if (hf == 0 && lane < 32) *(u32x4*)(ul + 4096 + lane * 16) = (u32x4){0u, 0u, 0u, 0u};
  ARRIVE_L(C_K2 + ch);
  STAMP();
  await_<2>(lds, C_PUB, 8);
  await_<0>(lds, C_K2 + ch, 2);
  __builtin_amdgcn_s_setprio(3);
  STAMP();
  f32x4 acc[8];
#pragma unroll
  for (int i = 0; i < 8; ++i) acc[i] = (f32x4){0.f, 0.f, 0.f, 0.f};
  {
    const int c = i16 + 8 * g;
    int fo[2];
#pragma unroll
    for (int par = 0; par < 2; ++par) {
      const int w = 32 * par + c;
      const int m = ((w >> 3) & 1) | (((w >> 5) & 1) << 1) | (((w >> 4) & 1) << 2);
      fo[par] = (w ^ m) << 4;
    }
    const char* fbase = wl + hf * (128 * 16);
    const char* gbase = ul + (2032 - 16 * i16 + 8 * g + 128 * hf) * 2 - 7 * 512;
#define CF(s, e) (*(const f16x8*)(fbase + fo[(s) & 1] + ((s) >> 1) * 1024 + (e) * 4096))
#define CG(s, mu) (*(const f16x8*)(gbase + (s) * 64 + (7 - (mu)) * 512))
    f16x8 F[2][8], G[2];
#pragma unroll
    for (int e = 0; e < 8; ++e) F[0][e] = CF(0, e);
    G[0] = CG(0, 0);
#pragma unroll
    for (int s = 0; s < 4; ++s) {
#pragma unroll
      for (int mu = 0; mu < 8; ++mu) {
        const int it = s * 8 + mu;
        SCHED();
        if (it + 1 < 32) G[(it + 1) & 1] = CG((it + 1) >> 3, (it + 1) & 7);
        if (s < 3) F[(s + 1) & 1][mu] = CF(s + 1, mu);
        SCHED();
#pragma unroll
        for (int e = 0; e + mu < 8; ++e) acc[e + mu] = __builtin_amdgcn_mfma_f32_16x16x32_f16(F[s & 1][e], G[it & 1], acc[e + mu], 0, 0, 0);
      }
    }
  }
  STAMP();
  ARRIVE_L(C_K1 + 1 + ch); await_<0>(lds, C_K1 + 1 + ch, 2);
  {
    char* dst = wl + hf * 4096 + lane * 16;
#pragma unroll
    for (int i = 0; i < 4; ++i) *(f32x4*)(dst + i * 1024) = sel(hf, acc[i], acc[4 + i]);
  }
  ARRIVE_L(C_K3 + ch); await_<0>(lds, C_K3 + ch, 2);
  STAMP();
  {
    const float dskip = (d & 1) ? bfhi(sds) : bflo(sds);
    const char* src = wl + (hf ^ 1) * 4096 + lane * 16;
    char* sdst = ws + WS_S + (size_t)d * 4096;
#pragma unroll
    for (int i = 0; i < 4; ++i) {
      const f32x4 yv = (sel(hf, acc[4 + i], acc[i]) + *(const f32x4*)(src + i * 1024)) * iscale;
      const int t = 256 * (4 * hf + i) + 16 * i16 + 4 * g;
      const u32x2 uu = *(const u32x2*)(ul + (2044 - t) * 2);
      const unsigned uu0 = uu[0], uu1 = uu[1];
      const f16x2 ua = __builtin_bit_cast(f16x2, uu0), ub = __builtin_bit_cast(f16x2, uu1);
      const unsigned y01 = pkbf(yv[0], yv[1]), y23 = pkbf(yv[2], yv[3]);
      const unsigned k01 = pkbf((float)ub[1] * dskip, (float)ub[0] * dskip), k23 = pkbf((float)ua[1] * dskip, (float)ua[0] * dskip);
      const u64 pk = (u64)pkbf(bflo(y01) + bflo(k01), bfhi(y01) + bfhi(k01)) | ((u64)pkbf(bflo(y23) + bflo(k23), bfhi(y23) + bfhi(k23)) << 32);
      __hip_atomic_store((u64*)(sdst + t * 2), pk, RLX_AGENT);
    }
  }
  asm volatile("s_waitcnt vmcnt(0) lgkmcnt(0)" ::: "memory");
  STAMP();
  if (last_of(lds, C_SP, 4, lane) && lane < 32) __hip_atomic_store((u64*)(ws + WS_FLAG + (size_t)blk_of(lane, cb) * 1024 + FL_S) + mb, aS + 1, RLX_AGENT);
  STAMP_FLUSH();
}

#define FIN_LOADS() { \
  asm volatile("global_load_dwordx4 %0, %1, off" : "=v"(xv) : "v"(x + (size_t)(64 * mb + (tid >> 3)) * D_ + 64 * cb + 8 * (tid & 7)) : "memory"); \
  asm volatile("global_load_dwordx4 %0, %1, off" : "=v"(bv) : "v"(b2 + 64 * cb + 8 * (tid & 7)) : "memory"); }
DI void final_stage(u32x4& xv, u32x4& bv, u16* __restrict__ out, char* lds, int mb, int cb, int tid) {
  await_<0>(lds, C_R1, 4);
  await_<0>(lds, C_ST, 4);
  const int t = tid >> 3, dq = tid & 7;
  const char* src = lds + L_RED + t * RED_RS + dq * 32;
  f32x4 ra[4], rb[4];
#pragma unroll
  for (int k = 0; k < 4; ++k) { ra[k] = *(const f32x4*)(src + k * 64 * RED_RS); rb[k] = *(const f32x4*)(src + k * 64 * RED_RS + 16); }
  const u32x4 sv = *(const u32x4*)(lds + L_ST + t * ST_RS + dq * 16);
  SCHED();
  const f32x4 a = (ra[0] + ra[1]) + (ra[2] + ra[3]), b = (rb[0] + rb[1]) + (rb[2] + rb[3]);
  asm volatile("s_waitcnt vmcnt(0)" : "+v"(xv), "+v"(bv) :: "memory");
  u32x4 o;
#pragma unroll
  for (int c = 0; c < 4; ++c) {
    const float v0 = (c < 2 ? a[2 * c] : b[2 * c - 4]) + bflo(bv[c]), v1 = (c < 2 ? a[2 * c + 1] : b[2 * c - 3]) + bfhi(bv[c]);
    const float g0 = v0 * __builtin_amdgcn_rcpf(1.f + __builtin_amdgcn_exp2f(-1.4426950408889634f * v0));
    const float g1 = v1 * __builtin_amdgcn_rcpf(1.f + __builtin_amdgcn_exp2f(-1.4426950408889634f * v1));
    const unsigned pv = pkbf(bflo(sv[c]) * g0, bfhi(sv[c]) * g1);
    o[c] = pkbf(bflo(xv[c]) + bflo(pv), bfhi(xv[c]) + bfhi(pv));
  }
  *(u32x4*)(out + (size_t)(64 * mb + t) * D_ + 64 * cb + 8 * dq) = o;
}
DI void p2_gemm(const u16* __restrict__ x, const u16* __restrict__ w2, u64 aOwn, const u16* __restrict__ b2, u16* __restrict__ out, char* __restrict__ ws,
                char* lds, int mb, int cb, int kq, int lane TK0_ARG) {
  STAMP_DECL STAMP_INIT(1) STAMP0();
  const int r = lane & 31, h = lane >> 5;
  unsigned* cnt = (unsigned*)(lds + L_CNT);
  const int tid = kq * 64 + lane;
  K2_SETUP(kq);
  char* ring = lds + L_RING2 + kq * (K2_R * 4096);
  const char* flg = ws + WS_FLAG + (size_t)blockIdx.x * 1024;
  const char* hbase = ws + WS_H + (size_t)(2 * mb) * HMT + 28 * kq * 1024;
  const unsigned hoff0 = lane * 16, hoff1 = lane * 16 + HMT;
  const int q = (lane & 15) >> 2;
  const int toff0 = (4 * h + q) * 128 + ((q >> 1) << 6) + ((lane >> 4) & 1) * 32 + 8 * (lane & 3), toff1 = toff0 ^ 64;
  u32x4 hb[3][4];
  s16x8 wf[2][4];
  f32x16 acc[2][2];
#pragma unroll
  for (int i = 0; i < 16; ++i) { acc[0][0][i] = 0.f; acc[0][1][i] = 0.f; acc[1][0][i] = 0.f; acc[1][1][i] = 0.f; }
  await_<1>(lds, C_PUB, 8);
  K2_DMA(2); K2_DMA(3);
  STAMP();
  if (kq == 0) {
    const u64* fp = (const u64*)(flg + FL_H) + (lane < 14 ? lane : 0);
    for (unsigned spin = 0; spin < (1u << 20); ++spin) {
      u64 v;
      asm volatile("global_load_dwordx2 %0, %1, off sc1\n\ts_waitcnt vmcnt(0)" : "=&v"(v) : "v"(fp) : "memory");
      if (__all(v == aOwn + 1)) break;
      __builtin_amdgcn_s_sleep(2);
    }
    lds_set(cnt + C_H, lane);
  } else {
    lds_wait(cnt + C_H);
  }
  STAMP();
#define K2_HL(S, s2) { const char* hs_ = hbase + K2S(S) * 2048 + (s2) * 1024; gload16(hb[(S) % 3][2 * (s2)], hoff0, hs_); gload16(hb[(S) % 3][2 * (s2) + 1], hoff1, hs_); SCHED(); }
#define K2_HLD(S) { K2_HL(S, 0); K2_HL(S, 1); }
#define K2_TR(S, i) { const char* sl_ = ring + ((S) % K2_R) * 4096 + ((i) >> 1) * 2048 + (((i) & 1) ? toff1 : toff0); wf[(S) & 1][i] = tr_frag(sl_, sl_ + 1024); SCHED(); }
#define K2_D1(S, pp) { dma16s(wvoff, wsrc + (size_t)(K2S(S) * 32 + (pp) * 8) * D_, ringa + ((S) % K2_R) * 4096 + (pp) * 1024); SCHED(); }
#define K2_M(S, s2, mi, di) { acc[mi][di] = __builtin_amdgcn_mfma_f32_32x32x16_bf16(wf[(S) & 1][2 * (s2) + (di)], __builtin_bit_cast(s16x8, hb[(S) % 3][2 * (s2) + (mi)]), acc[mi][di], 0, 0, 0); SCHED(); }
#define K2_STEP(S, W, TRN, HN, DN) { \
    asm volatile("s_waitcnt vmcnt(" #W ")" : "+v"(hb[(S) % 3][0]), "+v"(hb[(S) % 3][1]), "+v"(hb[(S) % 3][2]), "+v"(hb[(S) % 3][3]) :: "memory"); SCHED(); \
    K2_M(S,0,0,0); if (TRN) K2_TR((S)+1,0); K2_M(S,0,1,0); if (TRN) K2_TR((S)+1,1); K2_M(S,0,0,1); if (TRN) K2_TR((S)+1,2); K2_M(S,0,1,1); if (TRN) K2_TR((S)+1,3); \
    K2_M(S,1,0,0); if (DN) { K2_D1((S)+4,0); K2_D1((S)+4,1); } K2_M(S,1,1,0); if (DN) { K2_D1((S)+4,2); K2_D1((S)+4,3); } K2_M(S,1,0,1); if (HN) K2_HL((S)+3,0); K2_M(S,1,1,1); if (HN) K2_HL((S)+3,1); }
  K2_HLD(0); K2_HLD(1); K2_HLD(2);
  await_<0>(lds, C_W2, 4);
  K2_TR(0,0); K2_TR(0,1); K2_TR(0,2); K2_TR(0,3);
  K2_STEP(0, 8, 1, 1, 1); STAMP();
  K2_STEP(1, 12, 1, 1, 1);
  K2_STEP(2, 16, 1, 1, 1);
  K2_STEP(3, 16, 1, 1, 1);
  K2_STEP(4, 16, 1, 1, 1);
  K2_STEP(5, 16, 1, 1, 1);
  K2_STEP(6, 16, 1, 1, 1);
  K2_STEP(7, 16, 1, 1, 1); STAMP();
  K2_STEP(8, 16, 1, 1, 1);
  if (kq == 0) lds_set(cnt + C_G8, lane);
  K2_STEP(9, 16, 1, 1, 1);
  K2_STEP(10, 16, 1, 1, 0);
  K2_STEP(11, 12, 1, 0, 0);
  K2_STEP(12, 4, 1, 0, 0);
  K2_STEP(13, 0, 0, 0, 0);
  STAMP();
  await_<0>(lds, C_SP, 4);
  {
    char* dst = lds + L_RED + (kq * 64 + r) * RED_RS + h * 16;
#pragma unroll
    for (int mi = 0; mi < 2; ++mi)
#pragma unroll
      for (int di = 0; di < 2; ++di)
#pragma unroll
        for (int rq = 0; rq < 4; ++rq)
          *(f32x4*)(dst + mi * 32 * RED_RS + di * 128 + rq * 32) = (f32x4){acc[mi][di][4 * rq], acc[mi][di][4 * rq + 1], acc[mi][di][4 * rq + 2], acc[mi][di][4 * rq + 3]};
  }
  ARRIVE_L(C_R1);
  STAMP();
  await_<0>(lds, C_ST, 4);
  u32x4 xv = *(const u32x4*)(lds + L_XT + tid * 16), bv = *(const u32x4*)(lds + L_XT + 4096 + (tid & 7) * 16);
  final_stage(xv, bv, out, lds, mb, cb, tid);
  if (tid == 0) __hip_atomic_store((u64*)(ws + WS_EP) + blockIdx.x, aOwn + 1, RLX_AGENT);
  STAMP(); STAMP_FLUSH();
}

DI void p2_helper(const u16* __restrict__ x, const u16* __restrict__ w2, u64 aOwn, const u16* __restrict__ b2, u16* __restrict__ out, char* __restrict__ ws,
                  char* lds, int mb, int cb, int gw, int lane) {
  unsigned* cnt = (unsigned*)(lds + L_CNT);
  const int tid = gw * 64 + lane, hw = gw - 4;
  u32x4 xv, bv, x2;
  await_<2>(lds, C_PUB, 8);
  FIN_LOADS();
  asm volatile("global_load_dwordx4 %0, %3, off\n\ts_waitcnt vmcnt(0)" : "=&v"(x2), "+v"(xv), "+v"(bv) : "v"(x + (size_t)(64 * mb + ((tid - 256) >> 3)) * D_ + 64 * cb + 8 * (tid & 7)) : "memory");
  *(u32x4*)(lds + L_XT + (tid - 256) * 16) = x2;
  if (tid < 264) *(u32x4*)(lds + L_XT + 4096 + (tid & 7) * 16) = bv;
  if (hw == 0) {
    await_<8>(lds, C_SP, 4);
    while (__hip_atomic_load(cnt + C_G8, RLX_WG) == 0u) __builtin_amdgcn_s_sleep(8);
    const u64* fp = (const u64*)(ws + WS_FLAG + (size_t)blockIdx.x * 1024 + FL_S) + (lane & 31);
    for (unsigned spin = 0; spin < (1u << 18); ++spin) {
      const u64 v = __hip_atomic_load(fp, RLX_AGENT);
      if (__all(v == aOwn + 1)) break;
      __builtin_amdgcn_s_sleep(8);
    }
    asm volatile("" ::: "memory");
    lds_set(cnt + C_S, lane);
  } else {
    while (__hip_atomic_load(cnt + C_S, RLX_WG) == 0u) __builtin_amdgcn_s_sleep(4);
    asm volatile("" ::: "memory");
  }
  u32x4 sa, sb;
  const char* sp = ws + WS_S + (size_t)(64 * cb + 16 * hw) * 4096 + 64 * mb * 2;
  gload16(sa, (lane >> 3) * 4096 + (lane & 7) * 16, sp);
  gload16(sb, (lane >> 3) * 4096 + (lane & 7) * 16, sp + 8 * 4096);
  asm volatile("s_waitcnt vmcnt(0)" : "+v"(sa), "+v"(sb) :: "memory");
  char* dst = lds + L_ST + (8 * (lane & 7)) * ST_RS + (16 * hw + (lane >> 3)) * 2;
#pragma unroll
  for (int c = 0; c < 4; ++c) {
    *(u16*)(dst + (2 * c) * ST_RS) = (u16)sa[c]; *(u16*)(dst + (2 * c + 1) * ST_RS) = (u16)(sa[c] >> 16);
    *(u16*)(dst + (2 * c) * ST_RS + 16) = (u16)sb[c]; *(u16*)(dst + (2 * c + 1) * ST_RS + 16) = (u16)(sb[c] >> 16);
  }
  ARRIVE_L(C_ST);
  while (__hip_atomic_load(cnt + C_R1, RLX_WG) < 4u) __builtin_amdgcn_s_sleep(2);
  final_stage(xv, bv, out, lds, mb, cb, tid);
}

template <int PH>
__global__ __launch_bounds__(768, 3) void fused_kernel(const u16* __restrict__ x, const u16* __restrict__ lar, const u16* __restrict__ aim,
                                                       const u16* __restrict__ cre, const u16* __restrict__ cim, const u16* __restrict__ ldt,
                                                       const u16* __restrict__ dsk, const u16* __restrict__ w1, const u16* __restrict__ b1,
                                                       const u16* __restrict__ w2, const u16* __restrict__ b2, u16* __restrict__ out,
                                                       char* __restrict__ ws) {
  extern __shared__ __attribute__((aligned(16))) char lds[];
  const int lane = threadIdx.x & 63;
  const int wid = __builtin_amdgcn_readfirstlane(threadIdx.x >> 6);
  const int b = blockIdx.x, xcd = b & 7, j = b >> 3;
  const int mb = 4 * xcd + (j >> 3), cb = j & 7;
  const int jj = j < 28 ? j : 0;
  const int panel = 4 * (xcd >> 1) + jj / 7, fs = 7 * (xcd & 1) + jj % 7;
  if (wid < 4) {
    const int de = 64 * cb + 2 * mb;
    const unsigned sdt = *(const __attribute__((address_space(4))) unsigned*)(ldt + de);
    const unsigned sds = *(const __attribute__((address_space(4))) unsigned*)(dsk + de);
    x_wave(lar, aim, cre, cim, sdt, sds, w2, w1, b1, j >= 28 ? xcd * 4 + (j - 28) : -1, panel, fs, ws, lds, mb, cb, wid, lane TK0_PASS);
    return;
  }
  const int gw = wid - 4;
  __builtin_amdgcn_s_setprio(2);
  if (j < 28) p1_gemm(x, w1, ws, lds, panel, fs, gw, lane TK0_PASS);
  else p1_aux(x, ws, lds, xcd * 4 + (j - 28), gw, lane TK0_PASS);
  __builtin_amdgcn_s_setprio(0);
  await_<0>(lds, C_EP, 1);
  const u64 aOwn = *(const u64*)(lds + L_KSC + 16);
  if (gw < 4) p2_gemm(x, w2, aOwn, b2, out, ws, lds, mb, cb, gw, lane TK0_PASS);
  else p2_helper(x, w2, aOwn, b2, out, ws, lds, mb, cb, gw, lane);
}

extern "C" void kernel_launch(void* const* d_in, const int* in_sizes, int n_in, void* d_out, int out_size, void* d_ws, size_t ws_size,
                              hipStream_t stream) {
  const u16* x = (const u16*)d_in[0];
  const u16* lar = (const u16*)d_in[1];
  const u16* aim = (const u16*)d_in[2];
  const u16* cre = (const u16*)d_in[3];
  const u16* cim = (const u16*)d_in[4];
  const u16* ldt = (const u16*)d_in[5];
  const u16* dsk = (const u16*)d_in[6];
  const u16* w1 = (const u16*)d_in[8];
  const u16* b1 = (const u16*)d_in[9];
  const u16* w2 = (const u16*)d_in[10];
  const u16* b2 = (const u16*)d_in[11];
  (void)hipFuncSetAttribute((const void*)fused_kernel<3>, hipFuncAttributeMaxDynamicSharedMemorySize, LDS_TOTAL);
  fused_kernel<3><<<256, 768, LDS_TOTAL, stream>>>(x, lar, aim, cre, cim, ldt, dsk, w1, b1, w2, b2, (u16*)d_out, (char*)d_ws);
}
```

```cpp
#include <hip/hip_runtime.h>
#include <stdint.h>

typedef unsigned short u16;
typedef unsigned long long u64;
typedef __attribute__((ext_vector_type(4))) short s16x4;
typedef __attribute__((ext_vector_type(8))) short s16x8;
typedef __attribute__((ext_vector_type(8))) _Float16 f16x8;
typedef __attribute__((ext_vector_type(2))) _Float16 f16x2;
typedef __attribute__((ext_vector_type(2))) __bf16 bf16x2;
typedef __attribute__((ext_vector_type(2))) float f32x2;
typedef __attribute__((ext_vector_type(4))) float f32x4;
typedef __attribute__((ext_vector_type(16))) float f32x16;
typedef __attribute__((ext_vector_type(4))) unsigned u32x4;
typedef __attribute__((ext_vector_type(2))) unsigned u32x2;
#define LDSP __attribute__((address_space(3)))
#define GLBP __attribute__((address_space(1)))
#define DI __device__ __forceinline__

#define L_ 2048
#define D_ 512
#define N_ 128
#define F_ 1792

#define WS_H 0
#define WS_UR (8u << 20)
#define WS_S (10u << 20)
#define WS_STAT (12u << 20)
#define WS_CS ((12u << 20) + 65536)
#define WS_ST2 ((12u << 20) + 131072)
#define WS_FLAG (13u << 20)
#define WS_EP (14u << 20)
#define FL_LN 128
#define FL_H 384
#define FL_S 512
#define FL_ST 768

DI float bf2f(unsigned v) { return __uint_as_float(v << 16); }
DI float bflo(unsigned v) { return __uint_as_float(v << 16); }
DI float bfhi(unsigned v) { return __uint_as_float(v & 0xffff0000u); }
DI unsigned pkbf(float a, float b) { bf16x2 q = {(__bf16)a, (__bf16)b}; return __builtin_bit_cast(unsigned, q); }
DI float rbf(float a) { __bf16 q = (__bf16)a; return (float)q; }
DI unsigned pkh(float a, float b) { f32x2 v = {a, b}; f16x2 p = __builtin_convertvector(v, f16x2); return __builtin_bit_cast(unsigned, p); }
DI float dot2(unsigned a, unsigned b, float c) { return __builtin_amdgcn_fdot2_f32_bf16(__builtin_bit_cast(bf16x2, a), __builtin_bit_cast(bf16x2, b), c, false); }
template <class T> DI T sel(int c, T a, T b) { return c ? a : b; }
DI void dma16s(unsigned voff, const void* sbase, unsigned ldsaddr) {
  asm volatile("s_mov_b32 m0, %2\n\ts_nop 0\n\tglobal_load_lds_dwordx4 %0, %1" :: "v"(voff), "s"(sbase), "s"(ldsaddr) : "memory");
}
DI unsigned lds_base(char* lds) { return __builtin_amdgcn_readfirstlane((unsigned)(size_t)(LDSP char*)lds); }
DI s16x8 tr_frag(const char* p0, const char* p1) {
  s16x4 a = __builtin_amdgcn_ds_read_tr16_b64_v4i16((LDSP s16x4*)p0);
  s16x4 b = __builtin_amdgcn_ds_read_tr16_b64_v4i16((LDSP s16x4*)p1);
  return __builtin_shufflevector(a, b, 0, 1, 2, 3, 4, 5, 6, 7);
}
#define SCHED() __builtin_amdgcn_sched_barrier(0)
#define WAITV_BAR(n) do { SCHED(); asm volatile("s_waitcnt vmcnt(" #n ") lgkmcnt(0)\n\ts_barrier" ::: "memory"); SCHED(); } while (0)
#define LGKM_BAR() do { SCHED(); asm volatile("s_waitcnt lgkmcnt(0)\n\ts_barrier" ::: "memory"); SCHED(); } while (0)
#define RLX_AGENT __ATOMIC_RELAXED, __HIP_MEMORY_SCOPE_AGENT
#define RLX_WG __ATOMIC_RELAXED, __HIP_MEMORY_SCOPE_WORKGROUP

template <int CTRL, int RM> DI float dpp_add(float v) {
  return v + __builtin_bit_cast(float, __builtin_amdgcn_update_dpp(0, __builtin_bit_cast(int, v), CTRL, RM, 0xf, true));
}
DI float wave_sum63(float v) {
  v = dpp_add<0x111, 0xf>(v); v = dpp_add<0x112, 0xf>(v); v = dpp_add<0x114, 0xf>(v); v = dpp_add<0x118, 0xf>(v);
  v = dpp_add<0x142, 0xa>(v); v = dpp_add<0x143, 0xc>(v);
  return v;
}
DI float comb32(float a, float b) { auto r = __builtin_amdgcn_permlane32_swap(__float_as_uint(a), __float_as_uint(b), false, false); return __uint_as_float(r[0]) + __uint_as_float(r[1]); }
DI float comb16(float a, float b) { auto r = __builtin_amdgcn_permlane16_swap(__float_as_uint(a), __float_as_uint(b), false, false); return __uint_as_float(r[0]) + __uint_as_float(r[1]); }
DI float row_allsum(float v) { v = dpp_add<0x128, 0xf>(v); v = dpp_add<0x124, 0xf>(v); v = dpp_add<0x122, 0xf>(v); v = dpp_add<0x121, 0xf>(v); return v; }
DI float rdlane(float v, int l) { return __builtin_bit_cast(float, __builtin_amdgcn_readlane(__builtin_bit_cast(int, v), l)); }

DI void poll_flags(const u64* p, u64 expect) {
  for (unsigned spin = 0; spin < (1u << 20); ++spin) {
    const u64 v = __hip_atomic_load(p, RLX_AGENT);
    if (__all(v == expect)) break;
    __builtin_amdgcn_s_sleep(2);
  }
  asm volatile("" ::: "memory");
}
DI void lds_set(unsigned* w, int lane) { if (lane == 0) __hip_atomic_store(w, 1u, RLX_WG); }
DI void lds_wait(unsigned* w) { while (__hip_atomic_load(w, RLX_WG) == 0u) __builtin_amdgcn_s_sleep(1); asm volatile("" ::: "memory"); }
DI int blk_of(int mb, int cb) { return (mb >> 2) + 8 * (((mb & 3) << 3) | cb); }

#define LDS_TOTAL 163840
#define L_STAT 162304
#define L_BIAS 163328
#define L_ACON 163584
#define L_KSC 163712
#define L_CNT 163744
#define KAPSZ 2576
#define L_KAP 141696
#define L_COLS 152000
#define K1_SLOT 32768
#define L_KP 131584
#define KPSZ 4160
#define K2_R 4
#define L_RING2 0
#define L_W 65536
#define WSZ 33024
#define L_U L_KAP
#define USZ 4608
#define L_RED L_W
#define RED_RS 272
#define L_ST L_KAP
#define ST_RS 144
#define C_PUB 0
#define C_SP 1
#define C_H 2
#define C_S 3
#define C_W2 4
#define C_R1 5
#define C_ST 6
#define C_K0 7
#define C_K1 9
#define C_K2 12
#define C_K3 14
#define C_LN1 16
#define C_LN2 17
#define C_X2 18
#define C_CS 19
#define C_EP 20
#define C_G8 21
#define L_XT 153088
DI void arrive(char* lds, int c, int lane) { if (lane == 0) __hip_atomic_fetch_add((unsigned*)(lds + L_CNT) + c, 1u, RLX_WG); }
template <int SL> DI void await_(char* lds, int c, unsigned target) {
  while (__hip_atomic_load((unsigned*)(lds + L_CNT) + c, RLX_WG) < target) __builtin_amdgcn_s_sleep(SL);
  asm volatile("" ::: "memory"); SCHED();
}
#define ARRIVE_V(n, c) do { SCHED(); asm volatile("s_waitcnt vmcnt(" #n ") lgkmcnt(0)" ::: "memory"); arrive(lds, c, lane); SCHED(); } while (0)
#define ARRIVE_L(c) do { SCHED(); asm volatile("s_waitcnt lgkmcnt(0)" ::: "memory"); arrive(lds, c, lane); SCHED(); } while (0)

struct cf { float x, y; };
DI cf cmul(cf a, cf b) { return cf{fmaf(a.x, b.x, -(a.y * b.y)), fmaf(a.x, b.y, a.y * b.x)}; }
DI unsigned pkc(cf a) { return pkh(a.x, a.y); }
DI unsigned pkcc(cf a) { return pkh(a.x, -a.y); }
DI cf zpow(float al, float om, float p) {
  const float m = __builtin_amdgcn_exp2f(al * p), ph = __builtin_amdgcn_fractf(om * p);
  return cf{m * __builtin_amdgcn_cosf(ph), m * __builtin_amdgcn_sinf(ph)};
}
DI unsigned cmulh(unsigned a, unsigned b) {
  const f16x2 z = __builtin_bit_cast(f16x2, a), w = __builtin_bit_cast(f16x2, b);
  const f16x2 t = z * (f16x2){w[0], w[0]};
  const f16x2 r = __builtin_elementwise_fma((f16x2){-z[1], z[0]}, (f16x2){w[1], w[1]}, t);
  return __builtin_bit_cast(unsigned, r);
}
DI u32x4 cmulh4(u32x4 a, u32x4 b) { return (u32x4){cmulh(a[0], b[0]), cmulh(a[1], b[1]), cmulh(a[2], b[2]), cmulh(a[3], b[3])}; }
#define TB_RS 272
#define TB_N 33
DI void kgen_tables(unsigned (&T)[TB_N], float& isc, unsigned raw0, unsigned raw1, float ldtv, char* lds, int cw, int lane) {
  const int src = ((lane & 31) >> 1) * 4, sh = (lane & 1) ? 0 : 16;
  unsigned pv[4];
#pragma unroll
  for (int a = 0; a < 4; ++a) {
    const unsigned lo = (unsigned)__builtin_amdgcn_ds_bpermute(src + 64 * a, (int)raw0), hi = (unsigned)__builtin_amdgcn_ds_bpermute(src + 64 * a, (int)raw1);
    pv[a] = ((lane < 32 ? lo : hi) << sh) & 0xffff0000u;
  }
  const float dt = rbf(__builtin_amdgcn_exp2f(ldtv * 1.4426950408889634f));
  const float ar = rbf(__builtin_amdgcn_exp2f(__uint_as_float(pv[0]) * 1.4426950408889634f));
  const float al = -(ar * dt) * 1.4426950408889634f;
  const float om = (__uint_as_float(pv[1]) * dt) * 0.15915494309189535f;
  const float cr = __uint_as_float(pv[2]) * dt, ci = __uint_as_float(pv[3]) * dt;
  const float sabs = 2.f * rdlane(wave_sum63(fabsf(cr) + fabsf(ci)), 63);
  int ex = (int)((__float_as_uint(sabs) >> 23) & 255);
  ex = ex < 20 ? 20 : (ex > 230 ? 230 : ex);
  const float sc2 = __uint_as_float((unsigned)(268 - ex) << 23);
  isc = __uint_as_float((unsigned)(ex - 13) << 23);
  if (lane == 0) *(float*)(lds + L_KSC + cw * 4) = sabs;
  const cf z1 = zpow(al, om, 1.f), z8 = zpow(al, om, 8.f), z32 = zpow(al, om, 32.f), a1 = zpow(al, om, 128.f), a4 = zpow(al, om, 512.f);
  const unsigned one = 0x00003C00u;
  const cf a2 = cmul(a1, a1), a3 = cmul(a2, a1), a8 = cmul(a4, a4), a12 = cmul(a8, a4);
  T[0] = one; T[1] = pkc(a1); T[2] = pkc(a2); T[3] = pkc(a3);
  T[4] = pkc(a4); T[5] = pkc(cmul(a4, a1)); T[6] = pkc(cmul(a4, a2)); T[7] = pkc(cmul(a4, a3));
  T[8] = pkc(a8); T[9] = pkc(cmul(a8, a1)); T[10] = pkc(cmul(a8, a2)); T[11] = pkc(cmul(a8, a3));
  T[12] = pkc(a12); T[13] = pkc(cmul(a12, a1)); T[14] = pkc(cmul(a12, a2)); T[15] = pkc(cmul(a12, a3));
  T[16] = pkc(cmul(a8, a8));
  const cf e2 = cmul(z1, z1), e3 = cmul(e2, z1), e4 = cmul(e2, e2);
  T[17] = one; T[18] = pkc(z1); T[19] = pkc(e2); T[20] = pkc(e3); T[21] = pkc(e4);
  T[22] = pkc(cmul(e4, z1)); T[23] = pkc(cmul(e4, e2)); T[24] = pkc(cmul(e4, e3));
  const cf c0 = cf{cr * sc2, ci * sc2}, c2 = cmul(c0, cmul(z32, z32));
  T[25] = pkcc(c0); T[26] = pkcc(cmul(c0, z32)); T[27] = pkcc(c2); T[28] = pkcc(cmul(c2, z32));
  const cf z16 = cmul(z8, z8);
  T[29] = one; T[30] = pkcc(z8); T[31] = pkcc(z16); T[32] = pkcc(cmul(z16, z8));
}
#define KG_PARAMS f32x4& kX, f32x4& kY, f32x4& kZ, float isc
DI void kgen_store(KG_PARAMS, char* lds, int wid, int lane) {
  const int i16 = lane & 15, g = lane >> 4;
  float* kap = (float*)(lds + L_KAP + wid * KAPSZ);
  *(f32x4*)(kap + 128 * (g >> 1) + 4 * (g & 1) + 8 * i16) = kX * isc;
  if (g < 2) {
    *(f32x4*)(kap + 256 + 64 * g + 4 * i16) = kY * isc;
#pragma unroll
    for (int r = 0; r < 4; ++r) kap[512 + 16 * (4 * g + r) + i16] = kZ[r] * isc;
  } else {
    *(f32x2*)(kap + 384 + 64 * (g - 2) + 2 * i16) = (f32x2){kY[0] * isc, kY[1] * isc};
    *(f32x2*)(kap + 384 + 64 * (g - 2) + 32 + 2 * i16) = (f32x2){kY[2] * isc, kY[3] * isc};
    if (g == 2 && i16 == 0) kap[640] = kZ[0] * isc;
  }
}
DI const u16* prm_ptr(const u16* lar, const u16* aim, const u16* cre, const u16* cim, int d, int hf, int lane) {
  const int g = lane >> 4;
  const u16* p = g == 0 ? lar : (g == 1 ? aim : (g == 2 ? cre : cim));
  return p + d * N_ + 64 * hf + 2 * (lane & 15);
}
DI void ld_u32(unsigned& v, const void* p) { asm volatile("global_load_dword %0, %1, off" : "=v"(v) : "v"(p) : "memory"); }
DI void ld_u64(u64& v, const void* p) { asm volatile("global_load_dwordx2 %0, %1, off sc1" : "=v"(v) : "v"(p) : "memory"); }
DI void gload16(u32x4& v, unsigned voff, const char* sbase) {
  asm volatile("global_load_dwordx4 %0, %1, %2 sc1" : "=v"(v) : "v"(voff), "s"(sbase) : "memory");
}
DI u64 rdlane64(u64 v, int l) { return (u64)(unsigned)__builtin_amdgcn_readlane((int)(unsigned)v, l) | ((u64)(unsigned)__builtin_amdgcn_readlane((int)(unsigned)(v >> 32), l) << 32); }
DI bool last_of(char* lds, int c, unsigned n, int lane) {
  unsigned old = 0;
  if (lane == 0) old = __hip_atomic_fetch_add((unsigned*)(lds + L_CNT) + c, 1u, RLX_WG);
  return (unsigned)__builtin_amdgcn_readfirstlane(old) == n - 1;
}
DI float gelu_f(float v) {
  const float c0 = -2.302208198f;
  const float c1 = -0.1029432397f;
  const float z = v * fmaf(v * v, c1, c0);
  return v * __builtin_amdgcn_rcpf(1.f + __builtin_amdgcn_exp2f(z));
}
#define K2S(S) ((S) + r2 >= 14 ? (S) + r2 - 14 : (S) + r2)
#define K2_DMA(S) { _Pragma("unroll") for (int pp = 0; pp < 4; ++pp) dma16s(wvoff, wsrc + (size_t)(K2S(S) * 32 + pp * 8) * D_, ringa + ((S) % K2_R) * 4096 + pp * 1024); SCHED(); }
#define K2_SETUP(kq_) int lane2_ = lane; asm volatile("" : "+v"(lane2_)); \
  const unsigned wvoff = (lane2_ >> 3) * (D_ * 2) + (((lane2_ & 7) ^ (((lane2_ >> 4) & 1) << 2)) << 4); \
  const unsigned ringa = __builtin_amdgcn_readfirstlane(lds_base(lds) + L_RING2 + (kq_) * (K2_R * 4096)); \
  const u16* wsrc = w2 + (size_t)(14 * (kq_) * 32) * D_ + 64 * cb; \
  const int r2 = (2 * cb + 4 * (mb & 3)) % 14;
#define HMT 114688

DI void p1_gemm(const u16* __restrict__ x, const u16* __restrict__ w1, char* __restrict__ ws, char* lds, int panel, int fs, int wid, int lane) {
  const int wm = wid >> 2, wf = (wid >> 1) & 1, kh = wid & 1;
  const int r = lane & 31, h = lane >> 5;
  const int row0 = panel * 128, f0 = fs * 128;
  const unsigned xv0 = (lane >> 3) * 1024 + (((lane & 7) ^ ((lane >> 4) & 3)) << 4);
  const unsigned xv1 = xv0 ^ 64;
  const unsigned wv = (lane >> 4) * (F_ * 2) + (((lane & 15) ^ (((lane >> 4) & 3) << 2)) << 4);
  const u16* xsb = x + (size_t)(row0 + 16 * wid) * D_;
  const u16* wsb = w1 + (size_t)(8 * wid) * F_ + f0;
  const unsigned la = lds_base(lds) + wid * 2048;
  const int rot = 2 * (panel & 3) + (fs >= 7 ? fs - 7 : fs);
#define KS(S) (((S) + rot) & 7)
#define K1_D0(S) { dma16s(xv0, xsb + KS(S) * 64, la + ((S) % 4) * K1_SLOT); SCHED(); }
#define K1_D1(S) { dma16s(xv1, xsb + 8 * D_ + KS(S) * 64, la + ((S) % 4) * K1_SLOT + 1024); SCHED(); }
#define K1_D2(S) { dma16s(wv, wsb + (size_t)(KS(S) * 64) * F_, la + ((S) % 4) * K1_SLOT + 16384); SCHED(); }
#define K1_D3(S) { dma16s(wv, wsb + (size_t)(KS(S) * 64 + 4) * F_, la + ((S) % 4) * K1_SLOT + 16384 + 1024); SCHED(); }
#define K1_ISSUE(S) { K1_D0(S); K1_D1(S); K1_D2(S); K1_D3(S); }
  K1_ISSUE(0); K1_ISSUE(1); K1_ISSUE(2); K1_ISSUE(3);
  const int xoff = (64 * wm + r) * 128 + (((4 * kh + 2 * wm + h) ^ ((r >> 1) & 7)) << 4);
  const int q = (lane & 15) >> 2, p = lane & 3;
  const int woff0 = 16384 + (32 * kh + 16 * wm + 8 * h + q) * 256 + (((2 * wf + kh) ^ q) << 6) + ((lane >> 4) & 1) * 32 + 8 * p;
  f32x16 acc[2][2];
#pragma unroll
  for (int i = 0; i < 16; ++i) { acc[0][0][i] = 0.f; acc[0][1][i] = 0.f; acc[1][0][i] = 0.f; acc[1][1][i] = 0.f; }
  s16x8 xf[2][2][2], wfr[2][2][2];
#define K1_M(T, s, mi, fi) { acc[mi][fi] = __builtin_amdgcn_mfma_f32_32x32x16_bf16(wfr[(T) & 1][s][fi], xf[(T) & 1][s][mi], acc[mi][fi], 0, 0, 0); SCHED(); }
#define K1_RX(T, s, mi) { xf[(T) & 1][s][mi] = *(const s16x8*)(lds + (xa_ ^ ((s) << 5)) + (mi) * 4096); SCHED(); }
#define K1_RW(T, s, fi) { wfr[(T) & 1][s][fi] = tr_frag(lds + (wa_ ^ (((fi) << 6) | ((s) << 12))), lds + (wa_ ^ (((fi) << 6) | ((s) << 12))) + 1024); SCHED(); }
#define K1_ADDR(T) int xa_ = xoff + ((T) % 4) * K1_SLOT, wa_ = woff0 + ((T) % 4) * K1_SLOT; asm volatile("" : "+v"(xa_), "+v"(wa_));
#define K1_READ(T) { K1_ADDR(T) K1_RX(T,0,0); K1_RW(T,0,0); K1_RW(T,0,1); K1_RX(T,0,1); K1_RX(T,1,0); K1_RW(T,1,0); K1_RW(T,1,1); K1_RX(T,1,1); }
#define K1_MMA(T) { K1_M(T,0,0,0); K1_M(T,0,0,1); K1_M(T,0,1,0); K1_M(T,0,1,1); K1_M(T,1,0,0); K1_M(T,1,0,1); K1_M(T,1,1,0); K1_M(T,1,1,1); }
#define K1_STAGE(T, DM0, DM1, DM2, DM3) { K1_ADDR(T) \
    K1_M((T)-1,0,0,0); K1_RX(T,0,0); K1_RW(T,0,0); K1_M((T)-1,0,0,1); K1_RW(T,0,1); K1_RX(T,0,1); \
    K1_M((T)-1,0,1,0); K1_RX(T,1,0); K1_RW(T,1,0); K1_M((T)-1,0,1,1); K1_RW(T,1,1); K1_RX(T,1,1); \
    K1_M((T)-1,1,0,0); DM0; K1_M((T)-1,1,0,1); DM1; K1_M((T)-1,1,1,0); DM2; K1_M((T)-1,1,1,1); DM3; }
#define K1_STAGE_D(T, S) K1_STAGE(T, K1_D0(S), K1_D1(S), K1_D2(S), K1_D3(S))
  WAITV_BAR(12);
  K1_READ(0);
  WAITV_BAR(8); K1_STAGE_D(1, 4);
  WAITV_BAR(8); K1_STAGE_D(2, 5);
  WAITV_BAR(8); K1_STAGE_D(3, 6);
  WAITV_BAR(8); K1_STAGE_D(4, 7);
  WAITV_BAR(8); K1_STAGE(5, , , , );
  WAITV_BAR(4); K1_STAGE(6, , , , );
  K1_MMA(6);
  WAITV_BAR(0);
  K1_READ(7); K1_MMA(7);
  float* colS = (float*)(lds + L_COLS);
  {
    char* dst = lds + wid * 8192 + lane * 16;
#pragma unroll
    for (int mi = 0; mi < 2; ++mi)
#pragma unroll
      for (int rq = 0; rq < 4; ++rq)
        *(f32x4*)(dst + (mi * 4 + rq) * 1024) = (f32x4){acc[mi][1][4 * rq], acc[mi][1][4 * rq + 1], acc[mi][1][4 * rq + 2], acc[mi][1][4 * rq + 3]};
  }
  LGKM_BAR();
  f32x4 pa[2][4], cq[4];
  f32x2 st[2];
  u32x2 bq[4];
  {
    const char* src = lds + (wid ^ 1) * 8192 + lane * 16;
    const int fl = 64 * wf + 32 * kh + 4 * h;
#pragma unroll
    for (int rq = 0; rq < 4; ++rq) {
      cq[rq] = *(const f32x4*)(colS + fl + 8 * rq);
      bq[rq] = *(const u32x2*)(lds + L_BIAS + (fl + 8 * rq) * 2);
      pa[0][rq] = *(const f32x4*)(src + rq * 1024); pa[1][rq] = *(const f32x4*)(src + (4 + rq) * 1024);
    }
    st[0] = *(const f32x2*)(lds + L_STAT + (64 * wm + r) * 8); st[1] = *(const f32x2*)(lds + L_STAT + (64 * wm + 32 + r) * 8);
  }
  LGKM_BAR();
  {
    const __amdgpu_buffer_rsrc_t hr = __builtin_amdgcn_make_buffer_rsrc(ws + WS_H, 0, L_ * F_ * 2, 0x00020000);
    const unsigned hoff = (unsigned)(panel * 4 + 2 * wm) * HMT + (8 * fs + 4 * wf + 2 * kh) * 1024 + lane * 16;
#pragma unroll
    for (int mi = 0; mi < 2; ++mi) {
      const float mr = st[mi][0], rinv = st[mi][1];
#pragma unroll
      for (int s2 = 0; s2 < 2; ++s2) {
        u32x4 pk;
#pragma unroll
        for (int qq = 0; qq < 2; ++qq) {
          const int rq = 2 * s2 + qq;
          const float b0 = bflo(bq[rq][0]), b1_ = bfhi(bq[rq][0]), b2_ = bflo(bq[rq][1]), b3 = bfhi(bq[rq][1]);
          const float o0 = gelu_f(fmaf(acc[mi][0][4 * rq] + pa[mi][rq][0], rinv, fmaf(mr, cq[rq][0], b0)));
          const float o1 = gelu_f(fmaf(acc[mi][0][4 * rq + 1] + pa[mi][rq][1], rinv, fmaf(mr, cq[rq][1], b1_)));
          const float o2 = gelu_f(fmaf(acc[mi][0][4 * rq + 2] + pa[mi][rq][2], rinv, fmaf(mr, cq[rq][2], b2_)));
          const float o3 = gelu_f(fmaf(acc[mi][0][4 * rq + 3] + pa[mi][rq][3], rinv, fmaf(mr, cq[rq][3], b3)));
          pk[2 * qq] = pkbf(o0, o1); pk[2 * qq + 1] = pkbf(o2, o3);
        }
        __builtin_amdgcn_raw_buffer_store_b128(pk, hr, hoff + mi * HMT + s2 * 1024, 0, 16);
        SCHED();
      }
    }
  }
  asm volatile("s_waitcnt vmcnt(0)" ::: "memory");
  if (last_of(lds, C_PUB, 8, lane) && lane < 16) {
    const u64 ac = *(const u64*)(lds + L_ACON + lane * 8);
    int l2 = lane; asm volatile("" : "+v"(l2));
    __hip_atomic_store((u64*)(ws + WS_FLAG + (size_t)blk_of(2 * panel + ((l2 >> 3) & 1), l2 & 7) * 1024 + FL_H) + fs, ac + 1, RLX_AGENT);
  }
}

#define LN_RS 1040
DI void p1_aux(const u16* __restrict__ x, char* __restrict__ ws, char* lds, int idx, int wid, int lane) {
  const int t0 = idx * 64;
  u32x4 v[8];
#pragma unroll
  for (int i = 0; i < 8; ++i) v[i] = *(const u32x4*)(x + (size_t)(t0 + 8 * wid + i) * D_ + lane * 8);
  const u64 aCon = __hip_atomic_load((const u64*)(ws + WS_EP) + ((wid * 64 + lane) & 255), RLX_AGENT);
  const int sfs = lane < 14 ? lane : 0;
  const int sblk = 2 * (idx >> 3) + (sfs >= 7 ? 1 : 0) + 8 * (((idx >> 1) & 3) * 7 + (sfs >= 7 ? sfs - 7 : sfs));
  const u64 aSt = __hip_atomic_load((const u64*)(ws + WS_EP) + sblk, RLX_AGENT);
  SCHED();
  asm volatile("s_barrier" ::: "memory");
  SCHED();
#pragma unroll
  for (int i = 0; i < 8; ++i) *(u32x4*)(lds + (63 - (8 * wid + i)) * LN_RS + lane * 16) = v[i];
  ARRIVE_L(C_LN1);
  float sA[8], sB[8];
#pragma unroll
  for (int i = 0; i < 8; ++i) {
    float a = dot2(v[i][0], 0x3F803F80u, 0.f), b = dot2(v[i][0], v[i][0], 0.f);
#pragma unroll
    for (int c = 1; c < 4; ++c) { a = dot2(v[i][c], 0x3F803F80u, a); b = dot2(v[i][c], v[i][c], b); }
    sA[i] = a; sB[i] = b;
  }
#pragma unroll
  for (int i = 0; i < 4; ++i) { sA[i] = comb32(sA[i], sA[i + 4]); sB[i] = comb32(sB[i], sB[i + 4]); }
#pragma unroll
  for (int i = 0; i < 2; ++i) { sA[i] = row_allsum(comb16(sA[i], sA[i + 2])); sB[i] = row_allsum(comb16(sB[i], sB[i + 2])); }
  const float mv = ((lane & 1) ? sA[1] : sA[0]) * (1.f / 512.f);
  const float var = fmaxf(fmaf(-mv, mv, ((lane & 1) ? sB[1] : sB[0]) * (1.f / 512.f)), 0.f);
  const float mbv = rbf(mv);
  const float sd = rbf(sqrtf(rbf(rbf(var) + 1e-5f)));
  const float rv = 1.f / sd;
  if ((lane & 14) == 0) {
    const u64 pk = (u64)__float_as_uint(-mbv * rv) | ((u64)__float_as_uint(rv) << 32);
    const int tt = t0 + 8 * wid + (lane & 1) + 2 * (lane >> 4);
    __hip_atomic_store((u64*)(ws + WS_STAT) + tt, pk, RLX_AGENT);
    __hip_atomic_store((unsigned*)(ws + WS_ST2) + (L_ - 1 - tt), (__float_as_uint(mbv) >> 16) | __float_as_uint(sd), RLX_AGENT);
  }
  await_<0>(lds, C_LN1, 8);
  char* t2 = lds + 64 * LN_RS + wid * 8192;
  const int g = lane >> 4, q = (lane & 15) >> 2, p = lane & 3, dl = lane & 15;
  s16x8 f[8];
#pragma unroll
  for (int i = 0; i < 8; ++i) {
    const char* a = lds + (8 * (4 * (i & 1) + g) + q) * LN_RS + (64 * wid + 16 * (i >> 1) + 4 * p) * 2;
    f[i] = tr_frag(a, a + 4 * LN_RS);
  }
  SCHED();
#pragma unroll
  for (int i = 0; i < 8; ++i) {
    const int dd2 = 16 * (i >> 1) + dl, rc = 4 * (i & 1) + g;
    *(s16x8*)(t2 + dd2 * 128 + ((rc ^ (dd2 & 7)) << 4)) = f[i];
  }
  SCHED();
  const __amdgpu_buffer_rsrc_t ur = __builtin_amdgcn_make_buffer_rsrc(ws + WS_UR, 0, L_ * D_ * 2, 0x00020000);
  const int x0 = L_ - 64 - t0;
  u32x4 o[8];
#pragma unroll
  for (int i = 0; i < 8; ++i) {
    const int dd2 = 8 * i + (lane >> 3), c = lane & 7;
    o[i] = *(const u32x4*)(t2 + dd2 * 128 + ((c ^ (dd2 & 7)) << 4));
  }
  SCHED();
  asm volatile("s_waitcnt vmcnt(0)" ::: "memory");
  if (last_of(lds, C_PUB, 8, lane) && lane < 14) __hip_atomic_store((u64*)(ws + WS_FLAG + (size_t)sblk * 1024 + FL_ST) + (idx & 1), aSt + 1, RLX_AGENT);
#pragma unroll
  for (int i = 0; i < 8; ++i)
    __builtin_amdgcn_raw_buffer_store_b128(o[i], ur, (unsigned)(64 * wid + 8 * i + (lane >> 3)) * 4096 + x0 * 2 + (lane & 7) * 16, 0, 16);
  ARRIVE_L(C_X2);
  ARRIVE_V(0, C_LN2);
  if (wid < 4) {
    await_<0>(lds, C_LN2, 8);
    __hip_atomic_store((u64*)(ws + WS_FLAG + (size_t)(wid * 64 + lane) * 1024 + FL_LN) + idx, aCon + 1, RLX_AGENT);
  }
}

DI void x_wave(const u16* __restrict__ lar, const u16* __restrict__ aim, const u16* __restrict__ cre, const u16* __restrict__ cim,
               unsigned sdt, unsigned sds, const u16* __restrict__ w2, const u16* __restrict__ w1, const u16* __restrict__ b1, int auxidx, int panel, int fs,
               char* __restrict__ ws, char* lds, int mb, int cb, int cw, int lane) {
  __builtin_amdgcn_s_setprio(3);
  const bool aux = auxidx >= 0;
  const int ch = cw >> 1, hf = cw & 1;
  const int d = 64 * cb + 2 * mb + ch;
  const int i16 = lane & 15, g = lane >> 4;
  unsigned* cnt = (unsigned*)(lds + L_CNT);
  const char* flg = ws + WS_FLAG + (size_t)blockIdx.x * 1024;
  const u16* pp_ = prm_ptr(lar, aim, cre, cim, d, hf, lane);
  unsigned raw0, raw1;
  u64 aS, aH;
  ld_u32(raw0, pp_); ld_u32(raw1, pp_ + 32);
  ld_u64(aS, ws + WS_EP + (lane >= 32 ? (int)blockIdx.x : blk_of(lane, cb)) * 8);
  ld_u64(aH, ws + WS_EP + blk_of(2 * panel + ((lane >> 3) & 1), lane & 7) * 8);
  if (!aux && cw == 2) asm volatile("s_mov_b32 m0, %2\n\ts_nop 0\n\tglobal_load_lds_dword %0, %1" :: "v"(lane * 4), "s"(b1 + fs * 128), "s"(lds_base(lds) + L_BIAS) : "memory");
  if (cw == 0 && lane < 24) cnt[lane] = 0u;
  if (aux) {
    asm volatile("s_waitcnt lgkmcnt(0)\n\ts_barrier" ::: "memory");
    __builtin_amdgcn_s_setprio(1);
    if (auxidx < 28) {
      const u16* wp_ = w1 + (size_t)(128 * cw + (lane >> 3)) * F_ + 64 * auxidx + 8 * (lane & 7);
      const int cblk = 2 * ((lane & 15) >> 2) + (auxidx >= 14 ? 1 : 0) + 8 * ((lane & 3) * 7 + ((auxidx >> 1) % 7));
      const u64 aC = __hip_atomic_load((const u64*)(ws + WS_EP) + cblk, RLX_AGENT);
      float a8[8] = {0.f, 0.f, 0.f, 0.f, 0.f, 0.f, 0.f, 0.f};
      u32x4 wv[16];
#pragma unroll
      for (int i = 0; i < 16; ++i) wv[i] = *(const u32x4*)(wp_ + (size_t)(8 * i) * F_);
      SCHED();
#pragma unroll
      for (int i = 0; i < 16; ++i)
#pragma unroll
        for (int c = 0; c < 4; ++c) { a8[2 * c] += bflo(wv[i][c]); a8[2 * c + 1] += bfhi(wv[i][c]); }
#pragma unroll
      for (int i = 0; i < 4; ++i) a8[i] = comb32(a8[i], a8[i + 4]);
#pragma unroll
      for (int i = 0; i < 2; ++i) a8[i] = dpp_add<0x128, 0xf>(comb16(a8[i], a8[i + 2]));
      if (!(lane & 8)) *(f32x2*)(lds + L_COLS + cw * 256 + (8 * (lane & 7) + 2 * (g & 1) + 4 * (g >> 1)) * 4) = (f32x2){a8[0], a8[1]};
      SCHED(); asm volatile("s_waitcnt lgkmcnt(0)" ::: "memory");
      if (last_of(lds, C_CS, 4, lane)) {
        const float* cp = (const float*)(lds + L_COLS);
        const float tot = (cp[lane] + cp[64 + lane]) + (cp[128 + lane] + cp[192 + lane]);
        __hip_atomic_store((float*)(ws + WS_CS) + 64 * auxidx + lane, tot, RLX_AGENT);
        asm volatile("s_waitcnt vmcnt(0)" ::: "memory");
        if (lane < 16) __hip_atomic_store((u64*)(ws + WS_FLAG + (size_t)cblk * 1024 + FL_ST) + 2 + (auxidx & 1), aC + 1, RLX_AGENT);
      }
    }
  }
  asm volatile("s_waitcnt vmcnt(2)" : "+v"(raw0), "+v"(raw1) :: "memory");
  float isc;
  unsigned T[TB_N];
  kgen_tables(T, isc, raw0, raw1, (d & 1) ? bfhi(sdt) : bflo(sdt), lds, cw, lane);
#pragma unroll
  for (int i = 0; i < TB_N; ++i) asm volatile("" : "+v"(T[i]));
  asm volatile("s_waitcnt vmcnt(0)" : "+v"(aS), "+v"(aH) :: "memory");
  const u64 aOwn = rdlane64(aS, 32);
  if (cw == 1 && lane < 16) *(u64*)(lds + L_ACON + lane * 8) = aH;
  if (cw == 3) { if (lane == 0) *(u64*)(lds + L_KSC + 16) = aOwn; ARRIVE_L(C_EP); }
  __builtin_amdgcn_s_setprio(1);
  const u64* lnfl = (const u64*)(flg + FL_LN) + (lane & 31);
  const char* up = ws + WS_UR + (size_t)d * 4096 + hf * 2048;
  u64 fv;
  if (aux) {
    await_<2>(lds, C_X2, 8);
  } else {
    const u64* stfl = (const u64*)(flg + FL_ST) + (lane & 3);
    const char* stp = cw == 1 ? ws + WS_CS + fs * 512 : ws + WS_STAT + (size_t)panel * 1024;
    const unsigned svo = cw == 1 ? (lane & 31) * 16 : lane * 16;
    u32x4 stv;
#define XBAR() do { SCHED(); asm volatile("s_waitcnt lgkmcnt(0)\n\ts_barrier" ::: "memory"); SCHED(); } while (0)
    if (cw < 2) {
      bool ok = false, got = false;
      XBAR(); XBAR(); XBAR(); XBAR(); XBAR(); XBAR();
      ld_u64(fv, stfl); gload16(stv, svo, stp);
#define XPOLL() do { XBAR(); asm volatile("s_waitcnt vmcnt(0)" : "+v"(fv), "+v"(stv) :: "memory"); \
        if (ok && !got) { *(u32x4*)(lds + (cw == 1 ? L_COLS : L_STAT) + svo) = stv; got = true; } \
        ok = __all(fv == aOwn + 1); SCHED(); ld_u64(fv, stfl); gload16(stv, svo, stp); } while (0)
      XPOLL(); XPOLL();
      asm volatile("s_waitcnt vmcnt(0)" : "+v"(fv), "+v"(stv) :: "memory");
      if (!got) {
        if (!ok) {
          poll_flags(stfl, aOwn + 1);
          asm volatile("global_load_dwordx4 %0, %1, %2 sc1\n\ts_waitcnt vmcnt(0)" : "=&v"(stv) : "v"(svo), "s"(stp) : "memory");
        }
        *(u32x4*)(lds + (cw == 1 ? L_COLS : L_STAT) + svo) = stv;
      }
    } else {
      XBAR(); XBAR(); XBAR(); XBAR(); XBAR(); XBAR(); XBAR(); XBAR();
    }
    XBAR();
    XBAR();
  }
  __builtin_amdgcn_s_setprio(3);
  K2_SETUP(cw);
  K2_DMA(0); K2_DMA(1);
  char* wl = lds + L_W + ch * WSZ;
  {
    char* tb = wl + hf * 9216;
#pragma unroll
    for (int i = 0; i < TB_N; ++i) *(unsigned*)(tb + i * TB_RS + lane * 4) = T[i];
    const char* tr = tb + g * 16;
    const int rXa = (i16 >> 3) * TB_RS, rXe = (17 + (i16 & 7)) * TB_RS;
    const int rYa = (i16 < 8 ? 2 + (i16 >> 2) : 4 + ((i16 - 8) >> 1)) * TB_RS, rYe = (17 + (i16 < 8 ? 2 * (i16 & 3) : 4 * (i16 & 1))) * TB_RS;
    const int rZa = (i16 < 8 ? 8 + i16 : 16) * TB_RS;
    const int rB1 = (25 + (i16 >> 2)) * TB_RS, rB0 = (29 + (i16 & 3)) * TB_RS;
    f32x4 kX = {0.f, 0.f, 0.f, 0.f}, kY = kX, kZ = kX;
#pragma unroll
    for (int sb = 0; sb < 4; sb += 2) {
      u32x4 t_[2][7];
#pragma unroll
      for (int s = 0; s < 2; ++s) {
        const char* tq = tr + (sb + s) * 64;
        t_[s][0] = *(const u32x4*)(tq + rB1); t_[s][1] = *(const u32x4*)(tq + rB0); t_[s][2] = *(const u32x4*)(tq + rXa); t_[s][3] = *(const u32x4*)(tq + rXe);
        t_[s][4] = *(const u32x4*)(tq + rYa); t_[s][5] = *(const u32x4*)(tq + rYe); t_[s][6] = *(const u32x4*)(tq + rZa);
      }
      SCHED();
#pragma unroll
      for (int s = 0; s < 2; ++s) {
        const f16x8 hb = __builtin_bit_cast(f16x8, cmulh4(t_[s][0], t_[s][1]));
        kX = __builtin_amdgcn_mfma_f32_16x16x32_f16(__builtin_bit_cast(f16x8, cmulh4(t_[s][2], t_[s][3])), hb, kX, 0, 0, 0);
        kY = __builtin_amdgcn_mfma_f32_16x16x32_f16(__builtin_bit_cast(f16x8, cmulh4(t_[s][4], t_[s][5])), hb, kY, 0, 0, 0);
        kZ = __builtin_amdgcn_mfma_f32_16x16x32_f16(__builtin_bit_cast(f16x8, t_[s][6]), hb, kZ, 0, 0, 0);
      }
      SCHED();
    }
    kgen_store(kX, kY, kZ, isc, lds, cw, lane);
  }
  ARRIVE_L(C_K0 + ch);
  ld_u64(fv, lnfl);
  await_<0>(lds, C_K0 + ch, 2);
  const f32x2 ks = *(const f32x2*)(lds + L_KSC + ch * 8);
  int ex = (int)((__float_as_uint(ks[0] + ks[1]) >> 23) & 255);
  ex = ex < 20 ? 20 : (ex > 230 ? 230 : ex);
  const float scale = __uint_as_float((unsigned)(267 - ex) << 23);
  const float iscale = __uint_as_float((unsigned)(ex - 13) << 23);
  char* kp = lds + L_KP + ch * KPSZ;
  {
    const float* k0 = (const float*)(lds + L_KAP + (2 * ch) * KAPSZ);
#define KSUM2(T, i) ((*(const T*)(k0 + (i)) + *(const T*)(k0 + 644 + (i))) * scale)
    const int A = 64 * hf + lane, A2 = 128 + A;
    const float e0 = KSUM2(float, 512 + A), e1 = KSUM2(float, 513 + A);
    u32x4 o;
    if (hf == 0) {
      const int i0 = lane < 32 ? 8 * A : 256 + 4 * (A - 32);
      const f32x4 a = KSUM2(f32x4, i0), b = KSUM2(f32x4, i0 + 4);
      const bool lo = lane < 32;
      o = (u32x4){pkh(a[0], lo ? a[1] : a[0]), pkh(lo ? a[2] : a[1], lo ? a[3] : a[2]), pkh(lo ? b[0] : a[2], lo ? b[1] : a[2]), pkh(lo ? b[2] : a[3], lo ? b[3] : b[0])};
      if (lane < 2) *(u32x4*)(kp + lane * 16) = (u32x4){0u, 0u, 0u, 0u};
    } else {
      const int i0 = 384 + 2 * lane;
      const f32x2 a = KSUM2(f32x2, i0), b = KSUM2(f32x2, i0 + 2);
      o = (u32x4){pkh(a[0], a[0]), pkh(a[0], a[1]), pkh(a[1], a[1]), pkh(b[0], b[0])};
    }
    *(u32x4*)(kp + 32 + A * 16) = o;
    const unsigned p00 = pkh(e0, e0);
    *(u32x4*)(kp + 32 + A2 * 16) = (u32x4){p00, p00, pkh((A2 & 1) ? e1 : e0, e1), pkh(e1, e1)};
  }
  ARRIVE_L(C_K1);
  asm volatile("s_waitcnt vmcnt(0)" : "+v"(fv) :: "memory");
  arrive(lds, C_W2, lane);
  const bool urok = __all(fv == aOwn + 1);
  u32x4 ua_, ub_, q0, q1, q2, q3;
  const char* qp = ws + WS_ST2 + hf * 4096;
#define U_LOADS() { gload16(ua_, lane * 16, up); gload16(ub_, lane * 16, up + 1024); \
    gload16(q0, lane * 32, qp); gload16(q1, lane * 32, qp + 16); gload16(q2, lane * 32, qp + 2048); gload16(q3, lane * 32, qp + 2064); }
#define U_WAIT() asm volatile("s_waitcnt vmcnt(0)" : "+v"(ua_), "+v"(ub_), "+v"(q0), "+v"(q1), "+v"(q2), "+v"(q3) :: "memory")
  U_LOADS();
  await_<0>(lds, C_K1, 4);
#pragma unroll
  for (int rnd = 0; rnd < 3; ++rnd) {
    const int a = rnd < 2 ? 128 * rnd + 64 * hf + lane : 256;
    if (rnd == 2 && !(hf == 0 && lane == 0)) break;
    const u32x4 c0 = *(const u32x4*)(kp + a * 16), c1 = *(const u32x4*)(kp + a * 16 + 16);
    const unsigned e[8] = {c0[0], c0[1], c0[2], c0[3], c1[0], c1[1], c1[2], c1[3]};
    unsigned o[7];
#pragma unroll
    for (int i = 0; i < 7; ++i) o[i] = __builtin_amdgcn_alignbit(e[i + 1], e[i], 16);
    const int m = (a & 1) | (((a >> 2) & 1) << 1) | (((a >> 1) & 1) << 2);
    char* base = wl + a * 128;
#pragma unroll
    for (int c = 0; c < 8; ++c) {
      u32x4 wv;
      if (c & 1) { const int s = (c + 1) >> 1; wv = (u32x4){e[s], e[s + 1], e[s + 2], e[s + 3]}; }
      else { const int s = c >> 1; wv = (u32x4){o[s], o[s + 1], o[s + 2], o[s + 3]}; }
      *(u32x4*)(base + ((c ^ m) << 4)) = wv;
    }
  }
  SCHED();
  U_WAIT();
  if (!urok) { poll_flags(lnfl, aOwn + 1); U_LOADS(); U_WAIT(); }
  char* ul = lds + L_U + ch * USZ;
  {
#define UN2(xw, s0, s1) ({ const unsigned a_ = pkbf(bflo(xw) - bflo(s0), bfhi(xw) - bflo(s1)); \
      const unsigned b_ = pkbf(bflo(a_) * __builtin_amdgcn_rcpf(bfhi(s0)), bfhi(a_) * __builtin_amdgcn_rcpf(bfhi(s1))); pkh(bflo(b_), bfhi(b_)); })
    const u32x4 o0 = {UN2(ua_[0], q0[0], q0[1]), UN2(ua_[1], q0[2], q0[3]), UN2(ua_[2], q1[0], q1[1]), UN2(ua_[3], q1[2], q1[3])};
    const u32x4 o1 = {UN2(ub_[0], q2[0], q2[1]), UN2(ub_[1], q2[2], q2[3]), UN2(ub_[2], q3[0], q3[1]), UN2(ub_[3], q3[2], q3[3])};
    *(u32x4*)(ul + hf * 2048 + lane * 16) = o0;
    *(u32x4*)(ul + hf * 2048 + 1024 + lane * 16) = o1;
  }
  if (hf == 0 && lane < 32) *(u32x4*)(ul + 4096 + lane * 16) = (u32x4){0u, 0u, 0u, 0u};
  ARRIVE_L(C_K2 + ch);
  await_<2>(lds, C_PUB, 8);
  await_<0>(lds, C_K2 + ch, 2);
  __builtin_amdgcn_s_setprio(3);
  f32x4 acc[8];
#pragma unroll
  for (int i = 0; i < 8; ++i) acc[i] = (f32x4){0.f, 0.f, 0.f, 0.f};
  {
    const int c = i16 + 8 * g;
    int fo[2];
#pragma unroll
    for (int par = 0; par < 2; ++par) {
      const int w = 32 * par + c;
      const int m = ((w >> 3) & 1) | (((w >> 5) & 1) << 1) | (((w >> 4) & 1) << 2);
      fo[par] = (w ^ m) << 4;
    }
    const char* fbase = wl + hf * (128 * 16);
    const char* gbase = ul + (2032 - 16 * i16 + 8 * g + 128 * hf) * 2 - 7 * 512;
#define CF(s, e) (*(const f16x8*)(fbase + fo[(s) & 1] + ((s) >> 1) * 1024 + (e) * 4096))
#define CG(s, mu) (*(const f16x8*)(gbase + (s) * 64 + (7 - (mu)) * 512))
    f16x8 F[2][8], G[2];
#pragma unroll
    for (int e = 0; e < 8; ++e) F[0][e] = CF(0, e);
    G[0] = CG(0, 0);
#pragma unroll
    for (int s = 0; s < 4; ++s) {
#pragma unroll
      for (int mu = 0; mu < 8; ++mu) {
        const int it = s * 8 + mu;
        SCHED();
        if (it + 1 < 32) G[(it + 1) & 1] = CG((it + 1) >> 3, (it + 1) & 7);
        if (s < 3) F[(s + 1) & 1][mu] = CF(s + 1, mu);
        SCHED();
#pragma unroll
        for (int e = 0; e + mu < 8; ++e) acc[e + mu] = __builtin_amdgcn_mfma_f32_16x16x32_f16(F[s & 1][e], G[it & 1], acc[e + mu], 0, 0, 0);
      }
    }
  }
  ARRIVE_L(C_K1 + 1 + ch); await_<0>(lds, C_K1 + 1 + ch, 2);
  {
    char* dst = wl + hf * 4096 + lane * 16;
#pragma unroll
    for (int i = 0; i < 4; ++i) *(f32x4*)(dst + i * 1024) = sel(hf, acc[i], acc[4 + i]);
  }
  ARRIVE_L(C_K3 + ch); await_<0>(lds, C_K3 + ch, 2);
  {
    const float dskip = (d & 1) ? bfhi(sds) : bflo(sds);
    const char* src = wl + (hf ^ 1) * 4096 + lane * 16;
    char* sdst = ws + WS_S + (size_t)d * 4096;
#pragma unroll
    for (int i = 0; i < 4; ++i) {
      const f32x4 yv = (sel(hf, acc[4 + i], acc[i]) + *(const f32x4*)(src + i * 1024)) * iscale;
      const int t = 256 * (4 * hf + i) + 16 * i16 + 4 * g;
      const u32x2 uu = *(const u32x2*)(ul + (2044 - t) * 2);
      const unsigned uu0 = uu[0], uu1 = uu[1];
      const f16x2 ua = __builtin_bit_cast(f16x2, uu0), ub = __builtin_bit_cast(f16x2, uu1);
      const unsigned y01 = pkbf(yv[0], yv[1]), y23 = pkbf(yv[2], yv[3]);
      const unsigned k01 = pkbf((float)ub[1] * dskip, (float)ub[0] * dskip), k23 = pkbf((float)ua[1] * dskip, (float)ua[0] * dskip);
      const u64 pk = (u64)pkbf(bflo(y01) + bflo(k01), bfhi(y01) + bfhi(k01)) | ((u64)pkbf(bflo(y23) + bflo(k23), bfhi(y23) + bfhi(k23)) << 32);
      __hip_atomic_store((u64*)(sdst + t * 2), pk, RLX_AGENT);
    }
  }
  asm volatile("s_waitcnt vmcnt(0) lgkmcnt(0)" ::: "memory");
  if (last_of(lds, C_SP, 4, lane) && lane < 32) __hip_atomic_store((u64*)(ws + WS_FLAG + (size_t)blk_of(lane, cb) * 1024 + FL_S) + mb, aS + 1, RLX_AGENT);
}

#define FIN_LOADS() { \
  asm volatile("global_load_dwordx4 %0, %1, off" : "=v"(xv) : "v"(x + (size_t)(64 * mb + (tid >> 3)) * D_ + 64 * cb + 8 * (tid & 7)) : "memory"); \
  asm volatile("global_load_dwordx4 %0, %1, off" : "=v"(bv) : "v"(b2 + 64 * cb + 8 * (tid & 7)) : "memory"); }
DI void final_stage(u32x4& xv, u32x4& bv, u16* __restrict__ out, char* lds, int mb, int cb, int tid) {
  await_<0>(lds, C_R1, 4);
  await_<0>(lds, C_ST, 4);
  const int t = tid >> 3, dq = tid & 7;
  const char* src = lds + L_RED + t * RED_RS + dq * 32;
  f32x4 ra[4], rb[4];
#pragma unroll
  for (int k = 0; k < 4; ++k) { ra[k] = *(const f32x4*)(src + k * 64 * RED_RS); rb[k] = *(const f32x4*)(src + k * 64 * RED_RS + 16); }
  const u32x4 sv = *(const u32x4*)(lds + L_ST + t * ST_RS + dq * 16);
  SCHED();
  const f32x4 a = (ra[0] + ra[1]) + (ra[2] + ra[3]), b = (rb[0] + rb[1]) + (rb[2] + rb[3]);
  asm volatile("s_waitcnt vmcnt(0)" : "+v"(xv), "+v"(bv) :: "memory");
  u32x4 o;
#pragma unroll
  for (int c = 0; c < 4; ++c) {
    const float v0 = (c < 2 ? a[2 * c] : b[2 * c - 4]) + bflo(bv[c]), v1 = (c < 2 ? a[2 * c + 1] : b[2 * c - 3]) + bfhi(bv[c]);
    const float g0 = v0 * __builtin_amdgcn_rcpf(1.f + __builtin_amdgcn_exp2f(-1.4426950408889634f * v0));
    const float g1 = v1 * __builtin_amdgcn_rcpf(1.f + __builtin_amdgcn_exp2f(-1.4426950408889634f * v1));
    const unsigned pv = pkbf(bflo(sv[c]) * g0, bfhi(sv[c]) * g1);
    o[c] = pkbf(bflo(xv[c]) + bflo(pv), bfhi(xv[c]) + bfhi(pv));
  }
  *(u32x4*)(out + (size_t)(64 * mb + t) * D_ + 64 * cb + 8 * dq) = o;
}
DI void p2_gemm(const u16* __restrict__ x, const u16* __restrict__ w2, u64 aOwn, const u16* __restrict__ b2, u16* __restrict__ out, char* __restrict__ ws,
                char* lds, int mb, int cb, int kq, int lane) {
  const int r = lane & 31, h = lane >> 5;
  unsigned* cnt = (unsigned*)(lds + L_CNT);
  const int tid = kq * 64 + lane;
  K2_SETUP(kq);
  char* ring = lds + L_RING2 + kq * (K2_R * 4096);
  const char* flg = ws + WS_FLAG + (size_t)blockIdx.x * 1024;
  const char* hbase = ws + WS_H + (size_t)(2 * mb) * HMT + 28 * kq * 1024;
  const unsigned hoff0 = lane * 16, hoff1 = lane * 16 + HMT;
  const int q = (lane & 15) >> 2;
  const int toff0 = (4 * h + q) * 128 + ((q >> 1) << 6) + ((lane >> 4) & 1) * 32 + 8 * (lane & 3), toff1 = toff0 ^ 64;
  u32x4 hb[3][4];
  s16x8 wf[2][4];
  f32x16 acc[2][2];
#pragma unroll
  for (int i = 0; i < 16; ++i) { acc[0][0][i] = 0.f; acc[0][1][i] = 0.f; acc[1][0][i] = 0.f; acc[1][1][i] = 0.f; }
  await_<1>(lds, C_PUB, 8);
  K2_DMA(2); K2_DMA(3);
  if (kq == 0) {
    const u64* fp = (const u64*)(flg + FL_H) + (lane < 14 ? lane : 0);
    for (unsigned spin = 0; spin < (1u << 20); ++spin) {
      u64 v;
      asm volatile("global_load_dwordx2 %0, %1, off sc1\n\ts_waitcnt vmcnt(0)" : "=&v"(v) : "v"(fp) : "memory");
      if (__all(v == aOwn + 1)) break;
      __builtin_amdgcn_s_sleep(2);
    }
    lds_set(cnt + C_H, lane);
  } else {
    lds_wait(cnt + C_H);
  }
#define K2_HL(S, s2) { const char* hs_ = hbase + K2S(S) * 2048 + (s2) * 1024; gload16(hb[(S) % 3][2 * (s2)], hoff0, hs_); gload16(hb[(S) % 3][2 * (s2) + 1], hoff1, hs_); SCHED(); }
#define K2_HLD(S) { K2_HL(S, 0); K2_HL(S, 1); }
#define K2_TR(S, i) { const char* sl_ = ring + ((S) % K2_R) * 4096 + ((i) >> 1) * 2048 + (((i) & 1) ? toff1 : toff0); wf[(S) & 1][i] = tr_frag(sl_, sl_ + 1024); SCHED(); }
#define K2_D1(S, pp) { dma16s(wvoff, wsrc + (size_t)(K2S(S) * 32 + (pp) * 8) * D_, ringa + ((S) % K2_R) * 4096 + (pp) * 1024); SCHED(); }
#define K2_M(S, s2, mi, di) { acc[mi][di] = __builtin_amdgcn_mfma_f32_32x32x16_bf16(wf[(S) & 1][2 * (s2) + (di)], __builtin_bit_cast(s16x8, hb[(S) % 3][2 * (s2) + (mi)]), acc[mi][di], 0, 0, 0); SCHED(); }
#define K2_STEP(S, W, TRN, HN, DN) { \
    asm volatile("s_waitcnt vmcnt(" #W ")" : "+v"(hb[(S) % 3][0]), "+v"(hb[(S) % 3][1]), "+v"(hb[(S) % 3][2]), "+v"(hb[(S) % 3][3]) :: "memory"); SCHED(); \
    K2_M(S,0,0,0); if (TRN) K2_TR((S)+1,0); K2_M(S,0,1,0); if (TRN) K2_TR((S)+1,1); K2_M(S,0,0,1); if (TRN) K2_TR((S)+1,2); K2_M(S,0,1,1); if (TRN) K2_TR((S)+1,3); \
    K2_M(S,1,0,0); if (DN) { K2_D1((S)+4,0); K2_D1((S)+4,1); } K2_M(S,1,1,0); if (DN) { K2_D1((S)+4,2); K2_D1((S)+4,3); } K2_M(S,1,0,1); if (HN) K2_HL((S)+3,0); K2_M(S,1,1,1); if (HN) K2_HL((S)+3,1); }
  K2_HLD(0); K2_HLD(1); K2_HLD(2);
  await_<0>(lds, C_W2, 4);
  K2_TR(0,0); K2_TR(0,1); K2_TR(0,2); K2_TR(0,3);
  K2_STEP(0, 8, 1, 1, 1);
  K2_STEP(1, 12, 1, 1, 1);
  K2_STEP(2, 16, 1, 1, 1);
  K2_STEP(3, 16, 1, 1, 1);
  K2_STEP(4, 16, 1, 1, 1);
  K2_STEP(5, 16, 1, 1, 1);
  K2_STEP(6, 16, 1, 1, 1);
  K2_STEP(7, 16, 1, 1, 1);
  K2_STEP(8, 16, 1, 1, 1);
  if (kq == 0) lds_set(cnt + C_G8, lane);
  K2_STEP(9, 16, 1, 1, 1);
  K2_STEP(10, 16, 1, 1, 0);
  K2_STEP(11, 12, 1, 0, 0);
  K2_STEP(12, 4, 1, 0, 0);
  K2_STEP(13, 0, 0, 0, 0);
  await_<0>(lds, C_SP, 4);
  {
    char* dst = lds + L_RED + (kq * 64 + r) * RED_RS + h * 16;
#pragma unroll
    for (int mi = 0; mi < 2; ++mi)
#pragma unroll
      for (int di = 0; di < 2; ++di)
#pragma unroll
        for (int rq = 0; rq < 4; ++rq)
          *(f32x4*)(dst + mi * 32 * RED_RS + di * 128 + rq * 32) = (f32x4){acc[mi][di][4 * rq], acc[mi][di][4 * rq + 1], acc[mi][di][4 * rq + 2], acc[mi][di][4 * rq + 3]};
  }
  ARRIVE_L(C_R1);
  await_<0>(lds, C_ST, 4);
  u32x4 xv = *(const u32x4*)(lds + L_XT + tid * 16), bv = *(const u32x4*)(lds + L_XT + 4096 + (tid & 7) * 16);
  final_stage(xv, bv, out, lds, mb, cb, tid);
  if (tid == 0) __hip_atomic_store((u64*)(ws + WS_EP) + blockIdx.x, aOwn + 1, RLX_AGENT);
}

DI void p2_helper(const u16* __restrict__ x, const u16* __restrict__ w2, u64 aOwn, const u16* __restrict__ b2, u16* __restrict__ out, char* __restrict__ ws,
                  char* lds, int mb, int cb, int gw, int lane) {
  unsigned* cnt = (unsigned*)(lds + L_CNT);
  const int tid = gw * 64 + lane, hw = gw - 4;
  u32x4 xv, bv, x2;
  await_<2>(lds, C_PUB, 8);
  FIN_LOADS();
  asm volatile("global_load_dwordx4 %0, %3, off\n\ts_waitcnt vmcnt(0)" : "=&v"(x2), "+v"(xv), "+v"(bv) : "v"(x + (size_t)(64 * mb + ((tid - 256) >> 3)) * D_ + 64 * cb + 8 * (tid & 7)) : "memory");
  *(u32x4*)(lds + L_XT + (tid - 256) * 16) = x2;
  if (tid < 264) *(u32x4*)(lds + L_XT + 4096 + (tid & 7) * 16) = bv;
  if (hw == 0) {
    await_<8>(lds, C_SP, 4);
    while (__hip_atomic_load(cnt + C_G8, RLX_WG) == 0u) __builtin_amdgcn_s_sleep(8);
    const u64* fp = (const u64*)(ws + WS_FLAG + (size_t)blockIdx.x * 1024 + FL_S) + (lane & 31);
    for (unsigned spin = 0; spin < (1u << 18); ++spin) {
      const u64 v = __hip_atomic_load(fp, RLX_AGENT);
      if (__all(v == aOwn + 1)) break;
      __builtin_amdgcn_s_sleep(8);
    }
    asm volatile("" ::: "memory");
    lds_set(cnt + C_S, lane);
  } else {
    while (__hip_atomic_load(cnt + C_S, RLX_WG) == 0u) __builtin_amdgcn_s_sleep(4);
    asm volatile("" ::: "memory");
  }
  u32x4 sa, sb;
  const char* sp = ws + WS_S + (size_t)(64 * cb + 16 * hw) * 4096 + 64 * mb * 2;
  gload16(sa, (lane >> 3) * 4096 + (lane & 7) * 16, sp);
  gload16(sb, (lane >> 3) * 4096 + (lane & 7) * 16, sp + 8 * 4096);
  asm volatile("s_waitcnt vmcnt(0)" : "+v"(sa), "+v"(sb) :: "memory");
  char* dst = lds + L_ST + (8 * (lane & 7)) * ST_RS + (16 * hw + (lane >> 3)) * 2;
#pragma unroll
  for (int c = 0; c < 4; ++c) {
    *(u16*)(dst + (2 * c) * ST_RS) = (u16)sa[c]; *(u16*)(dst + (2 * c + 1) * ST_RS) = (u16)(sa[c] >> 16);
    *(u16*)(dst + (2 * c) * ST_RS + 16) = (u16)sb[c]; *(u16*)(dst + (2 * c + 1) * ST_RS + 16) = (u16)(sb[c] >> 16);
  }
  ARRIVE_L(C_ST);
  while (__hip_atomic_load(cnt + C_R1, RLX_WG) < 4u) __builtin_amdgcn_s_sleep(2);
  final_stage(xv, bv, out, lds, mb, cb, tid);
}

__global__ __launch_bounds__(768, 3) void fused_kernel(const u16* __restrict__ x, const u16* __restrict__ lar, const u16* __restrict__ aim,
                                                       const u16* __restrict__ cre, const u16* __restrict__ cim, const u16* __restrict__ ldt,
                                                       const u16* __restrict__ dsk, const u16* __restrict__ w1, const u16* __restrict__ b1,
                                                       const u16* __restrict__ w2, const u16* __restrict__ b2, u16* __restrict__ out,
                                                       char* __restrict__ ws) {
  extern __shared__ __attribute__((aligned(16))) char lds[];
  const int lane = threadIdx.x & 63;
  const int wid = __builtin_amdgcn_readfirstlane(threadIdx.x >> 6);
  const int b = blockIdx.x, xcd = b & 7, j = b >> 3;
  const int mb = 4 * xcd + (j >> 3), cb = j & 7;
  const int jj = j < 28 ? j : 0;
  const int panel = 4 * (xcd >> 1) + jj / 7, fs = 7 * (xcd & 1) + jj % 7;
  if (wid < 4) {
    const int de = 64 * cb + 2 * mb;
    const unsigned sdt = *(const __attribute__((address_space(4))) unsigned*)(ldt + de);
    const unsigned sds = *(const __attribute__((address_space(4))) unsigned*)(dsk + de);
    x_wave(lar, aim, cre, cim, sdt, sds, w2, w1, b1, j >= 28 ? xcd * 4 + (j - 28) : -1, panel, fs, ws, lds, mb, cb, wid, lane);
    return;
  }
  const int gw = wid - 4;
  __builtin_amdgcn_s_setprio(2);
  if (j < 28) p1_gemm(x, w1, ws, lds, panel, fs, gw, lane);
  else p1_aux(x, ws, lds, xcd * 4 + (j - 28), gw, lane);
  __builtin_amdgcn_s_setprio(0);
  await_<0>(lds, C_EP, 1);
  const u64 aOwn = *(const u64*)(lds + L_KSC + 16);
  if (gw < 4) p2_gemm(x, w2, aOwn, b2, out, ws, lds, mb, cb, gw, lane);
  else p2_helper(x, w2, aOwn, b2, out, ws, lds, mb, cb, gw, lane);
}

extern "C" void kernel_launch(void* const* d_in, const int* in_sizes, int n_in, void* d_out, int out_size, void* d_ws, size_t ws_size,
                              hipStream_t stream) {
  const u16* x = (const u16*)d_in[0];
  const u16* lar = (const u16*)d_in[1];
  const u16* aim = (const u16*)d_in[2];
  const u16* cre = (const u16*)d_in[3];
  const u16* cim = (const u16*)d_in[4];
  const u16* ldt = (const u16*)d_in[5];
  const u16* dsk = (const u16*)d_in[6];
  const u16* w1 = (const u16*)d_in[8];
  const u16* b1 = (const u16*)d_in[9];
  const u16* w2 = (const u16*)d_in[10];
  const u16* b2 = (const u16*)d_in[11];
  (void)hipFuncSetAttribute((const void*)fused_kernel, hipFuncAttributeMaxDynamicSharedMemorySize, LDS_TOTAL);
  fused_kernel<<<256, 768, LDS_TOTAL, stream>>>(x, lar, aim, cre, cim, ldt, dsk, w1, b1, w2, b2, (u16*)d_out, (char*)d_ws);
}
```
